```python
import torch

def _softmax(x, axis=-1):
    return torch.softmax(x, dim=axis)

def _gelu(x):
    return torch.nn.functional.gelu(x, approximate='tanh')

def _silu(x):
    return torch.nn.functional.silu(x)

def _sigmoid(x):
    return torch.sigmoid(x)

def _layernorm(x, axis=-1, eps=1e-05):
    m = x.mean(dim=axis, keepdim=True)
    v = x.var(dim=axis, keepdim=True, unbiased=False)
    return (x - m) / torch.sqrt(v + eps)

def _scatter_add(tgt, idx, src, axis=0):
    return tgt.clone().index_add_(axis, idx, src)

def _segment_sum(src, seg, n):
    out = torch.zeros((n,) + src.shape[1:], dtype=src.dtype)
    return out.index_add_(0, seg, src)

def _pad(x, pw):
    flat = tuple((v for pair in reversed(pw) for v in pair))
    return torch.nn.functional.pad(x, flat)
kernel_name = 'standalone__window_attn_davit__vision'

def setup_inputs():

    _w = lambda a: (lambda t: t.to(torch.bfloat16) if t.is_floating_point() else t)(torch.as_tensor(a))
    HW = 224
    C0 = 192
    ws = 7
    nh = 4
    H = W = max(HW // ws * ws, ws)
    C = max(C0 // nh * nh, nh)
    k = 3
    return {'x': _w((lambda *s: torch.randn(*s, dtype=torch.bfloat16) * 1.6)(1, H, W, C)), 'dw_pe': _w((lambda *s: torch.randn(*s, dtype=torch.bfloat16) * 1.6)(k, k, C) * 0.1), 'w_qkv': _w((lambda *s: torch.randn(*s, dtype=torch.bfloat16) * 1.6)(C, 3 * C) * C ** (-0.5)), 'w_o': _w((lambda *s: torch.randn(*s, dtype=torch.bfloat16) * 0.4)(C, C) * C ** (-0.5))}

def reference(x, dw_pe, w_qkv, w_o):
    B, H, W, C = x.shape
    ws = 7
    nh = 4
    dh = C // nh
    kk = dw_pe.shape[0]
    pad = kk // 2
    xp = _pad(x, ((0, 0), (pad, pad), (pad, pad), (0, 0)))
    pe = torch.zeros_like(x)
    for i in range(kk):
        for j in range(kk):
            pe = pe + xp[:, i:i + H, j:j + W, :] * dw_pe[i, j][None, None, None, :]
    x0 = x + pe
    h = _layernorm(x0, axis=-1)
    hw = h.reshape(B, H // ws, ws, W // ws, ws, C)
    hw = (lambda x, *d: x.permute(*d))(hw, 0, 1, 3, 2, 4, 5)
    hw = hw.reshape(-1, ws * ws, C)
    qkv = (hw @ w_qkv).reshape(-1, ws * ws, 3, nh, dh)
    q = qkv[:, :, 0].clone()
    k = qkv[:, :, 1].clone()
    v = qkv[:, :, 2].clone()
    scale = dh ** (-0.5)
    attn = torch.einsum('wnhd,wmhd->whnm', q, k) * scale
    attn = _softmax(attn, axis=-1)
    out = torch.einsum('whnm,wmhd->wnhd', attn, v)
    out = out.reshape(-1, ws * ws, C) @ w_o
    out = out.reshape(B, H // ws, W // ws, ws, ws, C)
    out = (lambda x, *d: x.permute(*d))(out, 0, 1, 3, 2, 4, 5)
    out = out.reshape(B, H, W, C)
    return x0 + out
```

```cpp
#include <hip/hip_runtime.h>
#include <hip/hip_bf16.h>
#include <stdint.h>

static __device__ __forceinline__ float bf2f(uint16_t v){ return __uint_as_float(((uint32_t)v)<<16); }
static __device__ __forceinline__ uint16_t f2bf(float f){ __hip_bfloat16 b = __float2bfloat16(f); return *(uint16_t*)&b; }

__global__ __launch_bounds__(256) void naive_kernel(const uint16_t* __restrict__ x, const uint16_t* __restrict__ dw,
    const uint16_t* __restrict__ wqkv, const uint16_t* __restrict__ wo, uint16_t* __restrict__ out) {
  __shared__ float x0[49*192];
  __shared__ uint16_t h[49*192];
  __shared__ uint16_t qkv[49*576];
  __shared__ float S[49*49];
  int w = blockIdx.x; int wy = w/32, wx = w%32;
  int tid = threadIdx.x;
  for (int i = tid; i < 49*192; i += 256) {
    int t = i/192, c = i%192; int y = wy*7 + t/7, xx = wx*7 + t%7;
    float acc = 0;
    for (int a=0;a<3;a++) for (int b=0;b<3;b++){ int yy=y+a-1, xb=xx+b-1; if (yy>=0&&yy<224&&xb>=0&&xb<224) acc += bf2f(x[(yy*224+xb)*192+c])*bf2f(dw[(a*3+b)*192+c]); }
    x0[i] = bf2f(f2bf(acc + bf2f(x[(y*224+xx)*192+c])));
  }
  __syncthreads();
  if (tid < 49) {
    float s=0; for (int c=0;c<192;c++) s+=x0[tid*192+c]; s/=192.f;
    float v=0; for (int c=0;c<192;c++){ float d=x0[tid*192+c]-s; v+=d*d;} v/=192.f;
    float r = rsqrtf(v+1e-5f);
    for (int c=0;c<192;c++) h[tid*192+c]=f2bf((x0[tid*192+c]-s)*r);
  }
  __syncthreads();
  for (int i = tid; i < 49*576; i += 256) {
    int t=i/576, n=i%576; float acc=0;
    for (int c=0;c<192;c++) acc += bf2f(h[t*192+c])*bf2f(wqkv[c*576+n]);
    qkv[i]=f2bf(acc);
  }
  __syncthreads();
  for (int hd=0; hd<4; hd++) {
    for (int i=tid;i<49*49;i+=256){ int n=i/49,m=i%49; float acc=0;
      for (int d=0;d<48;d++) acc += bf2f(qkv[n*576+hd*48+d])*bf2f(qkv[m*576+192+hd*48+d]);
      S[i]=acc*0.14433756729740643f; }
    __syncthreads();
    if (tid<49){ float mx=-1e30f; for(int m=0;m<49;m++) mx=fmaxf(mx,S[tid*49+m]); float sum=0; for(int m=0;m<49;m++){ float e=__expf(S[tid*49+m]-mx); S[tid*49+m]=e; sum+=e;} float inv=1.f/sum; for(int m=0;m<49;m++) S[tid*49+m]=bf2f(f2bf(S[tid*49+m]*inv)); }
    __syncthreads();
    for (int i=tid;i<49*48;i+=256){ int n=i/48,d=i%48; float acc=0; for(int m=0;m<49;m++) acc+=S[n*49+m]*bf2f(qkv[m*576+384+hd*48+d]); h[n*192+hd*48+d]=f2bf(acc);}
    __syncthreads();
  }
  for (int i = tid; i < 49*192; i += 256) {
    int t=i/192,n=i%192; float acc=0;
    for (int c=0;c<192;c++) acc += bf2f(h[t*192+c])*bf2f(wo[c*192+n]);
    int y = wy*7 + t/7, xx = wx*7 + t%7;
    out[(y*224+xx)*192+n]=f2bf(acc+x0[i]);
  }
}

extern "C" void kernel_launch(void* const* d_in, const int* in_sizes, int n_in,
                              void* d_out, int out_size, void* d_ws, size_t ws_size,
                              hipStream_t stream) {
  naive_kernel<<<1024,256,0,stream>>>((const uint16_t*)d_in[0],(const uint16_t*)d_in[1],(const uint16_t*)d_in[2],(const uint16_t*)d_in[3],(uint16_t*)d_out);
}
```

```cpp
#include <hip/hip_runtime.h>
#include <stdint.h>

__global__ __launch_bounds__(512, 2) void davit_window_attn_kernel(const void* x, const void* dw, const void* wqkv, const void* wo, void* out) {
  __shared__ __attribute__((aligned(16))) char lds[163840];

  unsigned wave = __builtin_amdgcn_readfirstlane(threadIdx.x >> 6);
  unsigned ldsb = (unsigned)(uintptr_t)(__attribute__((address_space(3))) char*)lds;
  asm volatile(
    "LSTART%=:\n"
    "s_mov_b32 s20, %0\n"
    "s_and_b32 s21, %1, 0xffff\n"
    "s_mov_b32 s22, 0x1260000\n"
    "s_mov_b32 s23, 0x20000\n"
    "s_mov_b32 s36, %2\n"
    "s_and_b32 s37, %3, 0xffff\n"
    "s_mov_b32 s38, 0xd80\n"
    "s_mov_b32 s39, 0x20000\n"
    "s_mov_b32 s24, %4\n"
    "s_and_b32 s25, %5, 0xffff\n"
    "s_mov_b32 s26, 0x36000\n"
    "s_mov_b32 s27, 0x20000\n"
    "s_mov_b32 s28, %6\n"
    "s_and_b32 s29, %7, 0xffff\n"
    "s_mov_b32 s30, 0x12000\n"
    "s_mov_b32 s31, 0x20000\n"
    "s_mov_b32 s40, %8\n"
    "s_and_b32 s41, %9, 0xffff\n"
    "s_mov_b32 s42, 0x1260000\n"
    "s_mov_b32 s43, 0x20000\n"
    "s_and_b32 s44, %11, 3\n"
    "s_and_b32 s45, %10, 7\n"
    "s_lshr_b32 s46, %10, 3\n"
    "s_and_b32 s47, s44, 1\n"
    "s_lshl_b32 s51, s47, 5\n"
    "s_add_u32 s52, s51, 32\n"
    "s_lshl_b32 s48, s47, 6\n"
    "s_sub_u32 s53, 64, s48\n"
    "s_mul_i32 s48, s44, 3\n"
    "s_add_u32 s49, s48, s47\n"
    "s_lshl_b32 s62, s49, 9\n"
    "s_lshr_b32 s49, s44, 1\n"
    "s_mul_i32 s49, s49, 3072\n"
    "s_add_u32 s49, s49, 1024\n"
    "s_lshl_b32 s50, s47, 3\n"
    "s_add_u32 s63, s49, s50\n"
    "s_cmp_lt_u32 %11, 4\n"
    "s_cbranch_scc1 LBSTART%=\n"
    "s_setprio 3\n"
    "v_mbcnt_lo_u32_b32 v210, -1, 0\n"
    "v_mbcnt_hi_u32_b32 v210, -1, v210\n"
    "s_getpc_b64 s[58:59]\n"
    "LPC%=:\n"
    "s_sub_u32 s58, s58, LPC%=-LSTART%=\n"
    "s_subb_u32 s59, s59, 0\n"
    "s_lshl_b32 s47, s46, 2\n"
    "s_add_u32 s47, s47, s44\n"
    "s_lshl_b32 s47, s47, 2\n"
    "v_add_u32 v213, s47, v210\n"
    "v_lshlrev_b32 v213, 7, v213\n"
    "v_min_u32 v213, LEND%=-LSTART%=, v213\n"
    "s_mov_b64 exec, 0xf\n"
    "global_load_dword v254, v213, s[58:59]\n"
    "s_mov_b64 exec, -1\n"
    "v_mul_u32_u24 v213, 43, v210\n"
    "v_lshrrev_b32 v213, 8, v213\n"
    "v_mul_u32_u24 v214, 6, v213\n"
    "v_sub_u32 v214, v210, v214\n"
    "s_lshr_b32 s47, s51, 4\n"
    "v_add_u32 v215, s47, v214\n"
    "v_subrev_u32 v216, 6, v215\n"
    "v_min_u32 v215, v215, v216\n"
    "s_mul_i32 s47, s44, 96\n"
    "v_lshlrev_b32 v215, 4, v215\n"
    "v_add_u32 v215, s47, v215\n"
    "v_mul_u32_u24 v216, 0x180, v213\n"
    "v_add_u32 v216, v216, v215\n"
    "s_lshl_b32 s48, s44, 10\n"
    "s_add_u32 s48, s48, 0x25ec0\n"
    "s_mov_b32 m0, s48\n"
    "s_nop 0\n"
    "buffer_load_dwordx4 v216, s[36:39], 0 offen lds\n"
    "v_lshlrev_b32 v216, 7, v210\n"
    "v_bfrev_b32 v217, 1\n"
    "v_cmp_gt_u32_e64 s[58:59], 18, v210\n"
    "s_nop 1\n"
    "v_cndmask_b32_e64 v218, v217, v216, s[58:59]\n"
    "s_mul_i32 s48, s46, 3\n"
    "s_add_u32 s48, s48, s44\n"
    "s_cmp_eq_u32 s44, 3\n"
    "s_cselect_b32 s48, s46, s48\n"
    "s_cselect_b64 s[40:41], s[28:29], s[24:25]\n"
    "s_cselect_b32 s42, s30, s26\n"
    "s_mul_i32 s48, s48, 2304\n"
    "buffer_load_dword v255, v218, s[40:43], s48 offen offset:0\n"
    "s_setprio 0\n"
    "s_branch LAMAIN%=\n"
    "LBSTART%=:\n"
    "s_setprio 2\n"
    "v_mbcnt_lo_u32_b32 v225, -1, 0\n"
    "v_mbcnt_hi_u32_b32 v225, -1, v225\n"
    "s_lshl_b32 s47, s44, 6\n"
    "v_add_u32 v231, s47, v225\n"
    "v_mul_u32_u24 v232, 0x9d9, v231\n"
    "v_lshrrev_b32 v232, 16, v232\n"
    "v_mul_u32_u24 v233, 26, v232\n"
    "v_sub_u32 v231, v231, v233\n"
    "v_mul_u32_u24 v233, 43, v231\n"
    "v_lshrrev_b32 v233, 8, v233\n"
    "v_mul_u32_u24 v198, 6, v233\n"
    "v_sub_u32 v198, v231, v198\n"
    "v_cmp_gt_u32_e64 s[58:59], 4, v198\n"
    "s_nop 1\n"
    "v_cndmask_b32_e64 v198, -4, 2, s[58:59]\n"
    "v_and_b32 v233, 1, v233\n"
    "v_mul_i32_i24 v198, v198, v233\n"
    "v_add_u32 v198, v198, v231\n"
    "v_lshlrev_b32 v198, 4, v198\n"
    "s_mul_i32 s47, s46, 7\n"
    "s_sub_u32 s47, s47, 1\n"
    "v_add_u32 v232, s47, v232\n"
    "v_mul_i32_i24 v233, 0x180, v232\n"
    "v_add_u32 v198, v198, v233\n"
    "v_cmp_lt_u32_e64 s[58:59], 23, v231\n"
    "s_mov_b32 s47, 223\n"
    "v_cmp_lt_u32_e64 s[60:61], s47, v232\n"
    "s_nop 1\n"
    "s_or_b64 s[58:59], s[58:59], s[60:61]\n"
    "v_bfrev_b32 v233, 1\n"
    "v_cndmask_b32_e64 v198, v198, v233, s[58:59]\n"
    "s_mov_b32 s56, -1\n"
    "s_cmp_eq_u32 s44, 3\n"
    "s_cselect_b32 s57, 0x3ff, -1\n"
    "s_mul_i32 s54, s45, 28\n"
    "s_add_u32 s54, s54, -1\n"
    "s_mul_i32 s54, s54, 0x15000\n"
    "s_lshl_b32 s55, s44, 10\n"
    "s_mov_b64 exec, s[56:57]\n"
    "s_mov_b32 m0, s55\n"
    "s_cmp_lt_i32 s54, 0\n"
    "s_cselect_b32 s50, 0x40000000, s54\n"
    "buffer_load_dwordx4 v198, s[20:23], s50 offen nt lds\n"
    "s_add_u32 s54, s54, 0x15000\n"
    "s_add_u32 s55, s55, 3744\n"
    "s_mov_b32 m0, s55\n"
    "s_nop 0\n"
    "buffer_load_dwordx4 v198, s[20:23], s54 offen nt lds\n"
    "s_add_u32 s54, s54, 0x15000\n"
    "s_add_u32 s55, s55, 3744\n"
    "s_mov_b32 m0, s55\n"
    "s_nop 0\n"
    "buffer_load_dwordx4 v198, s[20:23], s54 offen nt lds\n"
    "s_add_u32 s54, s54, 0x15000\n"
    "s_add_u32 s55, s55, 3744\n"
    "s_mov_b32 m0, s55\n"
    "s_nop 0\n"
    "buffer_load_dwordx4 v198, s[20:23], s54 offen nt lds\n"
    "s_add_u32 s54, s54, 0x15000\n"
    "s_add_u32 s55, s55, 3744\n"
    "s_mov_b32 m0, s55\n"
    "s_nop 0\n"
    "buffer_load_dwordx4 v198, s[20:23], s54 offen nt lds\n"
    "s_add_u32 s54, s54, 0x15000\n"
    "s_add_u32 s55, s55, 3744\n"
    "s_mov_b32 m0, s55\n"
    "s_nop 0\n"
    "buffer_load_dwordx4 v198, s[20:23], s54 offen nt lds\n"
    "s_add_u32 s54, s54, 0x15000\n"
    "s_add_u32 s55, s55, 3744\n"
    "s_mov_b32 m0, s55\n"
    "s_nop 0\n"
    "buffer_load_dwordx4 v198, s[20:23], s54 offen nt lds\n"
    "s_add_u32 s54, s54, 0x15000\n"
    "s_add_u32 s55, s55, 3744\n"
    "s_mov_b32 m0, s55\n"
    "s_nop 0\n"
    "buffer_load_dwordx4 v198, s[20:23], s54 offen nt lds\n"
    "s_add_u32 s54, s54, 0x15000\n"
    "s_add_u32 s55, s55, 3744\n"
    "s_mov_b32 m0, s55\n"
    "s_nop 0\n"
    "buffer_load_dwordx4 v198, s[20:23], s54 offen nt lds\n"
    "s_add_u32 s54, s54, 0x15000\n"
    "s_add_u32 s55, s55, 3744\n"
    "s_mov_b64 exec, -1\n"
    "v_mul_u32_u24 v228, 43, v225\n"
    "v_lshrrev_b32 v228, 8, v228\n"
    "v_mul_u32_u24 v229, 6, v228\n"
    "v_sub_u32 v229, v225, v229\n"
    "s_lshr_b32 s47, s51, 4\n"
    "v_add_u32 v230, s47, v229\n"
    "v_subrev_u32 v231, 6, v230\n"
    "v_min_u32 v230, v230, v231\n"
    "s_mul_i32 s47, s44, 96\n"
    "v_lshlrev_b32 v230, 4, v230\n"
    "v_add_u32 v230, s47, v230\n"
    "v_mul_u32_u24 v232, 11, v228\n"
    "v_lshrrev_b32 v232, 5, v232\n"
    "v_mul_u32_u24 v233, 3, v232\n"
    "v_sub_u32 v233, v228, v233\n"
    "s_mul_i32 s50, s46, 7\n"
    "s_sub_u32 s50, s50, 1\n"
    "v_add_u32 v233, s50, v233\n"
    "v_mul_u32_u24 v232, 0xe0, v232\n"
    "v_add_u32 v232, v232, v233\n"
    "v_mul_i32_i24 v232, 0x180, v232\n"
    "v_add_u32 v232, v232, v230\n"
    "v_cmp_gt_i32_e64 s[58:59], 0, v233\n"
    "s_nop 1\n"
    "s_or_b32 s59, s59, 0xffc00000\n"
    "v_bfrev_b32 v233, 1\n"
    "v_cndmask_b32_e64 v232, v232, v233, s[58:59]\n"
    "s_mul_i32 s49, s45, 28\n"
    "s_add_u32 s49, s49, 5\n"
    "s_mul_i32 s49, s49, 0x15000\n"
    "s_lshl_b32 s55, s44, 12\n"
    "s_add_u32 s55, s55, 0x186c0\n"
    "s_mov_b32 m0, s55\n"
    "s_add_u32 s55, s55, 1024\n"
    "buffer_load_dwordx4 v232, s[20:23], s49 offen nt lds\n"
    "s_add_u32 s49, s49, 0x93000\n"
    "s_mov_b32 m0, s55\n"
    "s_add_u32 s55, s55, 1024\n"
    "buffer_load_dwordx4 v232, s[20:23], s49 offen nt lds\n"
    "s_add_u32 s49, s49, 0x93000\n"
    "s_mov_b32 m0, s55\n"
    "s_add_u32 s55, s55, 1024\n"
    "buffer_load_dwordx4 v232, s[20:23], s49 offen nt lds\n"
    "s_add_u32 s49, s49, 0x93000\n"
    "s_mov_b32 m0, s55\n"
    "s_add_u32 s55, s55, 1024\n"
    "buffer_load_dwordx4 v232, s[20:23], s49 offen nt lds\n"
    "s_add_u32 s49, s49, 0x93000\n"
    "v_and_b32 v226, 15, v225\n"
    "v_lshrrev_b32 v227, 4, v225\n"
    "v_lshrrev_b32 v228, 1, v227\n"
    "v_and_b32 v229, 1, v227\n"
    "v_lshlrev_b32 v220, 4, v225\n"
    "v_add_u32 v220, 0x186c0, v220\n"
    "v_mul_u32_u24 v223, 0x1ba0, v228\n"
    "v_lshlrev_b32 v224, 3, v227\n"
    "v_lshlrev_b32 v238, 4, v229\n"
    "v_sub_u32 v224, v224, v238\n"
    "v_mul_u32_u24 v239, 0x1a0, v228\n"
    "v_sub_u32 v224, v224, v239\n"
    "v_add_u32 v238, v238, v239\n"
    "s_mul_i32 s47, s44, 96\n"
    "v_add_u32 v238, s47, v238\n"
    "v_and_b32 v239, 7, v226\n"
    "v_add_u32 v240, 4, v226\n"
    "v_bfe_u32 v240, v240, 3, 1\n"
    "v_add_u32 v241, 1, v239\n"
    "v_and_b32 v241, 7, v241\n"
    "v_add_u32 v242, 0, v240\n"
    "v_cmp_ge_u32_e64 s[58:59], v242, v241\n"
    "s_nop 1\n"
    "v_cndmask_b32_e64 v243, 0, 1, s[58:59]\n"
    "v_add_u32 v243, v243, v242\n"
    "v_sub_u32 v244, v239, v243\n"
    "v_and_b32 v244, 7, v244\n"
    "v_mad_u32_u24 v244, v243, 9, v244\n"
    "v_mul_u32_u24 v244, 0x1a0, v244\n"
    "v_add_u32 v216, v244, v238\n"
    "v_add_u32 v242, 2, v240\n"
    "v_cmp_ge_u32_e64 s[58:59], v242, v241\n"
    "s_nop 1\n"
    "v_cndmask_b32_e64 v243, 0, 1, s[58:59]\n"
    "v_add_u32 v243, v243, v242\n"
    "v_sub_u32 v244, v239, v243\n"
    "v_and_b32 v244, 7, v244\n"
    "v_mad_u32_u24 v244, v243, 9, v244\n"
    "v_mul_u32_u24 v244, 0x1a0, v244\n"
    "v_add_u32 v217, v244, v238\n"
    "v_add_u32 v242, 4, v240\n"
    "v_cmp_ge_u32_e64 s[58:59], v242, v241\n"
    "s_nop 1\n"
    "v_cndmask_b32_e64 v243, 0, 1, s[58:59]\n"
    "v_add_u32 v243, v243, v242\n"
    "v_sub_u32 v244, v239, v243\n"
    "v_and_b32 v244, 7, v244\n"
    "v_mad_u32_u24 v244, v243, 9, v244\n"
    "v_mul_u32_u24 v244, 0x1a0, v244\n"
    "v_add_u32 v218, v244, v238\n"
    "v_lshrrev_b32 v239, 3, v226\n"
    "v_cmp_eq_u32_e64 s[58:59], v239, v229\n"
    "v_lshlrev_b32 v239, 4, v226\n"
    "v_mov_b32 v240, 0x100\n"
    "v_cndmask_b32_e64 v219, v240, v239, s[58:59]\n"
    "v_mul_u32_u24 v239, 0xff0, v228\n"
    "s_mul_i32 s47, s44, 8160\n"
    "s_add_u32 s47, s47, 0x10740\n"
    "v_add3_u32 v219, v219, v239, s47\n"
    "v_lshlrev_b32 v221, 9, v229\n"
    "v_lshl_add_u32 v221, v228, 8, v221\n"
    "v_lshl_add_u32 v221, v226, 4, v221\n"
    "s_lshl_b32 s47, s44, 2\n"
    "s_add_u32 s47, s47, 0x276c0\n"
    "v_add_u32 v221, s47, v221\n"
    "v_cmp_lt_u32_e64 s[60:61], 23, v225\n"
    "s_nop 1\n"
    "v_cndmask_b32_e64 v244, 0, 1, s[60:61]\n"
    "v_mul_u32_u24 v245, 24, v244\n"
    "v_sub_u32 v245, v225, v245\n"
    "v_mul_u32_u24 v246, 43, v245\n"
    "v_lshrrev_b32 v246, 8, v246\n"
    "v_mul_u32_u24 v247, 6, v246\n"
    "v_sub_u32 v247, v245, v247\n"
    "v_cmp_lt_u32_e64 s[60:61], 1, v247\n"
    "s_nop 1\n"
    "v_cndmask_b32_e64 v247, 4, -2, s[60:61]\n"
    "v_and_b32 v246, 1, v246\n"
    "v_mul_i32_i24 v247, v247, v246\n"
    "v_add_u32 v245, v245, v247\n"
    "v_lshlrev_b32 v245, 4, v245\n"
    "v_mul_u32_u24 v244, 0x1a0, v244\n"
    "s_mul_i32 s47, s44, 832\n"
    "v_add3_u32 v222, v245, v244, s47\n"
    "v_lshrrev_b32 v199, 2, v225\n"
    "v_mul_u32_u24 v199, 0x480, v199\n"
    "v_lshrrev_b32 v244, 3, v225\n"
    "v_and_b32 v244, 2, v244\n"
    "v_xor_b32 v244, v244, v225\n"
    "v_and_b32 v244, 3, v244\n"
    "v_lshl_add_u32 v199, v244, 4, v199\n"
    "s_mul_i32 s47, s44, 96\n"
    "s_add_u32 s48, s47, s51\n"
    "v_add_u32 v199, s48, v199\n"
    "v_add_u32 v200, 0x4800, v199\n"
    "v_lshrrev_b32 v201, 1, v225\n"
    "v_mul_u32_u24 v201, 0x480, v201\n"
    "v_and_b32 v244, 1, v225\n"
    "v_lshl_add_u32 v201, v244, 4, v201\n"
    "s_add_u32 s48, s47, s53\n"
    "v_add_u32 v201, s48, v201\n"
    "s_mul_i32 s50, s44, 6144\n"
    "v_and_b32 v244, 16, v225\n"
    "v_lshlrev_b32 v244, 1, v244\n"
    "v_lshrrev_b32 v245, 2, v225\n"
    "v_lshlrev_b32 v202, 6, v245\n"
    "v_lshlrev_b32 v204, 5, v245\n"
    "v_and_b32 v245, 3, v225\n"
    "v_lshlrev_b32 v245, 3, v245\n"
    "v_add_u32 v204, v204, v245\n"
    "v_add_u32 v204, s50, v204\n"
    "v_xor_b32 v245, v245, v244\n"
    "v_add_u32 v202, v202, v245\n"
    "v_xor_b32 v203, 32, v202\n"
    "v_add_u32 v202, s50, v202\n"
    "v_add_u32 v203, s50, v203\n"
    "s_add_u32 s50, s62, 0x1cec0\n"
    "v_and_b32 v244, 16, v225\n"
    "v_lshlrev_b32 v244, 1, v244\n"
    "v_lshrrev_b32 v245, 2, v225\n"
    "v_lshlrev_b32 v212, 6, v245\n"
    "v_lshlrev_b32 v214, 5, v245\n"
    "v_and_b32 v245, 3, v225\n"
    "v_lshlrev_b32 v245, 3, v245\n"
    "v_add_u32 v214, v214, v245\n"
    "v_add_u32 v214, s50, v214\n"
    "v_xor_b32 v245, v245, v244\n"
    "v_add_u32 v212, v212, v245\n"
    "v_xor_b32 v213, 32, v212\n"
    "v_add_u32 v212, s50, v212\n"
    "v_add_u32 v213, s50, v213\n"
    "v_mov_b32 v205, 0x27ec0\n"
    "v_mov_b32 v211, 1\n"
    "v_lshrrev_b32 v206, 2, v225\n"
    "v_mul_u32_u24 v206, 0x180, v206\n"
    "v_lshrrev_b32 v244, 3, v225\n"
    "v_and_b32 v244, 2, v244\n"
    "v_xor_b32 v244, v244, v225\n"
    "v_and_b32 v244, 3, v244\n"
    "v_lshl_add_u32 v206, v244, 4, v206\n"
    "s_mul_i32 s47, s44, 96\n"
    "s_add_u32 s48, s47, s51\n"
    "v_add_u32 v206, s48, v206\n"
    "v_add_u32 v207, 0x1800, v206\n"
    "v_lshrrev_b32 v215, 1, v225\n"
    "v_mul_u32_u24 v215, 0x180, v215\n"
    "v_and_b32 v244, 1, v225\n"
    "v_lshl_add_u32 v215, v244, 4, v215\n"
    "s_add_u32 s48, s47, s53\n"
    "v_add_u32 v215, s48, v215\n"
    "v_lshrrev_b32 v208, 2, v226\n"
    "v_mul_u32_u24 v208, 7, v208\n"
    "s_mul_i32 s48, s45, 28\n"
    "s_add_u32 s48, s48, 6\n"
    "v_add_u32 v208, s48, v208\n"
    "s_mov_b32 s49, 0x15000\n"
    "v_mul_lo_u32 v208, v208, s49\n"
    "s_mul_i32 s48, s46, 2688\n"
    "s_mul_i32 s47, s44, 96\n"
    "s_add_u32 s48, s48, s47\n"
    "v_lshlrev_b32 v244, 3, v227\n"
    "v_add3_u32 v208, v208, v244, s48\n"
    "v_and_b32 v244, 3, v226\n"
    "v_cmp_ne_u32_e64 s[60:61], 0, v244\n"
    "v_bfrev_b32 v244, 1\n"
    "s_nop 0\n"
    "v_cndmask_b32_e64 v208, v208, v244, s[60:61]\n"
    "v_lshrrev_b32 v209, 2, v226\n"
    "v_lshlrev_b32 v209, 10, v209\n"
    "v_lshl_add_u32 v209, v229, 4, v209\n"
    "s_lshl_b32 s47, s44, 12\n"
    "s_add_u32 s47, s47, 0x186c0\n"
    "v_add_u32 v209, s47, v209\n"
    "v_mul_u32_u24 v244, 0x240, v228\n"
    "v_add_u32 v210, v209, v244\n"
    "v_mul_u32_u24 v245, 0x60, v228\n"
    "v_add_u32 v209, v209, v245\n"
    "s_mov_b32 s36, 0x3baaaaab\n"
    "s_mov_b32 s37, 0xb727c5ac\n"
    "s_waitcnt vmcnt(4)\n"
    "s_barrier\n"
    "ds_read_b128 v[226:229], v219 offset:3264\n"
    "ds_read_b128 v[230:233], v219 offset:3536\n"
    "v_add_u32 v225, v216, v223\n"
    "ds_read_b128 v[234:237], v225 offset:832\n"
    "ds_read_b128 v[246:249], v225 offset:864\n"
    "v_add_u32 v254, v217, v223\n"
    "ds_read_b128 v[238:241], v254 offset:832\n"
    "ds_read_b128 v[250:253], v254 offset:864\n"
    "v_add_u32 v255, v218, v223\n"
    "ds_read_b128 v[242:245], v255 offset:832\n"
    "s_waitcnt lgkmcnt(4)\n"
    "v_mfma_f32_16x16x32_bf16 v[144:147], v[226:229], v[234:237], 0\n"
    "ds_read_b128 v[234:237], v255 offset:864\n"
    "s_waitcnt lgkmcnt(4)\n"
    "v_mfma_f32_16x16x32_bf16 v[148:151], v[230:233], v[246:249], 0\n"
    "ds_read_b128 v[246:249], v255 offset:896\n"
    "s_waitcnt lgkmcnt(4)\n"
    "v_mfma_f32_16x16x32_bf16 v[156:159], v[226:229], v[238:241], 0\n"
    "ds_read_b128 v[238:241], v225 offset:896\n"
    "s_waitcnt lgkmcnt(3)\n"
    "v_mfma_f32_16x16x32_bf16 v[168:171], v[226:229], v[242:245], 0\n"
    "ds_read_b128 v[226:229], v219 offset:3808\n"
    "ds_read_b128 v[242:245], v254 offset:896\n"
    "v_mfma_f32_16x16x32_bf16 v[160:163], v[230:233], v[250:253], 0\n"
    "ds_read_b128 v[250:253], v216 offset:0\n"
    "s_waitcnt lgkmcnt(5)\n"
    "v_mfma_f32_16x16x32_bf16 v[172:175], v[230:233], v[234:237], 0\n"
    "ds_read_b128 v[230:233], v219 offset:0\n"
    "ds_read_b128 v[234:237], v217 offset:0\n"
    "s_waitcnt lgkmcnt(4)\n"
    "v_mfma_f32_16x16x32_bf16 v[152:155], v[226:229], v[238:241], 0\n"
    "ds_read_b128 v[238:241], v218 offset:0\n"
    "s_waitcnt lgkmcnt(4)\n"
    "v_mfma_f32_16x16x32_bf16 v[164:167], v[226:229], v[242:245], 0\n"
    "ds_read_b128 v[242:245], v216 offset:32\n"
    "v_mfma_f32_16x16x32_bf16 v[176:179], v[226:229], v[246:249], 0\n"
    "ds_read_b128 v[226:229], v219 offset:272\n"
    "ds_read_b128 v[246:249], v217 offset:32\n"
    "s_waitcnt lgkmcnt(5)\n"
    "v_mfma_f32_16x16x32_bf16 v[144:147], v[230:233], v[250:253], v[144:147]\n"
    "ds_read_b128 v[250:253], v218 offset:32\n"
    "s_waitcnt lgkmcnt(5)\n"
    "v_mfma_f32_16x16x32_bf16 v[156:159], v[230:233], v[234:237], v[156:159]\n"
    "ds_read_b128 v[234:237], v216 offset:64\n"
    "s_waitcnt lgkmcnt(5)\n"
    "v_mfma_f32_16x16x32_bf16 v[168:171], v[230:233], v[238:241], v[168:171]\n"
    "ds_read_b128 v[230:233], v219 offset:544\n"
    "ds_read_b128 v[238:241], v217 offset:64\n"
    "s_waitcnt lgkmcnt(5)\n"
    "v_mfma_f32_16x16x32_bf16 v[148:151], v[226:229], v[242:245], v[148:151]\n"
    "ds_read_b128 v[242:245], v218 offset:64\n"
    "s_waitcnt lgkmcnt(5)\n"
    "v_mfma_f32_16x16x32_bf16 v[160:163], v[226:229], v[246:249], v[160:163]\n"
    "ds_read_b128 v[246:249], v216 offset:3744\n"
    "s_waitcnt lgkmcnt(5)\n"
    "v_mfma_f32_16x16x32_bf16 v[172:175], v[226:229], v[250:253], v[172:175]\n"
    "ds_read_b128 v[226:229], v219 offset:816\n"
    "ds_read_b128 v[250:253], v217 offset:3744\n"
    "s_waitcnt lgkmcnt(5)\n"
    "v_mfma_f32_16x16x32_bf16 v[152:155], v[230:233], v[234:237], v[152:155]\n"
    "ds_read_b128 v[234:237], v218 offset:3744\n"
    "s_waitcnt lgkmcnt(5)\n"
    "v_mfma_f32_16x16x32_bf16 v[164:167], v[230:233], v[238:241], v[164:167]\n"
    "ds_read_b128 v[238:241], v216 offset:3776\n"
    "s_waitcnt lgkmcnt(5)\n"
    "v_mfma_f32_16x16x32_bf16 v[176:179], v[230:233], v[242:245], v[176:179]\n"
    "ds_read_b128 v[230:233], v219 offset:1088\n"
    "ds_read_b128 v[242:245], v217 offset:3776\n"
    "s_waitcnt lgkmcnt(5)\n"
    "v_mfma_f32_16x16x32_bf16 v[144:147], v[226:229], v[246:249], v[144:147]\n"
    "ds_read_b128 v[246:249], v218 offset:3776\n"
    "s_waitcnt lgkmcnt(5)\n"
    "v_mfma_f32_16x16x32_bf16 v[156:159], v[226:229], v[250:253], v[156:159]\n"
    "ds_read_b128 v[250:253], v216 offset:3808\n"
    "s_waitcnt lgkmcnt(5)\n"
    "v_mfma_f32_16x16x32_bf16 v[168:171], v[226:229], v[234:237], v[168:171]\n"
    "ds_read_b128 v[226:229], v219 offset:1360\n"
    "ds_read_b128 v[234:237], v217 offset:3808\n"
    "s_waitcnt lgkmcnt(5)\n"
    "v_mfma_f32_16x16x32_bf16 v[148:151], v[230:233], v[238:241], v[148:151]\n"
    "ds_read_b128 v[238:241], v218 offset:3808\n"
    "s_waitcnt lgkmcnt(5)\n"
    "v_mfma_f32_16x16x32_bf16 v[160:163], v[230:233], v[242:245], v[160:163]\n"
    "ds_read_b128 v[242:245], v216 offset:7488\n"
    "s_waitcnt lgkmcnt(5)\n"
    "v_mfma_f32_16x16x32_bf16 v[172:175], v[230:233], v[246:249], v[172:175]\n"
    "ds_read_b128 v[230:233], v219 offset:1632\n"
    "ds_read_b128 v[246:249], v217 offset:7488\n"
    "s_waitcnt lgkmcnt(5)\n"
    "v_mfma_f32_16x16x32_bf16 v[152:155], v[226:229], v[250:253], v[152:155]\n"
    "ds_read_b128 v[250:253], v218 offset:7488\n"
    "s_waitcnt lgkmcnt(5)\n"
    "v_mfma_f32_16x16x32_bf16 v[164:167], v[226:229], v[234:237], v[164:167]\n"
    "ds_read_b128 v[234:237], v216 offset:7520\n"
    "s_waitcnt lgkmcnt(5)\n"
    "v_mfma_f32_16x16x32_bf16 v[176:179], v[226:229], v[238:241], v[176:179]\n"
    "ds_read_b128 v[226:229], v219 offset:1904\n"
    "ds_read_b128 v[238:241], v217 offset:7520\n"
    "s_waitcnt lgkmcnt(5)\n"
    "v_mfma_f32_16x16x32_bf16 v[144:147], v[230:233], v[242:245], v[144:147]\n"
    "ds_read_b128 v[242:245], v218 offset:7520\n"
    "s_waitcnt lgkmcnt(5)\n"
    "v_mfma_f32_16x16x32_bf16 v[156:159], v[230:233], v[246:249], v[156:159]\n"
    "ds_read_b128 v[246:249], v216 offset:7552\n"
    "s_waitcnt lgkmcnt(5)\n"
    "v_mfma_f32_16x16x32_bf16 v[168:171], v[230:233], v[250:253], v[168:171]\n"
    "ds_read_b128 v[230:233], v219 offset:2176\n"
    "ds_read_b128 v[250:253], v217 offset:7552\n"
    "s_waitcnt lgkmcnt(5)\n"
    "v_mfma_f32_16x16x32_bf16 v[148:151], v[226:229], v[234:237], v[148:151]\n"
    "ds_read_b128 v[234:237], v218 offset:7552\n"
    "s_waitcnt lgkmcnt(5)\n"
    "v_mfma_f32_16x16x32_bf16 v[160:163], v[226:229], v[238:241], v[160:163]\n"
    "ds_read_b128 v[238:241], v216 offset:4160\n"
    "s_waitcnt lgkmcnt(5)\n"
    "v_mfma_f32_16x16x32_bf16 v[172:175], v[226:229], v[242:245], v[172:175]\n"
    "ds_read_b128 v[226:229], v219 offset:2448\n"
    "ds_read_b128 v[242:245], v217 offset:4160\n"
    "s_waitcnt lgkmcnt(5)\n"
    "v_mfma_f32_16x16x32_bf16 v[152:155], v[230:233], v[246:249], v[152:155]\n"
    "ds_read_b128 v[246:249], v218 offset:4160\n"
    "s_waitcnt lgkmcnt(5)\n"
    "v_mfma_f32_16x16x32_bf16 v[164:167], v[230:233], v[250:253], v[164:167]\n"
    "ds_read_b128 v[250:253], v216 offset:4192\n"
    "s_waitcnt lgkmcnt(5)\n"
    "v_mfma_f32_16x16x32_bf16 v[176:179], v[230:233], v[234:237], v[176:179]\n"
    "ds_read_b128 v[230:233], v219 offset:2720\n"
    "ds_read_b128 v[234:237], v217 offset:4192\n"
    "s_waitcnt lgkmcnt(5)\n"
    "v_mfma_f32_16x16x32_bf16 v[144:147], v[226:229], v[238:241], v[144:147]\n"
    "ds_read_b128 v[238:241], v218 offset:4192\n"
    "s_waitcnt lgkmcnt(5)\n"
    "v_mfma_f32_16x16x32_bf16 v[156:159], v[226:229], v[242:245], v[156:159]\n"
    "ds_read_b128 v[242:245], v216 offset:4224\n"
    "s_waitcnt lgkmcnt(5)\n"
    "v_mfma_f32_16x16x32_bf16 v[168:171], v[226:229], v[246:249], v[168:171]\n"
    "ds_read_b128 v[226:229], v219 offset:2992\n"
    "ds_read_b128 v[246:249], v217 offset:4224\n"
    "s_waitcnt lgkmcnt(5)\n"
    "v_mfma_f32_16x16x32_bf16 v[148:151], v[230:233], v[250:253], v[148:151]\n"
    "ds_read_b128 v[250:253], v218 offset:4224\n"
    "s_waitcnt lgkmcnt(5)\n"
    "v_mfma_f32_16x16x32_bf16 v[160:163], v[230:233], v[234:237], v[160:163]\n"
    "s_waitcnt lgkmcnt(4)\n"
    "v_mfma_f32_16x16x32_bf16 v[172:175], v[230:233], v[238:241], v[172:175]\n"
    "s_waitcnt lgkmcnt(2)\n"
    "v_mfma_f32_16x16x32_bf16 v[152:155], v[226:229], v[242:245], v[152:155]\n"
    "s_waitcnt lgkmcnt(1)\n"
    "v_mfma_f32_16x16x32_bf16 v[164:167], v[226:229], v[246:249], v[164:167]\n"
    "s_waitcnt lgkmcnt(0)\n"
    "v_mfma_f32_16x16x32_bf16 v[176:179], v[226:229], v[250:253], v[176:179]\n"
    "s_waitcnt vmcnt(0)\n"
    "ds_read_b128 v[84:87], v219 offset:3264\n"
    "ds_read_b128 v[88:91], v210 offset:192\n"
    "ds_read_b128 v[100:103], v219 offset:3808\n"
    "ds_read_b128 v[104:107], v210 offset:256\n"
    "ds_read_b128 v[92:95], v219 offset:3536\n"
    "ds_read_b128 v[96:99], v210 offset:224\n"
    "ds_read_b128 v[108:111], v219 offset:0\n"
    "ds_read_b128 v[112:115], v209 offset:0\n"
    "ds_read_b128 v[124:127], v219 offset:544\n"
    "ds_read_b128 v[128:131], v209 offset:64\n"
    "ds_read_b128 v[116:119], v219 offset:272\n"
    "ds_read_b128 v[120:123], v209 offset:32\n"
    "ds_read_b128 v[132:135], v219 offset:816\n"
    "ds_read_b128 v[136:139], v209 offset:288\n"
    "v_mul_f32 v227, v144, v144\n"
    "s_waitcnt lgkmcnt(12)\n"
    "v_mfma_f32_16x16x32_bf16 v[72:75], v[84:87], v[88:91], 0\n"
    "ds_read_b128 v[84:87], v219 offset:1088\n"
    "ds_read_b128 v[88:91], v209 offset:320\n"
    "v_mul_f32 v229, v145, v145\n"
    "s_waitcnt lgkmcnt(12)\n"
    "v_mfma_f32_16x16x32_bf16 v[80:83], v[100:103], v[104:107], 0\n"
    "ds_read_b128 v[100:103], v219 offset:1632\n"
    "ds_read_b128 v[104:107], v209 offset:576\n"
    "v_mul_f32 v231, v156, v156\n"
    "s_waitcnt lgkmcnt(12)\n"
    "v_mfma_f32_16x16x32_bf16 v[76:79], v[92:95], v[96:99], 0\n"
    "ds_read_b128 v[92:95], v219 offset:1360\n"
    "ds_read_b128 v[96:99], v209 offset:352\n"
    "v_mul_f32 v233, v157, v157\n"
    "s_waitcnt lgkmcnt(12)\n"
    "v_mfma_f32_16x16x32_bf16 v[72:75], v[108:111], v[112:115], v[72:75]\n"
    "ds_read_b128 v[108:111], v219 offset:1904\n"
    "ds_read_b128 v[112:115], v209 offset:608\n"
    "v_add_f32 v226, v144, v145\n"
    "s_waitcnt lgkmcnt(12)\n"
    "v_mfma_f32_16x16x32_bf16 v[80:83], v[124:127], v[128:131], v[80:83]\n"
    "ds_read_b128 v[124:127], v219 offset:2448\n"
    "ds_read_b128 v[128:131], v209 offset:384\n"
    "v_add_f32 v228, v146, v147\n"
    "s_waitcnt lgkmcnt(12)\n"
    "v_mfma_f32_16x16x32_bf16 v[76:79], v[116:119], v[120:123], v[76:79]\n"
    "ds_read_b128 v[116:119], v219 offset:2176\n"
    "ds_read_b128 v[120:123], v209 offset:640\n"
    "v_fmac_f32 v227, v146, v146\n"
    "s_waitcnt lgkmcnt(12)\n"
    "v_mfma_f32_16x16x32_bf16 v[72:75], v[132:135], v[136:139], v[72:75]\n"
    "ds_read_b128 v[132:135], v219 offset:2720\n"
    "ds_read_b128 v[136:139], v209 offset:416\n"
    "v_fmac_f32 v229, v147, v147\n"
    "s_waitcnt lgkmcnt(12)\n"
    "v_mfma_f32_16x16x32_bf16 v[76:79], v[84:87], v[88:91], v[76:79]\n"
    "ds_read_b128 v[84:87], v219 offset:2992\n"
    "ds_read_b128 v[88:91], v209 offset:448\n"
    "v_add_f32 v230, v156, v157\n"
    "s_waitcnt lgkmcnt(12)\n"
    "v_mfma_f32_16x16x32_bf16 v[72:75], v[100:103], v[104:107], v[72:75]\n"
    "v_add_f32 v232, v158, v159\n"
    "v_fmac_f32 v231, v158, v158\n"
    "s_waitcnt lgkmcnt(10)\n"
    "v_mfma_f32_16x16x32_bf16 v[80:83], v[92:95], v[96:99], v[80:83]\n"
    "v_fmac_f32 v233, v159, v159\n"
    "v_mul_f32 v235, v168, v168\n"
    "s_waitcnt lgkmcnt(8)\n"
    "v_mfma_f32_16x16x32_bf16 v[76:79], v[108:111], v[112:115], v[76:79]\n"
    "v_mul_f32 v237, v169, v169\n"
    "v_add_f32 v226, v226, v148\n"
    "s_waitcnt lgkmcnt(6)\n"
    "v_mfma_f32_16x16x32_bf16 v[72:75], v[124:127], v[128:131], v[72:75]\n"
    "v_add_f32 v228, v228, v149\n"
    "v_fmac_f32 v227, v148, v148\n"
    "s_waitcnt lgkmcnt(4)\n"
    "v_mfma_f32_16x16x32_bf16 v[80:83], v[116:119], v[120:123], v[80:83]\n"
    "v_fmac_f32 v229, v149, v149\n"
    "v_add_f32 v230, v230, v160\n"
    "s_waitcnt lgkmcnt(2)\n"
    "v_mfma_f32_16x16x32_bf16 v[76:79], v[132:135], v[136:139], v[76:79]\n"
    "v_add_f32 v232, v232, v161\n"
    "v_fmac_f32 v231, v160, v160\n"
    "s_waitcnt lgkmcnt(0)\n"
    "v_mfma_f32_16x16x32_bf16 v[80:83], v[84:87], v[88:91], v[80:83]\n"
    "v_mul_f32 v239, v72, v72\n"
    "v_mul_f32 v241, v73, v73\n"
    "v_fmac_f32 v233, v161, v161\n"
    "v_add_f32 v234, v168, v169\n"
    "v_add_f32 v236, v170, v171\n"
    "v_fmac_f32 v235, v170, v170\n"
    "v_fmac_f32 v237, v171, v171\n"
    "v_add_f32 v238, v72, v73\n"
    "v_add_f32 v240, v74, v75\n"
    "v_fmac_f32 v239, v74, v74\n"
    "v_fmac_f32 v241, v75, v75\n"
    "v_add_f32 v226, v226, v150\n"
    "v_add_f32 v228, v228, v151\n"
    "v_fmac_f32 v227, v150, v150\n"
    "v_fmac_f32 v229, v151, v151\n"
    "v_add_f32 v230, v230, v162\n"
    "v_add_f32 v232, v232, v163\n"
    "v_fmac_f32 v231, v162, v162\n"
    "v_fmac_f32 v233, v163, v163\n"
    "v_add_f32 v234, v234, v172\n"
    "v_add_f32 v236, v236, v173\n"
    "v_fmac_f32 v235, v172, v172\n"
    "v_fmac_f32 v237, v173, v173\n"
    "v_add_f32 v238, v238, v76\n"
    "v_add_f32 v240, v240, v77\n"
    "v_fmac_f32 v239, v76, v76\n"
    "v_fmac_f32 v241, v77, v77\n"
    "v_add_f32 v226, v226, v152\n"
    "v_add_f32 v228, v228, v153\n"
    "v_fmac_f32 v227, v152, v152\n"
    "v_fmac_f32 v229, v153, v153\n"
    "v_add_f32 v230, v230, v164\n"
    "v_add_f32 v232, v232, v165\n"
    "v_fmac_f32 v231, v164, v164\n"
    "v_fmac_f32 v233, v165, v165\n"
    "v_add_f32 v234, v234, v174\n"
    "v_add_f32 v236, v236, v175\n"
    "v_fmac_f32 v235, v174, v174\n"
    "v_fmac_f32 v237, v175, v175\n"
    "v_add_f32 v238, v238, v78\n"
    "v_add_f32 v240, v240, v79\n"
    "v_fmac_f32 v239, v78, v78\n"
    "v_fmac_f32 v241, v79, v79\n"
    "v_add_f32 v226, v226, v154\n"
    "v_add_f32 v228, v228, v155\n"
    "v_fmac_f32 v227, v154, v154\n"
    "v_fmac_f32 v229, v155, v155\n"
    "v_add_f32 v230, v230, v166\n"
    "v_add_f32 v232, v232, v167\n"
    "v_fmac_f32 v231, v166, v166\n"
    "v_fmac_f32 v233, v167, v167\n"
    "v_add_f32 v234, v234, v176\n"
    "v_add_f32 v236, v236, v177\n"
    "v_fmac_f32 v235, v176, v176\n"
    "v_fmac_f32 v237, v177, v177\n"
    "v_add_f32 v238, v238, v80\n"
    "v_add_f32 v240, v240, v81\n"
    "v_fmac_f32 v239, v80, v80\n"
    "v_fmac_f32 v241, v81, v81\n"
    "v_add_f32 v226, v226, v228\n"
    "v_add_f32 v227, v227, v229\n"
    "v_add_f32 v230, v230, v232\n"
    "v_add_f32 v231, v231, v233\n"
    "v_add_f32 v234, v234, v178\n"
    "v_add_f32 v236, v236, v179\n"
    "v_fmac_f32 v235, v178, v178\n"
    "v_fmac_f32 v237, v179, v179\n"
    "v_add_f32 v238, v238, v82\n"
    "v_add_f32 v240, v240, v83\n"
    "v_fmac_f32 v239, v82, v82\n"
    "v_fmac_f32 v241, v83, v83\n"
    "v_add_f32 v234, v234, v236\n"
    "v_add_f32 v235, v235, v237\n"
    "v_add_f32 v238, v238, v240\n"
    "v_add_f32 v239, v239, v241\n"
    "v_permlane32_swap_b32 v226, v227\n"
    "v_permlane32_swap_b32 v230, v231\n"
    "v_permlane32_swap_b32 v234, v235\n"
    "v_permlane32_swap_b32 v238, v239\n"
    "v_add_f32 v226, v226, v227\n"
    "v_add_f32 v230, v230, v231\n"
    "v_add_f32 v234, v234, v235\n"
    "v_add_f32 v238, v238, v239\n"
    "v_permlane16_swap_b32 v226, v230\n"
    "s_nop 0\n"
    "v_permlane16_swap_b32 v234, v238\n"
    "v_add_f32 v226, v226, v230\n"
    "ds_write_b32 v221, v226 offset:0\n"
    "v_add_f32 v234, v234, v238\n"
    "ds_write_b32 v221, v234 offset:1024\n"
    "s_waitcnt lgkmcnt(0)\n"
    "s_barrier\n"
    "s_mul_i32 s60, s44, 6144\n"
    "s_add_u32 s61, s62, 0x1cec0\n"
    "v_subrev_u32 v225, 0x186c0, v220\n"
    "v_and_b32 v225, 0xf0, v225\n"
    "v_add_u32 v225, 0x276c0, v225\n"
    "ds_read_b128 v[238:241], v225 offset:1536\n"
    "ds_read_b128 v[242:245], v225 offset:1792\n"
    "ds_read_b128 v[246:249], v225 offset:0\n"
    "ds_read_b128 v[250:253], v225 offset:256\n"
    "v_cvt_pk_bf16_f32 v108, v72, v73\n"
    "v_cvt_pk_bf16_f32 v109, v74, v75\n"
    "v_cvt_pk_bf16_f32 v110, v76, v77\n"
    "v_cvt_pk_bf16_f32 v111, v78, v79\n"
    "v_add_u32 v234, s62, v220\n"
    "v_cvt_pk_bf16_f32 v112, v80, v81\n"
    "v_cvt_pk_bf16_f32 v113, v82, v83\n"
    "v_cvt_pk_bf16_f32 v180, v144, v145\n"
    "s_waitcnt lgkmcnt(3)\n"
    "v_add_f32 v238, v238, v239\n"
    "v_add_f32 v240, v240, v241\n"
    "v_add_f32 v238, v238, v240\n"
    "s_waitcnt lgkmcnt(2)\n"
    "v_add_f32 v242, v242, v243\n"
    "v_add_f32 v244, v244, v245\n"
    "v_mul_f32 v238, s36, v238\n"
    "v_add_f32 v242, v242, v244\n"
    "v_fma_f32 v239, v238, v238, s37\n"
    "s_waitcnt lgkmcnt(1)\n"
    "v_add_f32 v246, v246, v247\n"
    "v_add_f32 v248, v248, v249\n"
    "v_fma_f32 v242, v242, s36, -v239\n"
    "v_add_f32 v246, v246, v248\n"
    "v_max_f32 v242, 0x3727c5ac, v242\n"
    "s_waitcnt lgkmcnt(0)\n"
    "v_add_f32 v250, v250, v251\n"
    "v_add_f32 v252, v252, v253\n"
    "v_mul_f32 v246, s36, v246\n"
    "v_rsq_f32 v226, v242\n"
    "ds_read_b128 v[242:245], v225 offset:768\n"
    "v_add_f32 v250, v250, v252\n"
    "v_fma_f32 v247, v246, v246, s37\n"
    "v_fma_f32 v250, v250, s36, -v247\n"
    "v_max_f32 v250, 0x3727c5ac, v250\n"
    "v_mul_f32_e64 v227, -v238, v226\n"
    "ds_read_b128 v[238:241], v225 offset:512\n"
    "v_rsq_f32 v228, v250\n"
    "ds_read_b128 v[250:253], v225 offset:1280\n"
    "v_mul_f32_e64 v229, -v246, v228\n"
    "ds_read_b128 v[246:249], v225 offset:1024\n"
    "s_waitcnt lgkmcnt(3)\n"
    "v_add_f32 v242, v242, v243\n"
    "v_add_f32 v244, v244, v245\n"
    "s_waitcnt lgkmcnt(2)\n"
    "v_add_f32 v238, v238, v239\n"
    "v_add_f32 v240, v240, v241\n"
    "v_add_f32 v238, v238, v240\n"
    "v_mul_f32 v238, s36, v238\n"
    "v_add_f32 v242, v242, v244\n"
    "v_fma_f32 v239, v238, v238, s37\n"
    "v_fma_f32 v72, v72, v226, v227\n"
    "v_fma_f32 v73, v73, v226, v227\n"
    "v_fma_f32 v242, v242, s36, -v239\n"
    "s_waitcnt lgkmcnt(0)\n"
    "v_add_f32 v246, v246, v247\n"
    "v_add_f32 v248, v248, v249\n"
    "v_add_f32 v246, v246, v248\n"
    "v_fma_f32 v74, v74, v226, v227\n"
    "v_fma_f32 v75, v75, v226, v227\n"
    "v_fma_f32 v76, v76, v226, v227\n"
    "v_fma_f32 v77, v77, v226, v227\n"
    "v_fma_f32 v78, v78, v226, v227\n"
    "v_fma_f32 v79, v79, v226, v227\n"
    "v_cvt_pk_bf16_f32 v72, v72, v73\n"
    "v_max_f32 v242, 0x3727c5ac, v242\n"
    "v_add_f32 v250, v250, v251\n"
    "v_add_f32 v252, v252, v253\n"
    "v_mul_f32 v246, s36, v246\n"
    "v_cvt_pk_bf16_f32 v73, v74, v75\n"
    "v_cvt_pk_bf16_f32 v74, v76, v77\n"
    "v_cvt_pk_bf16_f32 v75, v78, v79\n"
    "v_rsq_f32 v230, v242\n"
    "v_add_f32 v250, v250, v252\n"
    "v_fma_f32 v247, v246, v246, s37\n"
    "v_fma_f32 v80, v80, v226, v227\n"
    "v_fma_f32 v81, v81, v226, v227\n"
    "v_fma_f32 v82, v82, v226, v227\n"
    "v_fma_f32 v83, v83, v226, v227\n"
    "ds_write_b128 v234, v[72:75] offset:49152\n"
    "v_cvt_pk_bf16_f32 v181, v146, v147\n"
    "v_fma_f32 v144, v144, v228, v229\n"
    "v_fma_f32 v145, v145, v228, v229\n"
    "v_fma_f32 v250, v250, s36, -v247\n"
    "v_add_u32 v235, s63, v220\n"
    "v_cvt_pk_bf16_f32 v76, v80, v81\n"
    "v_cvt_pk_bf16_f32 v77, v82, v83\n"
    "v_cvt_pk_bf16_f32 v182, v148, v149\n"
    "v_cvt_pk_bf16_f32 v183, v150, v151\n"
    "v_fma_f32 v146, v146, v228, v229\n"
    "v_fma_f32 v147, v147, v228, v229\n"
    "v_fma_f32 v148, v148, v228, v229\n"
    "v_fma_f32 v149, v149, v228, v229\n"
    "v_fma_f32 v150, v150, v228, v229\n"
    "v_fma_f32 v151, v151, v228, v229\n"
    "v_cvt_pk_bf16_f32 v144, v144, v145\n"
    "v_max_f32 v250, 0x3727c5ac, v250\n"
    "ds_write_b64 v235, v[76:77] offset:49152\n"
    "v_cvt_pk_bf16_f32 v145, v146, v147\n"
    "v_mul_f32_e64 v231, -v238, v230\n"
    "v_cvt_pk_bf16_f32 v184, v152, v153\n"
    "v_cvt_pk_bf16_f32 v185, v154, v155\n"
    "v_cvt_pk_bf16_f32 v146, v148, v149\n"
    "v_cvt_pk_bf16_f32 v147, v150, v151\n"
    "v_rsq_f32 v232, v250\n"
    "v_fma_f32 v152, v152, v228, v229\n"
    "v_fma_f32 v153, v153, v228, v229\n"
    "v_fma_f32 v154, v154, v228, v229\n"
    "v_fma_f32 v155, v155, v228, v229\n"
    "v_cvt_pk_bf16_f32 v186, v156, v157\n"
    "ds_write_b128 v234, v[144:147] offset:0\n"
    "v_cvt_pk_bf16_f32 v187, v158, v159\n"
    "v_fma_f32 v156, v156, v230, v231\n"
    "v_fma_f32 v157, v157, v230, v231\n"
    "v_cvt_pk_bf16_f32 v148, v152, v153\n"
    "v_cvt_pk_bf16_f32 v149, v154, v155\n"
    "v_cvt_pk_bf16_f32 v188, v160, v161\n"
    "v_cvt_pk_bf16_f32 v189, v162, v163\n"
    "v_fma_f32 v158, v158, v230, v231\n"
    "v_fma_f32 v159, v159, v230, v231\n"
    "v_fma_f32 v160, v160, v230, v231\n"
    "v_fma_f32 v161, v161, v230, v231\n"
    "v_fma_f32 v162, v162, v230, v231\n"
    "v_fma_f32 v163, v163, v230, v231\n"
    "v_cvt_pk_bf16_f32 v156, v156, v157\n"
    "ds_write_b64 v235, v[148:149] offset:0\n"
    "v_cvt_pk_bf16_f32 v157, v158, v159\n"
    "v_mul_f32_e64 v233, -v246, v232\n"
    "v_cvt_pk_bf16_f32 v190, v164, v165\n"
    "v_cvt_pk_bf16_f32 v191, v166, v167\n"
    "v_cvt_pk_bf16_f32 v158, v160, v161\n"
    "v_cvt_pk_bf16_f32 v159, v162, v163\n"
    "v_fma_f32 v164, v164, v230, v231\n"
    "v_fma_f32 v165, v165, v230, v231\n"
    "v_fma_f32 v166, v166, v230, v231\n"
    "v_fma_f32 v167, v167, v230, v231\n"
    "v_cvt_pk_bf16_f32 v192, v168, v169\n"
    "ds_write_b128 v234, v[156:159] offset:6144\n"
    "v_cvt_pk_bf16_f32 v193, v170, v171\n"
    "v_fma_f32 v168, v168, v232, v233\n"
    "v_fma_f32 v169, v169, v232, v233\n"
    "v_cvt_pk_bf16_f32 v160, v164, v165\n"
    "v_cvt_pk_bf16_f32 v161, v166, v167\n"
    "v_cvt_pk_bf16_f32 v194, v172, v173\n"
    "v_cvt_pk_bf16_f32 v195, v174, v175\n"
    "v_fma_f32 v170, v170, v232, v233\n"
    "v_fma_f32 v171, v171, v232, v233\n"
    "v_fma_f32 v172, v172, v232, v233\n"
    "v_fma_f32 v173, v173, v232, v233\n"
    "v_fma_f32 v174, v174, v232, v233\n"
    "v_fma_f32 v175, v175, v232, v233\n"
    "v_cvt_pk_bf16_f32 v168, v168, v169\n"
    "ds_write_b64 v235, v[160:161] offset:6144\n"
    "v_cvt_pk_bf16_f32 v169, v170, v171\n"
    "v_cvt_pk_bf16_f32 v196, v176, v177\n"
    "v_cvt_pk_bf16_f32 v197, v178, v179\n"
    "v_cvt_pk_bf16_f32 v170, v172, v173\n"
    "v_cvt_pk_bf16_f32 v171, v174, v175\n"
    "v_fma_f32 v176, v176, v232, v233\n"
    "v_fma_f32 v177, v177, v232, v233\n"
    "v_fma_f32 v178, v178, v232, v233\n"
    "v_fma_f32 v179, v179, v232, v233\n"
    "ds_write_b128 v234, v[168:171] offset:12288\n"
    "v_cvt_pk_bf16_f32 v172, v176, v177\n"
    "v_cvt_pk_bf16_f32 v173, v178, v179\n"
    "ds_write_b64 v235, v[172:173] offset:12288\n"
    "s_add_u32 m0, s60, 0x0\n"
    "s_mov_b32 s49, 0x0\n"
    "buffer_load_dwordx4 v199, s[24:27], s49 offen lds\n"
    "s_add_u32 m0, s60, 0x400\n"
    "s_mov_b32 s49, 0x0\n"
    "buffer_load_dwordx4 v200, s[24:27], s49 offen lds\n"
    "s_waitcnt lgkmcnt(0)\n"
    "s_barrier\n"
    "s_add_u32 m0, s60, 0x800\n"
    "s_mov_b32 s49, 0x0\n"
    "buffer_load_dwordx4 v201, s[24:27], s49 offen lds\n"
    "s_add_u32 m0, s60, 0xc00\n"
    "s_mov_b32 s49, 0x9000\n"
    "buffer_load_dwordx4 v199, s[24:27], s49 offen lds\n"
    "s_add_u32 m0, s60, 0x1000\n"
    "s_mov_b32 s49, 0x9000\n"
    "buffer_load_dwordx4 v200, s[24:27], s49 offen lds\n"
    "s_add_u32 m0, s60, 0x1400\n"
    "s_mov_b32 s49, 0x9000\n"
    "buffer_load_dwordx4 v201, s[24:27], s49 offen lds\n"
    "s_add_u32 m0, s61, 0x0\n"
    "s_mov_b32 s49, 0x12000\n"
    "buffer_load_dwordx4 v199, s[24:27], s49 offen lds\n"
    "s_add_u32 m0, s61, 0x1800\n"
    "s_mov_b32 s49, 0x12000\n"
    "buffer_load_dwordx4 v200, s[24:27], s49 offen lds\n"
    "s_add_u32 m0, s61, 0x3000\n"
    "s_mov_b32 s49, 0x12000\n"
    "buffer_load_dwordx4 v201, s[24:27], s49 offen lds\n"
    "s_waitcnt vmcnt(6)\n"
    "ds_read_b64_tr_b16 v[0:1], v202 offset:0\n"
    "ds_read_b64_tr_b16 v[2:3], v202 offset:1024\n"
    "ds_read_b64_tr_b16 v[4:5], v203 offset:0\n"
    "ds_read_b64_tr_b16 v[6:7], v203 offset:1024\n"
    "ds_read_b64_tr_b16 v[8:9], v204 offset:2048\n"
    "ds_read_b64_tr_b16 v[10:11], v204 offset:2560\n"
    "s_waitcnt lgkmcnt(0)\n"
    "s_mov_b32 s49, 0x1b000\n"
    "s_add_u32 m0, s60, 0x0\n"
    "s_nop 0\n"
    "buffer_load_dwordx4 v199, s[24:27], s49 offen lds\n"
    "s_add_u32 m0, s60, 0x400\n"
    "s_nop 0\n"
    "buffer_load_dwordx4 v200, s[24:27], s49 offen lds\n"
    "s_add_u32 m0, s60, 0x800\n"
    "s_nop 0\n"
    "buffer_load_dwordx4 v201, s[24:27], s49 offen lds\n"
    "s_waitcnt vmcnt(6)\n"
    "ds_read_b64_tr_b16 v[12:13], v202 offset:3072\n"
    "ds_read_b64_tr_b16 v[14:15], v202 offset:4096\n"
    "ds_read_b64_tr_b16 v[16:17], v203 offset:3072\n"
    "ds_read_b64_tr_b16 v[18:19], v203 offset:4096\n"
    "ds_read_b64_tr_b16 v[20:21], v204 offset:5120\n"
    "ds_read_b64_tr_b16 v[22:23], v204 offset:5632\n"
    "s_waitcnt lgkmcnt(0)\n"
    "s_mov_b32 s49, 0x24000\n"
    "s_add_u32 m0, s60, 0xc00\n"
    "s_nop 0\n"
    "buffer_load_dwordx4 v199, s[24:27], s49 offen lds\n"
    "s_add_u32 m0, s60, 0x1000\n"
    "s_nop 0\n"
    "buffer_load_dwordx4 v200, s[24:27], s49 offen lds\n"
    "s_add_u32 m0, s60, 0x1400\n"
    "s_nop 0\n"
    "buffer_load_dwordx4 v201, s[24:27], s49 offen lds\n"
    "s_waitcnt vmcnt(6)\n"
    "ds_read_b64_tr_b16 v[24:25], v212 offset:0\n"
    "ds_read_b64_tr_b16 v[26:27], v212 offset:6144\n"
    "ds_read_b64_tr_b16 v[28:29], v213 offset:0\n"
    "ds_read_b64_tr_b16 v[30:31], v213 offset:6144\n"
    "ds_read_b64_tr_b16 v[32:33], v214 offset:12288\n"
    "ds_read_b64_tr_b16 v[34:35], v214 offset:12800\n"
    "s_waitcnt lgkmcnt(0)\n"
    "s_mov_b32 s49, 0x2d000\n"
    "s_add_u32 m0, s61, 0x0\n"
    "s_nop 0\n"
    "buffer_load_dwordx4 v199, s[24:27], s49 offen lds\n"
    "s_add_u32 m0, s61, 0x1800\n"
    "s_nop 0\n"
    "buffer_load_dwordx4 v200, s[24:27], s49 offen lds\n"
    "s_add_u32 m0, s61, 0x3000\n"
    "s_nop 0\n"
    "buffer_load_dwordx4 v201, s[24:27], s49 offen lds\n"
    "s_waitcnt vmcnt(6)\n"
    "ds_read_b64_tr_b16 v[36:37], v202 offset:0\n"
    "ds_read_b64_tr_b16 v[38:39], v202 offset:1024\n"
    "ds_read_b64_tr_b16 v[40:41], v203 offset:0\n"
    "ds_read_b64_tr_b16 v[42:43], v203 offset:1024\n"
    "ds_read_b64_tr_b16 v[44:45], v204 offset:2048\n"
    "ds_read_b64_tr_b16 v[46:47], v204 offset:2560\n"
    "LSP1_%=:\n"
    "ds_read_b32 v225, v205 offset:0\n"
    "s_waitcnt lgkmcnt(0)\n"
    "v_readfirstlane_b32 s50, v225\n"
    "s_nop 0\n"
    "s_cmp_ge_u32 s50, 8\n"
    "s_cbranch_scc0 LSP1_%=\n"
    "s_mul_i32 s54, s45, 28\n"
    "s_add_u32 s54, s54, 6\n"
    "s_mul_i32 s54, s54, 0x15000\n"
    "s_lshl_b32 s55, s44, 10\n"
    "s_add_u32 s55, s55, 0x83a0\n"
    "s_mov_b64 exec, s[56:57]\n"
    "s_mov_b32 m0, s55\n"
    "s_nop 0\n"
    "buffer_load_dwordx4 v198, s[20:23], s54 offen nt lds\n"
    "s_add_u32 s54, s54, 0x15000\n"
    "s_add_u32 s55, s55, 3744\n"
    "s_mov_b32 m0, s55\n"
    "s_nop 0\n"
    "buffer_load_dwordx4 v198, s[20:23], s54 offen nt lds\n"
    "s_add_u32 s54, s54, 0x15000\n"
    "s_add_u32 s55, s55, 3744\n"
    "s_mov_b32 m0, s55\n"
    "s_nop 0\n"
    "buffer_load_dwordx4 v198, s[20:23], s54 offen nt lds\n"
    "s_add_u32 s54, s54, 0x15000\n"
    "s_add_u32 s55, s55, 3744\n"
    "s_mov_b32 m0, s55\n"
    "s_nop 0\n"
    "buffer_load_dwordx4 v198, s[20:23], s54 offen nt lds\n"
    "s_add_u32 s54, s54, 0x15000\n"
    "s_add_u32 s55, s55, 3744\n"
    "s_mov_b32 m0, s55\n"
    "s_nop 0\n"
    "buffer_load_dwordx4 v198, s[20:23], s54 offen nt lds\n"
    "s_add_u32 s54, s54, 0x15000\n"
    "s_add_u32 s55, s55, 3744\n"
    "s_mov_b32 m0, s55\n"
    "s_nop 0\n"
    "buffer_load_dwordx4 v198, s[20:23], s54 offen nt lds\n"
    "s_add_u32 s54, s54, 0x15000\n"
    "s_add_u32 s55, s55, 3744\n"
    "s_mov_b32 m0, s55\n"
    "s_nop 0\n"
    "buffer_load_dwordx4 v198, s[20:23], s54 offen nt lds\n"
    "s_add_u32 s54, s54, 0x15000\n"
    "s_add_u32 s55, s55, 3744\n"
    "s_mov_b32 m0, s55\n"
    "s_nop 0\n"
    "buffer_load_dwordx4 v198, s[20:23], s54 offen nt lds\n"
    "s_add_u32 s54, s54, 0x15000\n"
    "s_add_u32 s55, s55, 3744\n"
    "s_mov_b32 m0, s55\n"
    "s_nop 0\n"
    "buffer_load_dwordx4 v198, s[20:23], s54 offen nt lds\n"
    "s_add_u32 s54, s54, 0x15000\n"
    "s_add_u32 s55, s55, 3744\n"
    "s_mov_b64 exec, -1\n"
    "s_waitcnt vmcnt(12)\n"
    "ds_read_b64_tr_b16 v[48:49], v202 offset:3072\n"
    "ds_read_b64_tr_b16 v[50:51], v202 offset:4096\n"
    "ds_read_b64_tr_b16 v[52:53], v203 offset:3072\n"
    "ds_read_b64_tr_b16 v[54:55], v203 offset:4096\n"
    "ds_read_b64_tr_b16 v[56:57], v204 offset:5120\n"
    "ds_read_b64_tr_b16 v[58:59], v204 offset:5632\n"
    "s_waitcnt vmcnt(9)\n"
    "ds_read_b64_tr_b16 v[60:61], v212 offset:0\n"
    "ds_read_b64_tr_b16 v[62:63], v212 offset:6144\n"
    "ds_read_b64_tr_b16 v[64:65], v213 offset:0\n"
    "ds_read_b64_tr_b16 v[66:67], v213 offset:6144\n"
    "ds_read_b64_tr_b16 v[68:69], v214 offset:12288\n"
    "ds_read_b64_tr_b16 v[70:71], v214 offset:12800\n"
    "s_setprio 1\n"
    "ds_read_b128 v[226:229], v220 offset:49152\n"
    "ds_read_b128 v[230:233], v220 offset:50176\n"
    "ds_read_b128 v[250:253], v220 offset:0\n"
    "ds_read_b128 v[234:237], v220 offset:51200\n"
    "ds_read_b128 v[238:241], v220 offset:52224\n"
    "ds_read_b128 v[242:245], v220 offset:53248\n"
    "ds_read_b128 v[246:249], v220 offset:54272\n"
    "v_add_u32 v225, s62, v220\n"
    "v_add_u32 v254, s63, v220\n"
    "s_waitcnt lgkmcnt(6)\n"
    "v_mfma_f32_16x16x32_bf16 v[72:75], v[0:3], v[226:229], 0\n"
    "v_mfma_f32_16x16x32_bf16 v[76:79], v[4:7], v[226:229], 0\n"
    "v_mfma_f32_16x16x32_bf16 v[80:83], v[8:11], v[226:229], 0\n"
    "ds_read_b128 v[226:229], v220 offset:1024\n"
    "s_waitcnt lgkmcnt(6)\n"
    "v_mfma_f32_16x16x32_bf16 v[72:75], v[12:15], v[230:233], v[72:75]\n"
    "v_mfma_f32_16x16x32_bf16 v[76:79], v[16:19], v[230:233], v[76:79]\n"
    "v_mfma_f32_16x16x32_bf16 v[80:83], v[20:23], v[230:233], v[80:83]\n"
    "ds_read_b128 v[230:233], v220 offset:2048\n"
    "s_waitcnt lgkmcnt(6)\n"
    "v_mfma_f32_16x16x32_bf16 v[84:87], v[0:3], v[250:253], 0\n"
    "v_mfma_f32_16x16x32_bf16 v[88:91], v[4:7], v[250:253], 0\n"
    "v_mfma_f32_16x16x32_bf16 v[92:95], v[8:11], v[250:253], 0\n"
    "ds_read_b128 v[250:253], v220 offset:7168\n"
    "s_waitcnt lgkmcnt(6)\n"
    "v_mfma_f32_16x16x32_bf16 v[72:75], v[24:27], v[234:237], v[72:75]\n"
    "v_mfma_f32_16x16x32_bf16 v[76:79], v[28:31], v[234:237], v[76:79]\n"
    "v_mfma_f32_16x16x32_bf16 v[80:83], v[32:35], v[234:237], v[80:83]\n"
    "ds_read_b128 v[234:237], v220 offset:3072\n"
    "s_waitcnt lgkmcnt(3)\n"
    "v_mfma_f32_16x16x32_bf16 v[84:87], v[12:15], v[226:229], v[84:87]\n"
    "v_mfma_f32_16x16x32_bf16 v[88:91], v[16:19], v[226:229], v[88:91]\n"
    "v_mfma_f32_16x16x32_bf16 v[92:95], v[20:23], v[226:229], v[92:95]\n"
    "ds_read_b128 v[226:229], v220 offset:8192\n"
    "v_mfma_f32_16x16x32_bf16 v[72:75], v[36:39], v[238:241], v[72:75]\n"
    "v_mfma_f32_16x16x32_bf16 v[76:79], v[40:43], v[238:241], v[76:79]\n"
    "v_mfma_f32_16x16x32_bf16 v[80:83], v[44:47], v[238:241], v[80:83]\n"
    "ds_read_b128 v[238:241], v220 offset:4096\n"
    "s_waitcnt lgkmcnt(4)\n"
    "v_mfma_f32_16x16x32_bf16 v[84:87], v[24:27], v[230:233], v[84:87]\n"
    "v_mfma_f32_16x16x32_bf16 v[88:91], v[28:31], v[230:233], v[88:91]\n"
    "v_mfma_f32_16x16x32_bf16 v[92:95], v[32:35], v[230:233], v[92:95]\n"
    "ds_read_b128 v[230:233], v220 offset:9216\n"
    "v_mfma_f32_16x16x32_bf16 v[72:75], v[48:51], v[242:245], v[72:75]\n"
    "v_mfma_f32_16x16x32_bf16 v[76:79], v[52:55], v[242:245], v[76:79]\n"
    "v_mfma_f32_16x16x32_bf16 v[80:83], v[56:59], v[242:245], v[80:83]\n"
    "ds_read_b128 v[242:245], v220 offset:5120\n"
    "s_waitcnt lgkmcnt(4)\n"
    "v_mfma_f32_16x16x32_bf16 v[84:87], v[36:39], v[234:237], v[84:87]\n"
    "v_mfma_f32_16x16x32_bf16 v[88:91], v[40:43], v[234:237], v[88:91]\n"
    "v_mfma_f32_16x16x32_bf16 v[92:95], v[44:47], v[234:237], v[92:95]\n"
    "ds_read_b128 v[234:237], v220 offset:10240\n"
    "v_mfma_f32_16x16x32_bf16 v[72:75], v[60:63], v[246:249], v[72:75]\n"
    "v_mfma_f32_16x16x32_bf16 v[76:79], v[64:67], v[246:249], v[76:79]\n"
    "v_mfma_f32_16x16x32_bf16 v[80:83], v[68:71], v[246:249], v[80:83]\n"
    "ds_read_b128 v[246:249], v220 offset:6144\n"
    "s_waitcnt lgkmcnt(4)\n"
    "v_mfma_f32_16x16x32_bf16 v[84:87], v[48:51], v[238:241], v[84:87]\n"
    "v_mfma_f32_16x16x32_bf16 v[88:91], v[52:55], v[238:241], v[88:91]\n"
    "v_mfma_f32_16x16x32_bf16 v[92:95], v[56:59], v[238:241], v[92:95]\n"
    "ds_read_b128 v[238:241], v220 offset:11264\n"
    "v_cvt_pk_bf16_f32 v72, v72, v73\n"
    "s_waitcnt lgkmcnt(3)\n"
    "v_mfma_f32_16x16x32_bf16 v[84:87], v[60:63], v[242:245], v[84:87]\n"
    "v_cvt_pk_bf16_f32 v73, v74, v75\n"
    "v_cvt_pk_bf16_f32 v74, v76, v77\n"
    "v_mfma_f32_16x16x32_bf16 v[88:91], v[64:67], v[242:245], v[88:91]\n"
    "v_cvt_pk_bf16_f32 v75, v78, v79\n"
    "ds_write_b128 v225, v[72:75] offset:55296\n"
    "v_mfma_f32_16x16x32_bf16 v[92:95], v[68:71], v[242:245], v[92:95]\n"
    "ds_read_b128 v[242:245], v220 offset:12288\n"
    "v_cvt_pk_bf16_f32 v76, v80, v81\n"
    "v_cvt_pk_bf16_f32 v77, v82, v83\n"
    "s_waitcnt lgkmcnt(3)\n"
    "v_mfma_f32_16x16x32_bf16 v[96:99], v[0:3], v[246:249], 0\n"
    "ds_write_b64 v254, v[76:77] offset:55296\n"
    "v_mfma_f32_16x16x32_bf16 v[100:103], v[4:7], v[246:249], 0\n"
    "v_cvt_pk_bf16_f32 v84, v84, v85\n"
    "v_cvt_pk_bf16_f32 v85, v86, v87\n"
    "v_mfma_f32_16x16x32_bf16 v[104:107], v[8:11], v[246:249], 0\n"
    "ds_read_b128 v[246:249], v220 offset:13312\n"
    "v_cvt_pk_bf16_f32 v86, v88, v89\n"
    "v_cvt_pk_bf16_f32 v87, v90, v91\n"
    "v_mfma_f32_16x16x32_bf16 v[96:99], v[12:15], v[250:253], v[96:99]\n"
    "ds_write_b128 v225, v[84:87] offset:18432\n"
    "v_mfma_f32_16x16x32_bf16 v[100:103], v[16:19], v[250:253], v[100:103]\n"
    "v_cvt_pk_bf16_f32 v88, v92, v93\n"
    "v_cvt_pk_bf16_f32 v89, v94, v95\n"
    "v_mfma_f32_16x16x32_bf16 v[104:107], v[20:23], v[250:253], v[104:107]\n"
    "ds_read_b128 v[250:253], v220 offset:14336\n"
    "ds_write_b64 v254, v[88:89] offset:18432\n"
    "s_waitcnt lgkmcnt(5)\n"
    "v_mfma_f32_16x16x32_bf16 v[72:75], v[0:3], v[242:245], 0\n"
    "v_mfma_f32_16x16x32_bf16 v[96:99], v[24:27], v[226:229], v[96:99]\n"
    "v_mfma_f32_16x16x32_bf16 v[100:103], v[28:31], v[226:229], v[100:103]\n"
    "v_mfma_f32_16x16x32_bf16 v[104:107], v[32:35], v[226:229], v[104:107]\n"
    "ds_read_b128 v[226:229], v220 offset:15360\n"
    "v_mfma_f32_16x16x32_bf16 v[76:79], v[4:7], v[242:245], 0\n"
    "v_mfma_f32_16x16x32_bf16 v[80:83], v[8:11], v[242:245], 0\n"
    "s_waitcnt lgkmcnt(4)\n"
    "v_mfma_f32_16x16x32_bf16 v[72:75], v[12:15], v[246:249], v[72:75]\n"
    "v_mfma_f32_16x16x32_bf16 v[96:99], v[36:39], v[230:233], v[96:99]\n"
    "v_mfma_f32_16x16x32_bf16 v[100:103], v[40:43], v[230:233], v[100:103]\n"
    "v_mfma_f32_16x16x32_bf16 v[104:107], v[44:47], v[230:233], v[104:107]\n"
    "ds_read_b128 v[230:233], v220 offset:16384\n"
    "v_mfma_f32_16x16x32_bf16 v[76:79], v[16:19], v[246:249], v[76:79]\n"
    "v_mfma_f32_16x16x32_bf16 v[80:83], v[20:23], v[246:249], v[80:83]\n"
    "s_waitcnt lgkmcnt(3)\n"
    "v_mfma_f32_16x16x32_bf16 v[72:75], v[24:27], v[250:253], v[72:75]\n"
    "v_mfma_f32_16x16x32_bf16 v[96:99], v[48:51], v[234:237], v[96:99]\n"
    "v_mfma_f32_16x16x32_bf16 v[100:103], v[52:55], v[234:237], v[100:103]\n"
    "v_mfma_f32_16x16x32_bf16 v[104:107], v[56:59], v[234:237], v[104:107]\n"
    "ds_read_b128 v[234:237], v220 offset:17408\n"
    "v_mfma_f32_16x16x32_bf16 v[76:79], v[28:31], v[250:253], v[76:79]\n"
    "v_mfma_f32_16x16x32_bf16 v[80:83], v[32:35], v[250:253], v[80:83]\n"
    "s_waitcnt lgkmcnt(2)\n"
    "v_mfma_f32_16x16x32_bf16 v[72:75], v[36:39], v[226:229], v[72:75]\n"
    "v_mfma_f32_16x16x32_bf16 v[76:79], v[40:43], v[226:229], v[76:79]\n"
    "v_mfma_f32_16x16x32_bf16 v[80:83], v[44:47], v[226:229], v[80:83]\n"
    "v_mfma_f32_16x16x32_bf16 v[96:99], v[60:63], v[238:241], v[96:99]\n"
    "s_waitcnt lgkmcnt(1)\n"
    "v_mfma_f32_16x16x32_bf16 v[72:75], v[48:51], v[230:233], v[72:75]\n"
    "v_mfma_f32_16x16x32_bf16 v[100:103], v[64:67], v[238:241], v[100:103]\n"
    "v_mfma_f32_16x16x32_bf16 v[76:79], v[52:55], v[230:233], v[76:79]\n"
    "v_mfma_f32_16x16x32_bf16 v[104:107], v[68:71], v[238:241], v[104:107]\n"
    "v_mfma_f32_16x16x32_bf16 v[80:83], v[56:59], v[230:233], v[80:83]\n"
    "s_waitcnt lgkmcnt(0)\n"
    "v_mfma_f32_16x16x32_bf16 v[72:75], v[60:63], v[234:237], v[72:75]\n"
    "v_cvt_pk_bf16_f32 v96, v96, v97\n"
    "v_mfma_f32_16x16x32_bf16 v[76:79], v[64:67], v[234:237], v[76:79]\n"
    "v_cvt_pk_bf16_f32 v97, v98, v99\n"
    "v_cvt_pk_bf16_f32 v98, v100, v101\n"
    "v_mfma_f32_16x16x32_bf16 v[80:83], v[68:71], v[234:237], v[80:83]\n"
    "v_cvt_pk_bf16_f32 v99, v102, v103\n"
    "ds_write_b128 v225, v[96:99] offset:24576\n"
    "v_cvt_pk_bf16_f32 v100, v104, v105\n"
    "v_cvt_pk_bf16_f32 v101, v106, v107\n"
    "v_cvt_pk_bf16_f32 v72, v72, v73\n"
    "ds_write_b64 v254, v[100:101] offset:24576\n"
    "v_cvt_pk_bf16_f32 v73, v74, v75\n"
    "v_cvt_pk_bf16_f32 v74, v76, v77\n"
    "v_cvt_pk_bf16_f32 v75, v78, v79\n"
    "ds_write_b128 v225, v[72:75] offset:30720\n"
    "v_cvt_pk_bf16_f32 v76, v80, v81\n"
    "v_cvt_pk_bf16_f32 v77, v82, v83\n"
    "ds_write_b64 v254, v[76:77] offset:30720\n"
    "s_waitcnt lgkmcnt(0)\n"
    "s_barrier\n"
    "s_waitcnt vmcnt(0)\n"
    "s_mov_b64 exec, 1\n"
    "ds_add_rtn_u32 v225, v205, v211 offset:4\n"
    "s_mov_b64 exec, -1\n"
    "s_waitcnt lgkmcnt(0)\n"
    "v_readfirstlane_b32 s50, v225\n"
    "s_nop 0\n"
    "s_cmp_ge_u32 s50, 3\n"
    "s_cbranch_scc1 LSD2_%=\n"
    "LSP2_%=:\n"
    "ds_read_b32 v225, v205 offset:4\n"
    "s_waitcnt lgkmcnt(0)\n"
    "v_readfirstlane_b32 s50, v225\n"
    "s_nop 0\n"
    "s_cmp_ge_u32 s50, 4\n"
    "s_cbranch_scc0 LSP2_%=\n"
    "LSD2_%=:\n"
    "s_setprio 2\n"
    "ds_read_b128 v[226:229], v219 offset:3264\n"
    "ds_read_b128 v[230:233], v219 offset:3536\n"
    "s_add_u32 m0, s60, 0x0\n"
    "s_mov_b32 s49, 0x0\n"
    "buffer_load_dwordx4 v206, s[28:31], s49 offen lds\n"
    "v_add_u32 v225, v216, v223\n"
    "ds_read_b128 v[234:237], v225 offset:34528\n"
    "ds_read_b128 v[246:249], v225 offset:34560\n"
    "v_add_u32 v254, v217, v223\n"
    "ds_read_b128 v[238:241], v254 offset:34528\n"
    "ds_read_b128 v[250:253], v254 offset:34560\n"
    "v_add_u32 v255, v218, v223\n"
    "ds_read_b128 v[242:245], v255 offset:34528\n"
    "s_waitcnt lgkmcnt(4)\n"
    "v_mfma_f32_16x16x32_bf16 v[144:147], v[226:229], v[234:237], 0\n"
    "ds_read_b128 v[234:237], v255 offset:34560\n"
    "s_add_u32 m0, s60, 0x400\n"
    "s_mov_b32 s49, 0x0\n"
    "buffer_load_dwordx4 v207, s[28:31], s49 offen lds\n"
    "s_waitcnt lgkmcnt(4)\n"
    "v_mfma_f32_16x16x32_bf16 v[148:151], v[230:233], v[246:249], 0\n"
    "ds_read_b128 v[246:249], v255 offset:34592\n"
    "s_waitcnt lgkmcnt(4)\n"
    "v_mfma_f32_16x16x32_bf16 v[156:159], v[226:229], v[238:241], 0\n"
    "ds_read_b128 v[238:241], v225 offset:34592\n"
    "s_waitcnt lgkmcnt(3)\n"
    "v_mfma_f32_16x16x32_bf16 v[168:171], v[226:229], v[242:245], 0\n"
    "ds_read_b128 v[226:229], v219 offset:3808\n"
    "ds_read_b128 v[242:245], v254 offset:34592\n"
    "v_mfma_f32_16x16x32_bf16 v[160:163], v[230:233], v[250:253], 0\n"
    "ds_read_b128 v[250:253], v216 offset:33696\n"
    "s_add_u32 m0, s60, 0x800\n"
    "s_mov_b32 s49, 0x0\n"
    "buffer_load_dwordx4 v215, s[28:31], s49 offen lds\n"
    "s_waitcnt lgkmcnt(5)\n"
    "v_mfma_f32_16x16x32_bf16 v[172:175], v[230:233], v[234:237], 0\n"
    "ds_read_b128 v[230:233], v219 offset:0\n"
    "ds_read_b128 v[234:237], v217 offset:33696\n"
    "s_waitcnt lgkmcnt(4)\n"
    "v_mfma_f32_16x16x32_bf16 v[152:155], v[226:229], v[238:241], 0\n"
    "ds_read_b128 v[238:241], v218 offset:33696\n"
    "s_waitcnt lgkmcnt(4)\n"
    "v_mfma_f32_16x16x32_bf16 v[164:167], v[226:229], v[242:245], 0\n"
    "ds_read_b128 v[242:245], v216 offset:33728\n"
    "v_mfma_f32_16x16x32_bf16 v[176:179], v[226:229], v[246:249], 0\n"
    "ds_read_b128 v[226:229], v219 offset:272\n"
    "ds_read_b128 v[246:249], v217 offset:33728\n"
    "s_add_u32 m0, s60, 0xc00\n"
    "s_mov_b32 s49, 0x3000\n"
    "buffer_load_dwordx4 v206, s[28:31], s49 offen lds\n"
    "s_waitcnt lgkmcnt(5)\n"
    "v_mfma_f32_16x16x32_bf16 v[144:147], v[230:233], v[250:253], v[144:147]\n"
    "ds_read_b128 v[250:253], v218 offset:33728\n"
    "s_waitcnt lgkmcnt(5)\n"
    "v_mfma_f32_16x16x32_bf16 v[156:159], v[230:233], v[234:237], v[156:159]\n"
    "ds_read_b128 v[234:237], v216 offset:33760\n"
    "s_waitcnt lgkmcnt(5)\n"
    "v_mfma_f32_16x16x32_bf16 v[168:171], v[230:233], v[238:241], v[168:171]\n"
    "ds_read_b128 v[230:233], v219 offset:544\n"
    "ds_read_b128 v[238:241], v217 offset:33760\n"
    "s_waitcnt lgkmcnt(5)\n"
    "v_mfma_f32_16x16x32_bf16 v[148:151], v[226:229], v[242:245], v[148:151]\n"
    "ds_read_b128 v[242:245], v218 offset:33760\n"
    "s_add_u32 m0, s60, 0x1000\n"
    "s_mov_b32 s49, 0x3000\n"
    "buffer_load_dwordx4 v207, s[28:31], s49 offen lds\n"
    "s_waitcnt lgkmcnt(5)\n"
    "v_mfma_f32_16x16x32_bf16 v[160:163], v[226:229], v[246:249], v[160:163]\n"
    "ds_read_b128 v[246:249], v216 offset:37440\n"
    "s_waitcnt lgkmcnt(5)\n"
    "v_mfma_f32_16x16x32_bf16 v[172:175], v[226:229], v[250:253], v[172:175]\n"
    "ds_read_b128 v[226:229], v219 offset:816\n"
    "ds_read_b128 v[250:253], v217 offset:37440\n"
    "s_waitcnt lgkmcnt(5)\n"
    "v_mfma_f32_16x16x32_bf16 v[152:155], v[230:233], v[234:237], v[152:155]\n"
    "ds_read_b128 v[234:237], v218 offset:37440\n"
    "s_waitcnt lgkmcnt(5)\n"
    "v_mfma_f32_16x16x32_bf16 v[164:167], v[230:233], v[238:241], v[164:167]\n"
    "ds_read_b128 v[238:241], v216 offset:37472\n"
    "s_add_u32 m0, s60, 0x1400\n"
    "s_mov_b32 s49, 0x3000\n"
    "buffer_load_dwordx4 v215, s[28:31], s49 offen lds\n"
    "s_waitcnt lgkmcnt(5)\n"
    "v_mfma_f32_16x16x32_bf16 v[176:179], v[230:233], v[242:245], v[176:179]\n"
    "ds_read_b128 v[230:233], v219 offset:1088\n"
    "ds_read_b128 v[242:245], v217 offset:37472\n"
    "s_waitcnt lgkmcnt(5)\n"
    "v_mfma_f32_16x16x32_bf16 v[144:147], v[226:229], v[246:249], v[144:147]\n"
    "ds_read_b128 v[246:249], v218 offset:37472\n"
    "s_waitcnt lgkmcnt(5)\n"
    "v_mfma_f32_16x16x32_bf16 v[156:159], v[226:229], v[250:253], v[156:159]\n"
    "ds_read_b128 v[250:253], v216 offset:37504\n"
    "s_waitcnt lgkmcnt(5)\n"
    "v_mfma_f32_16x16x32_bf16 v[168:171], v[226:229], v[234:237], v[168:171]\n"
    "ds_read_b128 v[226:229], v219 offset:1360\n"
    "ds_read_b128 v[234:237], v217 offset:37504\n"
    "s_add_u32 m0, s61, 0x4800\n"
    "s_mov_b32 s49, 0x6000\n"
    "buffer_load_dwordx4 v206, s[28:31], s49 offen lds\n"
    "s_waitcnt lgkmcnt(5)\n"
    "v_mfma_f32_16x16x32_bf16 v[148:151], v[230:233], v[238:241], v[148:151]\n"
    "ds_read_b128 v[238:241], v218 offset:37504\n"
    "s_waitcnt lgkmcnt(5)\n"
    "v_mfma_f32_16x16x32_bf16 v[160:163], v[230:233], v[242:245], v[160:163]\n"
    "ds_read_b128 v[242:245], v216 offset:41184\n"
    "s_waitcnt lgkmcnt(5)\n"
    "v_mfma_f32_16x16x32_bf16 v[172:175], v[230:233], v[246:249], v[172:175]\n"
    "ds_read_b128 v[230:233], v219 offset:1632\n"
    "ds_read_b128 v[246:249], v217 offset:41184\n"
    "s_waitcnt lgkmcnt(5)\n"
    "v_mfma_f32_16x16x32_bf16 v[152:155], v[226:229], v[250:253], v[152:155]\n"
    "ds_read_b128 v[250:253], v218 offset:41184\n"
    "s_add_u32 m0, s61, 0x6000\n"
    "s_mov_b32 s49, 0x6000\n"
    "buffer_load_dwordx4 v207, s[28:31], s49 offen lds\n"
    "s_waitcnt lgkmcnt(5)\n"
    "v_mfma_f32_16x16x32_bf16 v[164:167], v[226:229], v[234:237], v[164:167]\n"
    "ds_read_b128 v[234:237], v216 offset:41216\n"
    "s_waitcnt lgkmcnt(5)\n"
    "v_mfma_f32_16x16x32_bf16 v[176:179], v[226:229], v[238:241], v[176:179]\n"
    "ds_read_b128 v[226:229], v219 offset:1904\n"
    "ds_read_b128 v[238:241], v217 offset:41216\n"
    "s_waitcnt lgkmcnt(5)\n"
    "v_mfma_f32_16x16x32_bf16 v[144:147], v[230:233], v[242:245], v[144:147]\n"
    "ds_read_b128 v[242:245], v218 offset:41216\n"
    "s_waitcnt lgkmcnt(5)\n"
    "v_mfma_f32_16x16x32_bf16 v[156:159], v[230:233], v[246:249], v[156:159]\n"
    "ds_read_b128 v[246:249], v216 offset:41248\n"
    "s_add_u32 m0, s61, 0x7800\n"
    "s_mov_b32 s49, 0x6000\n"
    "buffer_load_dwordx4 v215, s[28:31], s49 offen lds\n"
    "s_waitcnt lgkmcnt(5)\n"
    "v_mfma_f32_16x16x32_bf16 v[168:171], v[230:233], v[250:253], v[168:171]\n"
    "ds_read_b128 v[230:233], v219 offset:2176\n"
    "ds_read_b128 v[250:253], v217 offset:41248\n"
    "s_waitcnt lgkmcnt(5)\n"
    "v_mfma_f32_16x16x32_bf16 v[148:151], v[226:229], v[234:237], v[148:151]\n"
    "ds_read_b128 v[234:237], v218 offset:41248\n"
    "s_waitcnt lgkmcnt(5)\n"
    "v_mfma_f32_16x16x32_bf16 v[160:163], v[226:229], v[238:241], v[160:163]\n"
    "ds_read_b128 v[238:241], v216 offset:37856\n"
    "s_waitcnt lgkmcnt(5)\n"
    "v_mfma_f32_16x16x32_bf16 v[172:175], v[226:229], v[242:245], v[172:175]\n"
    "ds_read_b128 v[226:229], v219 offset:2448\n"
    "ds_read_b128 v[242:245], v217 offset:37856\n"
    "s_waitcnt lgkmcnt(5)\n"
    "v_mfma_f32_16x16x32_bf16 v[152:155], v[230:233], v[246:249], v[152:155]\n"
    "ds_read_b128 v[246:249], v218 offset:37856\n"
    "s_waitcnt lgkmcnt(5)\n"
    "v_mfma_f32_16x16x32_bf16 v[164:167], v[230:233], v[250:253], v[164:167]\n"
    "ds_read_b128 v[250:253], v216 offset:37888\n"
    "s_waitcnt lgkmcnt(5)\n"
    "v_mfma_f32_16x16x32_bf16 v[176:179], v[230:233], v[234:237], v[176:179]\n"
    "ds_read_b128 v[230:233], v219 offset:2720\n"
    "ds_read_b128 v[234:237], v217 offset:37888\n"
    "s_waitcnt lgkmcnt(5)\n"
    "v_mfma_f32_16x16x32_bf16 v[144:147], v[226:229], v[238:241], v[144:147]\n"
    "ds_read_b128 v[238:241], v218 offset:37888\n"
    "s_waitcnt lgkmcnt(5)\n"
    "v_mfma_f32_16x16x32_bf16 v[156:159], v[226:229], v[242:245], v[156:159]\n"
    "ds_read_b128 v[242:245], v216 offset:37920\n"
    "s_waitcnt lgkmcnt(5)\n"
    "v_mfma_f32_16x16x32_bf16 v[168:171], v[226:229], v[246:249], v[168:171]\n"
    "ds_read_b128 v[226:229], v219 offset:2992\n"
    "ds_read_b128 v[246:249], v217 offset:37920\n"
    "s_waitcnt lgkmcnt(5)\n"
    "v_mfma_f32_16x16x32_bf16 v[148:151], v[230:233], v[250:253], v[148:151]\n"
    "ds_read_b128 v[250:253], v218 offset:37920\n"
    "s_waitcnt lgkmcnt(5)\n"
    "v_mfma_f32_16x16x32_bf16 v[160:163], v[230:233], v[234:237], v[160:163]\n"
    "s_waitcnt lgkmcnt(4)\n"
    "v_mfma_f32_16x16x32_bf16 v[172:175], v[230:233], v[238:241], v[172:175]\n"
    "s_setprio 0\n"
    "v_mul_f32 v231, v156, v156\n"
    "v_mul_f32 v233, v157, v157\n"
    "s_setprio 2\n"
    "s_waitcnt lgkmcnt(2)\n"
    "v_mfma_f32_16x16x32_bf16 v[152:155], v[226:229], v[242:245], v[152:155]\n"
    "s_setprio 0\n"
    "v_add_f32 v230, v156, v157\n"
    "v_add_f32 v232, v158, v159\n"
    "s_setprio 2\n"
    "s_waitcnt lgkmcnt(1)\n"
    "v_mfma_f32_16x16x32_bf16 v[164:167], v[226:229], v[246:249], v[164:167]\n"
    "s_setprio 0\n"
    "v_fmac_f32 v231, v158, v158\n"
    "v_fmac_f32 v233, v159, v159\n"
    "s_setprio 2\n"
    "s_waitcnt lgkmcnt(0)\n"
    "v_mfma_f32_16x16x32_bf16 v[176:179], v[226:229], v[250:253], v[176:179]\n"
    "s_setprio 0\n"
    "v_mul_f32 v227, v144, v144\n"
    "v_mul_f32 v229, v145, v145\n"
    "v_add_f32 v226, v144, v145\n"
    "v_add_f32 v228, v146, v147\n"
    "v_fmac_f32 v227, v146, v146\n"
    "v_fmac_f32 v229, v147, v147\n"
    "v_mul_f32 v235, v168, v168\n"
    "v_mul_f32 v237, v169, v169\n"
    "v_add_f32 v226, v226, v148\n"
    "v_add_f32 v228, v228, v149\n"
    "v_fmac_f32 v227, v148, v148\n"
    "v_fmac_f32 v229, v149, v149\n"
    "v_add_f32 v230, v230, v160\n"
    "v_add_f32 v232, v232, v161\n"
    "v_fmac_f32 v231, v160, v160\n"
    "v_fmac_f32 v233, v161, v161\n"
    "v_add_f32 v234, v168, v169\n"
    "v_add_f32 v236, v170, v171\n"
    "v_fmac_f32 v235, v170, v170\n"
    "v_fmac_f32 v237, v171, v171\n"
    "v_add_f32 v226, v226, v150\n"
    "v_add_f32 v228, v228, v151\n"
    "v_fmac_f32 v227, v150, v150\n"
    "v_fmac_f32 v229, v151, v151\n"
    "v_add_f32 v230, v230, v162\n"
    "v_add_f32 v232, v232, v163\n"
    "v_fmac_f32 v231, v162, v162\n"
    "v_fmac_f32 v233, v163, v163\n"
    "v_add_f32 v234, v234, v172\n"
    "v_add_f32 v236, v236, v173\n"
    "v_fmac_f32 v235, v172, v172\n"
    "v_fmac_f32 v237, v173, v173\n"
    "v_add_f32 v226, v226, v152\n"
    "v_add_f32 v228, v228, v153\n"
    "v_fmac_f32 v227, v152, v152\n"
    "v_fmac_f32 v229, v153, v153\n"
    "v_add_f32 v230, v230, v164\n"
    "v_add_f32 v232, v232, v165\n"
    "v_fmac_f32 v231, v164, v164\n"
    "v_fmac_f32 v233, v165, v165\n"
    "v_add_f32 v234, v234, v174\n"
    "v_add_f32 v236, v236, v175\n"
    "v_fmac_f32 v235, v174, v174\n"
    "v_fmac_f32 v237, v175, v175\n"
    "v_add_f32 v226, v226, v154\n"
    "v_add_f32 v228, v228, v155\n"
    "v_fmac_f32 v227, v154, v154\n"
    "v_fmac_f32 v229, v155, v155\n"
    "v_add_f32 v230, v230, v166\n"
    "v_add_f32 v232, v232, v167\n"
    "v_fmac_f32 v231, v166, v166\n"
    "v_fmac_f32 v233, v167, v167\n"
    "v_add_f32 v234, v234, v176\n"
    "v_add_f32 v236, v236, v177\n"
    "v_fmac_f32 v235, v176, v176\n"
    "v_fmac_f32 v237, v177, v177\n"
    "v_add_f32 v226, v226, v228\n"
    "v_add_f32 v227, v227, v229\n"
    "v_add_f32 v230, v230, v232\n"
    "v_add_f32 v231, v231, v233\n"
    "v_add_f32 v234, v234, v178\n"
    "v_add_f32 v236, v236, v179\n"
    "v_fmac_f32 v235, v178, v178\n"
    "v_fmac_f32 v237, v179, v179\n"
    "v_add_f32 v234, v234, v236\n"
    "v_add_f32 v235, v235, v237\n"
    "v_permlane32_swap_b32 v226, v227\n"
    "v_permlane32_swap_b32 v230, v231\n"
    "v_permlane32_swap_b32 v234, v235\n"
    "v_add_f32 v226, v226, v227\n"
    "v_add_f32 v230, v230, v231\n"
    "v_add_f32 v234, v234, v235\n"
    "s_nop 0\n"
    "v_permlane16_swap_b32 v226, v230\n"
    "v_permlane16_swap_b32 v234, v235\n"
    "v_add_f32 v226, v226, v230\n"
    "ds_write_b32 v221, v226 offset:0\n"
    "v_add_f32 v234, v234, v235\n"
    "ds_write_b32 v221, v234 offset:1024\n"
    "s_waitcnt vmcnt(0)\n"
    "ds_read_b64_tr_b16 v[72:73], v202 offset:0\n"
    "ds_read_b64_tr_b16 v[74:75], v202 offset:1024\n"
    "ds_read_b64_tr_b16 v[76:77], v203 offset:0\n"
    "ds_read_b64_tr_b16 v[78:79], v203 offset:1024\n"
    "ds_read_b64_tr_b16 v[80:81], v204 offset:2048\n"
    "ds_read_b64_tr_b16 v[82:83], v204 offset:2560\n"
    "ds_read_b64_tr_b16 v[84:85], v202 offset:3072\n"
    "ds_read_b64_tr_b16 v[86:87], v202 offset:4096\n"
    "ds_read_b64_tr_b16 v[88:89], v203 offset:3072\n"
    "ds_read_b64_tr_b16 v[90:91], v203 offset:4096\n"
    "ds_read_b64_tr_b16 v[92:93], v204 offset:5120\n"
    "ds_read_b64_tr_b16 v[94:95], v204 offset:5632\n"
    "ds_read_b64_tr_b16 v[96:97], v212 offset:18432\n"
    "ds_read_b64_tr_b16 v[98:99], v212 offset:24576\n"
    "ds_read_b64_tr_b16 v[100:101], v213 offset:18432\n"
    "ds_read_b64_tr_b16 v[102:103], v213 offset:24576\n"
    "ds_read_b64_tr_b16 v[104:105], v214 offset:30720\n"
    "ds_read_b64_tr_b16 v[106:107], v214 offset:31232\n"
    "s_mov_b64 exec, 1\n"
    "ds_add_rtn_u32 v225, v205, v211 offset:4\n"
    "s_mov_b64 exec, -1\n"
    "s_waitcnt lgkmcnt(0)\n"
    "v_readfirstlane_b32 s50, v225\n"
    "s_nop 0\n"
    "s_cmp_ge_u32 s50, 7\n"
    "s_cbranch_scc1 LSD3_%=\n"
    "LSP3_%=:\n"
    "ds_read_b32 v225, v205 offset:4\n"
    "s_waitcnt lgkmcnt(0)\n"
    "v_readfirstlane_b32 s50, v225\n"
    "s_nop 0\n"
    "s_cmp_ge_u32 s50, 8\n"
    "s_cbranch_scc0 LSP3_%=\n"
    "LSD3_%=:\n"
    "s_mul_i32 s54, s45, 28\n"
    "s_add_u32 s54, s54, 13\n"
    "s_mul_i32 s54, s54, 0x15000\n"
    "s_lshl_b32 s55, s44, 10\n"
    "v_mov_b32 v234, v198\n"
    "v_mov_b32 v235, v206\n"
    "v_mov_b32 v236, v207\n"
    "v_mov_b32 v237, v215\n"
    "v_mov_b32 v254, v208\n"
    "s_add_u32 m0, s60, 0x83a0\n"
    "s_mov_b32 s49, 0x9000\n"
    "buffer_load_dwordx4 v235, s[28:31], s49 offen lds\n"
    "v_subrev_u32 v225, 0x186c0, v220\n"
    "v_and_b32 v225, 0xf0, v225\n"
    "v_add_u32 v225, 0x276c0, v225\n"
    "ds_read_b128 v[238:241], v225 offset:0\n"
    "ds_read_b128 v[242:245], v225 offset:256\n"
    "ds_read_b128 v[246:249], v225 offset:512\n"
    "ds_read_b128 v[250:253], v225 offset:768\n"
    "v_cvt_pk_bf16_f32 v198, v144, v145\n"
    "v_cvt_pk_bf16_f32 v199, v146, v147\n"
    "v_cvt_pk_bf16_f32 v200, v148, v149\n"
    "v_cvt_pk_bf16_f32 v201, v150, v151\n"
    "v_add_u32 v232, s62, v220\n"
    "v_cvt_pk_bf16_f32 v202, v152, v153\n"
    "v_cvt_pk_bf16_f32 v203, v154, v155\n"
    "s_add_u32 m0, s60, 0x87a0\n"
    "s_mov_b32 s49, 0x9000\n"
    "buffer_load_dwordx4 v236, s[28:31], s49 offen lds\n"
    "s_waitcnt lgkmcnt(3)\n"
    "v_add_f32 v238, v238, v239\n"
    "v_add_f32 v240, v240, v241\n"
    "v_add_f32 v238, v238, v240\n"
    "s_waitcnt lgkmcnt(2)\n"
    "v_add_f32 v242, v242, v243\n"
    "v_add_f32 v244, v244, v245\n"
    "v_mul_f32 v238, s36, v238\n"
    "v_add_f32 v242, v242, v244\n"
    "v_fma_f32 v239, v238, v238, s37\n"
    "v_fma_f32 v242, v242, s36, -v239\n"
    "v_max_f32 v242, 0x3727c5ac, v242\n"
    "v_rsq_f32 v226, v242\n"
    "ds_read_b128 v[242:245], v225 offset:1280\n"
    "v_mul_f32_e64 v227, -v238, v226\n"
    "s_add_u32 m0, s60, 0x8ba0\n"
    "s_mov_b32 s49, 0x9000\n"
    "buffer_load_dwordx4 v237, s[28:31], s49 offen lds\n"
    "ds_read_b128 v[238:241], v225 offset:1024\n"
    "s_waitcnt lgkmcnt(3)\n"
    "v_add_f32 v246, v246, v247\n"
    "v_add_f32 v248, v248, v249\n"
    "v_add_f32 v246, v246, v248\n"
    "s_waitcnt lgkmcnt(2)\n"
    "v_add_f32 v250, v250, v251\n"
    "v_add_f32 v252, v252, v253\n"
    "v_mul_f32 v246, s36, v246\n"
    "v_add_f32 v250, v250, v252\n"
    "v_fma_f32 v247, v246, v246, s37\n"
    "v_fma_f32 v250, v250, s36, -v247\n"
    "v_max_f32 v250, 0x3727c5ac, v250\n"
    "s_waitcnt lgkmcnt(1)\n"
    "v_add_f32 v242, v242, v243\n"
    "v_add_f32 v244, v244, v245\n"
    "v_rsq_f32 v228, v250\n"
    "s_waitcnt lgkmcnt(0)\n"
    "v_add_f32 v238, v238, v239\n"
    "s_add_u32 m0, s60, 0x8fa0\n"
    "s_mov_b32 s49, 0xc000\n"
    "buffer_load_dwordx4 v235, s[28:31], s49 offen lds\n"
    "v_add_f32 v240, v240, v241\n"
    "v_add_f32 v238, v238, v240\n"
    "v_mul_f32 v238, s36, v238\n"
    "v_add_f32 v242, v242, v244\n"
    "v_fma_f32 v239, v238, v238, s37\n"
    "v_fma_f32 v144, v144, v226, v227\n"
    "v_fma_f32 v145, v145, v226, v227\n"
    "v_fma_f32 v242, v242, s36, -v239\n"
    "v_fma_f32 v146, v146, v226, v227\n"
    "v_fma_f32 v147, v147, v226, v227\n"
    "v_fma_f32 v148, v148, v226, v227\n"
    "v_fma_f32 v149, v149, v226, v227\n"
    "v_fma_f32 v150, v150, v226, v227\n"
    "v_fma_f32 v151, v151, v226, v227\n"
    "v_cvt_pk_bf16_f32 v144, v144, v145\n"
    "v_max_f32 v242, 0x3727c5ac, v242\n"
    "s_add_u32 m0, s60, 0x93a0\n"
    "s_mov_b32 s49, 0xc000\n"
    "buffer_load_dwordx4 v236, s[28:31], s49 offen lds\n"
    "v_cvt_pk_bf16_f32 v145, v146, v147\n"
    "v_mul_f32_e64 v229, -v246, v228\n"
    "v_cvt_pk_bf16_f32 v146, v148, v149\n"
    "v_cvt_pk_bf16_f32 v147, v150, v151\n"
    "v_rsq_f32 v230, v242\n"
    "v_fma_f32 v152, v152, v226, v227\n"
    "v_fma_f32 v153, v153, v226, v227\n"
    "v_fma_f32 v154, v154, v226, v227\n"
    "v_fma_f32 v155, v155, v226, v227\n"
    "v_cvt_pk_bf16_f32 v204, v156, v157\n"
    "ds_write_b128 v232, v[144:147] offset:0\n"
    "s_add_u32 m0, s60, 0x97a0\n"
    "s_mov_b32 s49, 0xc000\n"
    "buffer_load_dwordx4 v237, s[28:31], s49 offen lds\n"
    "v_cvt_pk_bf16_f32 v205, v158, v159\n"
    "v_fma_f32 v156, v156, v228, v229\n"
    "v_fma_f32 v157, v157, v228, v229\n"
    "v_add_u32 v233, s63, v220\n"
    "v_cvt_pk_bf16_f32 v148, v152, v153\n"
    "v_cvt_pk_bf16_f32 v149, v154, v155\n"
    "v_cvt_pk_bf16_f32 v206, v160, v161\n"
    "v_cvt_pk_bf16_f32 v207, v162, v163\n"
    "v_fma_f32 v158, v158, v228, v229\n"
    "v_fma_f32 v159, v159, v228, v229\n"
    "v_fma_f32 v160, v160, v228, v229\n"
    "v_fma_f32 v161, v161, v228, v229\n"
    "v_fma_f32 v162, v162, v228, v229\n"
    "v_fma_f32 v163, v163, v228, v229\n"
    "v_cvt_pk_bf16_f32 v156, v156, v157\n"
    "ds_write_b64 v233, v[148:149] offset:0\n"
    "s_add_u32 m0, s61, 0x4800\n"
    "s_mov_b32 s49, 0xf000\n"
    "buffer_load_dwordx4 v235, s[28:31], s49 offen lds\n"
    "v_cvt_pk_bf16_f32 v157, v158, v159\n"
    "v_mul_f32_e64 v231, -v238, v230\n"
    "v_cvt_pk_bf16_f32 v208, v164, v165\n"
    "v_cvt_pk_bf16_f32 v209, v166, v167\n"
    "v_cvt_pk_bf16_f32 v158, v160, v161\n"
    "v_cvt_pk_bf16_f32 v159, v162, v163\n"
    "v_fma_f32 v164, v164, v228, v229\n"
    "v_fma_f32 v165, v165, v228, v229\n"
    "v_fma_f32 v166, v166, v228, v229\n"
    "v_fma_f32 v167, v167, v228, v229\n"
    "v_cvt_pk_bf16_f32 v210, v168, v169\n"
    "ds_write_b128 v232, v[156:159] offset:6144\n"
    "s_add_u32 m0, s61, 0x6000\n"
    "s_mov_b32 s49, 0xf000\n"
    "buffer_load_dwordx4 v236, s[28:31], s49 offen lds\n"
    "v_cvt_pk_bf16_f32 v211, v170, v171\n"
    "v_fma_f32 v168, v168, v230, v231\n"
    "v_fma_f32 v169, v169, v230, v231\n"
    "v_cvt_pk_bf16_f32 v160, v164, v165\n"
    "v_cvt_pk_bf16_f32 v161, v166, v167\n"
    "v_cvt_pk_bf16_f32 v212, v172, v173\n"
    "v_cvt_pk_bf16_f32 v213, v174, v175\n"
    "v_fma_f32 v170, v170, v230, v231\n"
    "v_fma_f32 v171, v171, v230, v231\n"
    "v_fma_f32 v172, v172, v230, v231\n"
    "v_fma_f32 v173, v173, v230, v231\n"
    "v_fma_f32 v174, v174, v230, v231\n"
    "v_fma_f32 v175, v175, v230, v231\n"
    "v_cvt_pk_bf16_f32 v168, v168, v169\n"
    "ds_write_b64 v233, v[160:161] offset:6144\n"
    "s_add_u32 m0, s61, 0x7800\n"
    "s_mov_b32 s49, 0xf000\n"
    "buffer_load_dwordx4 v237, s[28:31], s49 offen lds\n"
    "v_cvt_pk_bf16_f32 v169, v170, v171\n"
    "v_cvt_pk_bf16_f32 v214, v176, v177\n"
    "v_cvt_pk_bf16_f32 v215, v178, v179\n"
    "v_cvt_pk_bf16_f32 v170, v172, v173\n"
    "v_cvt_pk_bf16_f32 v171, v174, v175\n"
    "v_fma_f32 v176, v176, v230, v231\n"
    "v_fma_f32 v177, v177, v230, v231\n"
    "v_fma_f32 v178, v178, v230, v231\n"
    "v_fma_f32 v179, v179, v230, v231\n"
    "ds_write_b128 v232, v[168:171] offset:12288\n"
    "v_cvt_pk_bf16_f32 v172, v176, v177\n"
    "v_cvt_pk_bf16_f32 v173, v178, v179\n"
    "s_mov_b32 m0, s55\n"
    "s_mov_b64 exec, s[56:57]\n"
    "buffer_load_dwordx4 v234, s[20:23], s54 offen nt lds\n"
    "s_mov_b64 exec, -1\n"
    "s_add_u32 s54, s54, 0x15000\n"
    "s_add_u32 s55, s55, 3744\n"
    "ds_write_b64 v233, v[172:173] offset:12288\n"
    "v_mov_b32 v177, v234\n"
    "s_mov_b32 m0, s55\n"
    "s_mov_b64 exec, s[56:57]\n"
    "buffer_load_dwordx4 v234, s[20:23], s54 offen nt lds\n"
    "s_mov_b64 exec, -1\n"
    "s_add_u32 s54, s54, 0x15000\n"
    "s_add_u32 s55, s55, 3744\n"
    "s_mov_b32 m0, s55\n"
    "s_mov_b64 exec, s[56:57]\n"
    "buffer_load_dwordx4 v234, s[20:23], s54 offen nt lds\n"
    "s_mov_b64 exec, -1\n"
    "s_add_u32 s54, s54, 0x15000\n"
    "s_add_u32 s55, s55, 3744\n"
    "s_mov_b32 m0, s55\n"
    "s_mov_b64 exec, s[56:57]\n"
    "buffer_load_dwordx4 v234, s[20:23], s54 offen nt lds\n"
    "s_mov_b64 exec, -1\n"
    "s_add_u32 s54, s54, 0x15000\n"
    "s_add_u32 s55, s55, 3744\n"
    "s_mov_b32 m0, s55\n"
    "s_mov_b64 exec, s[56:57]\n"
    "buffer_load_dwordx4 v234, s[20:23], s54 offen nt lds\n"
    "s_mov_b64 exec, -1\n"
    "s_add_u32 s54, s54, 0x15000\n"
    "s_add_u32 s55, s55, 3744\n"
    "s_mov_b32 m0, s55\n"
    "s_mov_b64 exec, s[56:57]\n"
    "buffer_load_dwordx4 v234, s[20:23], s54 offen nt lds\n"
    "s_mov_b64 exec, -1\n"
    "s_add_u32 s54, s54, 0x15000\n"
    "s_add_u32 s55, s55, 3744\n"
    "s_mov_b32 m0, s55\n"
    "s_mov_b64 exec, s[56:57]\n"
    "buffer_load_dwordx4 v234, s[20:23], s54 offen nt lds\n"
    "s_mov_b64 exec, -1\n"
    "s_add_u32 s54, s54, 0x15000\n"
    "s_add_u32 s55, s55, 3744\n"
    "s_mov_b32 m0, s55\n"
    "s_mov_b64 exec, s[56:57]\n"
    "buffer_load_dwordx4 v234, s[20:23], s54 offen nt lds\n"
    "s_mov_b64 exec, -1\n"
    "s_add_u32 s54, s54, 0x15000\n"
    "s_add_u32 s55, s55, 3744\n"
    "s_mov_b32 m0, s55\n"
    "s_mov_b64 exec, s[56:57]\n"
    "buffer_load_dwordx4 v234, s[20:23], s54 offen nt lds\n"
    "s_mov_b64 exec, -1\n"
    "s_add_u32 s54, s54, 0x15000\n"
    "s_add_u32 s55, s55, 3744\n"
    "buffer_store_dwordx2 v[108:109], v254, s[40:43], s51 offen offset:0 sc0 sc1\n"
    "buffer_store_dwordx2 v[110:111], v254, s[40:43], s52 offen offset:0 sc0 sc1\n"
    "buffer_store_dwordx2 v[112:113], v254, s[40:43], s53 offen offset:0 sc0 sc1\n"
    "s_cmp_eq_u32 s44, 3\n"
    "s_cselect_b32 s24, 0xffffff, -1\n"
    "s_cselect_b32 s25, 0, 0xffff\n"
    "s_cmp_eq_u32 s44, 0\n"
    "s_cselect_b32 s26, 0xff000000, s24\n"
    "s_mov_b32 s27, s25\n"
    "s_sub_u32 s28, s40, 0x186c0\n"
    "s_subb_u32 s29, s41, 0\n"
    "s_mov_b32 s30, 0x12786c0\n"
    "s_mov_b32 s31, 0x20000\n"
    "s_waitcnt lgkmcnt(0)\n"
    "s_barrier\n"
    "v_mbcnt_lo_u32_b32 v174, -1, 0\n"
    "v_mbcnt_hi_u32_b32 v174, -1, v174\n"
    "v_lshrrev_b32 v176, 2, v174\n"
    "v_lshlrev_b32 v168, 6, v176\n"
    "v_lshlrev_b32 v170, 5, v176\n"
    "v_and_b32 v176, 3, v174\n"
    "v_and_b32 v175, 16, v174\n"
    "v_lshlrev_b32 v176, 3, v176\n"
    "v_lshlrev_b32 v175, 1, v175\n"
    "v_add_u32 v170, v170, v176\n"
    "v_xor_b32 v176, v176, v175\n"
    "v_add_u32 v168, v168, v176\n"
    "v_xor_b32 v169, 32, v168\n"
    "v_add_u32 v170, s60, v170\n"
    "v_add_u32 v168, s60, v168\n"
    "v_add_u32 v169, s60, v169\n"
    "s_sub_u32 s47, s61, s60\n"
    "v_add_u32 v171, s47, v168\n"
    "v_add_u32 v172, s47, v169\n"
    "v_add_u32 v173, s47, v170\n"
    "s_waitcnt vmcnt(12)\n"
    "s_setprio 1\n"
    "ds_read_b128 v[226:229], v220 offset:0\n"
    "ds_read_b128 v[230:233], v220 offset:1024\n"
    "ds_read_b128 v[234:237], v220 offset:2048\n"
    "ds_read_b128 v[238:241], v220 offset:3072\n"
    "ds_read_b128 v[242:245], v220 offset:4096\n"
    "ds_read_b128 v[246:249], v220 offset:5120\n"
    "ds_read_b128 v[250:253], v220 offset:6144\n"
    "ds_read_b64_tr_b16 v[108:109], v168 offset:33696\n"
    "ds_read_b64_tr_b16 v[110:111], v168 offset:34720\n"
    "ds_read_b64_tr_b16 v[112:113], v169 offset:33696\n"
    "ds_read_b64_tr_b16 v[114:115], v169 offset:34720\n"
    "ds_read_b64_tr_b16 v[116:117], v170 offset:35744\n"
    "ds_read_b64_tr_b16 v[118:119], v170 offset:36256\n"
    "ds_read_b64_tr_b16 v[120:121], v168 offset:36768\n"
    "s_waitcnt lgkmcnt(13)\n"
    "v_mfma_f32_16x16x32_bf16 v[144:147], v[0:3], v[226:229], 0\n"
    "ds_read_b64_tr_b16 v[122:123], v168 offset:37792\n"
    "ds_read_b64_tr_b16 v[124:125], v169 offset:36768\n"
    "ds_read_b64_tr_b16 v[126:127], v169 offset:37792\n"
    "v_mfma_f32_16x16x32_bf16 v[148:151], v[4:7], v[226:229], 0\n"
    "ds_read_b64_tr_b16 v[128:129], v170 offset:38816\n"
    "ds_read_b64_tr_b16 v[130:131], v170 offset:39328\n"
    "ds_read_b64_tr_b16 v[132:133], v171 offset:18432\n"
    "v_mfma_f32_16x16x32_bf16 v[152:155], v[8:11], v[226:229], 0\n"
    "ds_read_b128 v[226:229], v220 offset:7168\n"
    "ds_read_b64_tr_b16 v[134:135], v171 offset:24576\n"
    "ds_read_b64_tr_b16 v[136:137], v172 offset:18432\n"
    "s_waitcnt lgkmcnt(15)\n"
    "v_mfma_f32_16x16x32_bf16 v[144:147], v[12:15], v[230:233], v[144:147]\n"
    "ds_read_b64_tr_b16 v[138:139], v172 offset:24576\n"
    "ds_read_b64_tr_b16 v[140:141], v173 offset:30720\n"
    "ds_read_b64_tr_b16 v[142:143], v173 offset:31232\n"
    "v_mfma_f32_16x16x32_bf16 v[148:151], v[16:19], v[230:233], v[148:151]\n"
    "v_add_u32 v225, s62, v220\n"
    "v_add_u32 v254, s63, v220\n"
    "v_mfma_f32_16x16x32_bf16 v[152:155], v[20:23], v[230:233], v[152:155]\n"
    "ds_read_b128 v[230:233], v220 offset:8192\n"
    "v_mfma_f32_16x16x32_bf16 v[144:147], v[24:27], v[234:237], v[144:147]\n"
    "v_mfma_f32_16x16x32_bf16 v[148:151], v[28:31], v[234:237], v[148:151]\n"
    "v_mfma_f32_16x16x32_bf16 v[152:155], v[32:35], v[234:237], v[152:155]\n"
    "ds_read_b128 v[234:237], v220 offset:9216\n"
    "v_mfma_f32_16x16x32_bf16 v[144:147], v[36:39], v[238:241], v[144:147]\n"
    "v_mfma_f32_16x16x32_bf16 v[148:151], v[40:43], v[238:241], v[148:151]\n"
    "v_mfma_f32_16x16x32_bf16 v[152:155], v[44:47], v[238:241], v[152:155]\n"
    "ds_read_b128 v[238:241], v220 offset:10240\n"
    "v_mfma_f32_16x16x32_bf16 v[144:147], v[48:51], v[242:245], v[144:147]\n"
    "v_mfma_f32_16x16x32_bf16 v[148:151], v[52:55], v[242:245], v[148:151]\n"
    "v_mfma_f32_16x16x32_bf16 v[152:155], v[56:59], v[242:245], v[152:155]\n"
    "ds_read_b128 v[242:245], v220 offset:11264\n"
    "v_mfma_f32_16x16x32_bf16 v[144:147], v[60:63], v[246:249], v[144:147]\n"
    "v_mfma_f32_16x16x32_bf16 v[148:151], v[64:67], v[246:249], v[148:151]\n"
    "v_mfma_f32_16x16x32_bf16 v[152:155], v[68:71], v[246:249], v[152:155]\n"
    "ds_read_b128 v[246:249], v220 offset:12288\n"
    "v_mfma_f32_16x16x32_bf16 v[156:159], v[0:3], v[250:253], 0\n"
    "v_mfma_f32_16x16x32_bf16 v[160:163], v[4:7], v[250:253], 0\n"
    "v_mfma_f32_16x16x32_bf16 v[164:167], v[8:11], v[250:253], 0\n"
    "ds_read_b128 v[250:253], v220 offset:13312\n"
    "s_nop 0\n"
    "v_cvt_pk_bf16_f32 v144, v144, v145\n"
    "s_waitcnt lgkmcnt(11)\n"
    "v_mfma_f32_16x16x32_bf16 v[156:159], v[12:15], v[226:229], v[156:159]\n"
    "v_cvt_pk_bf16_f32 v145, v146, v147\n"
    "v_cvt_pk_bf16_f32 v146, v148, v149\n"
    "v_mfma_f32_16x16x32_bf16 v[160:163], v[16:19], v[226:229], v[160:163]\n"
    "v_cvt_pk_bf16_f32 v147, v150, v151\n"
    "ds_write_b128 v225, v[144:147] offset:36864\n"
    "v_mfma_f32_16x16x32_bf16 v[164:167], v[20:23], v[226:229], v[164:167]\n"
    "ds_read_b128 v[226:229], v220 offset:14336\n"
    "v_cvt_pk_bf16_f32 v148, v152, v153\n"
    "v_cvt_pk_bf16_f32 v149, v154, v155\n"
    "s_waitcnt lgkmcnt(3)\n"
    "v_mfma_f32_16x16x32_bf16 v[144:147], v[0:3], v[246:249], 0\n"
    "ds_write_b64 v254, v[148:149] offset:36864\n"
    "v_mfma_f32_16x16x32_bf16 v[156:159], v[24:27], v[230:233], v[156:159]\n"
    "v_mfma_f32_16x16x32_bf16 v[160:163], v[28:31], v[230:233], v[160:163]\n"
    "v_mfma_f32_16x16x32_bf16 v[164:167], v[32:35], v[230:233], v[164:167]\n"
    "ds_read_b128 v[230:233], v220 offset:15360\n"
    "v_mfma_f32_16x16x32_bf16 v[148:151], v[4:7], v[246:249], 0\n"
    "v_mfma_f32_16x16x32_bf16 v[152:155], v[8:11], v[246:249], 0\n"
    "s_waitcnt lgkmcnt(4)\n"
    "v_mfma_f32_16x16x32_bf16 v[144:147], v[12:15], v[250:253], v[144:147]\n"
    "v_mfma_f32_16x16x32_bf16 v[156:159], v[36:39], v[234:237], v[156:159]\n"
    "v_mfma_f32_16x16x32_bf16 v[160:163], v[40:43], v[234:237], v[160:163]\n"
    "v_mfma_f32_16x16x32_bf16 v[164:167], v[44:47], v[234:237], v[164:167]\n"
    "ds_read_b128 v[234:237], v220 offset:16384\n"
    "v_mfma_f32_16x16x32_bf16 v[148:151], v[16:19], v[250:253], v[148:151]\n"
    "v_mfma_f32_16x16x32_bf16 v[152:155], v[20:23], v[250:253], v[152:155]\n"
    "s_waitcnt lgkmcnt(3)\n"
    "v_mfma_f32_16x16x32_bf16 v[144:147], v[24:27], v[226:229], v[144:147]\n"
    "v_mfma_f32_16x16x32_bf16 v[156:159], v[48:51], v[238:241], v[156:159]\n"
    "v_mfma_f32_16x16x32_bf16 v[160:163], v[52:55], v[238:241], v[160:163]\n"
    "v_mfma_f32_16x16x32_bf16 v[164:167], v[56:59], v[238:241], v[164:167]\n"
    "ds_read_b128 v[238:241], v220 offset:17408\n"
    "v_mfma_f32_16x16x32_bf16 v[148:151], v[28:31], v[226:229], v[148:151]\n"
    "v_mfma_f32_16x16x32_bf16 v[152:155], v[32:35], v[226:229], v[152:155]\n"
    "s_waitcnt lgkmcnt(2)\n"
    "v_mfma_f32_16x16x32_bf16 v[144:147], v[36:39], v[230:233], v[144:147]\n"
    "v_mfma_f32_16x16x32_bf16 v[148:151], v[40:43], v[230:233], v[148:151]\n"
    "v_mfma_f32_16x16x32_bf16 v[152:155], v[44:47], v[230:233], v[152:155]\n"
    "v_mfma_f32_16x16x32_bf16 v[156:159], v[60:63], v[242:245], v[156:159]\n"
    "s_waitcnt lgkmcnt(1)\n"
    "v_mfma_f32_16x16x32_bf16 v[144:147], v[48:51], v[234:237], v[144:147]\n"
    "v_mfma_f32_16x16x32_bf16 v[160:163], v[64:67], v[242:245], v[160:163]\n"
    "v_mfma_f32_16x16x32_bf16 v[148:151], v[52:55], v[234:237], v[148:151]\n"
    "v_mfma_f32_16x16x32_bf16 v[164:167], v[68:71], v[242:245], v[164:167]\n"
    "v_mfma_f32_16x16x32_bf16 v[152:155], v[56:59], v[234:237], v[152:155]\n"
    "s_waitcnt lgkmcnt(0)\n"
    "v_mfma_f32_16x16x32_bf16 v[144:147], v[60:63], v[238:241], v[144:147]\n"
    "v_cvt_pk_bf16_f32 v156, v156, v157\n"
    "v_mfma_f32_16x16x32_bf16 v[148:151], v[64:67], v[238:241], v[148:151]\n"
    "v_cvt_pk_bf16_f32 v157, v158, v159\n"
    "v_cvt_pk_bf16_f32 v158, v160, v161\n"
    "v_mfma_f32_16x16x32_bf16 v[152:155], v[68:71], v[238:241], v[152:155]\n"
    "v_cvt_pk_bf16_f32 v159, v162, v163\n"
    "ds_write_b128 v225, v[156:159] offset:43008\n"
    "v_cvt_pk_bf16_f32 v160, v164, v165\n"
    "v_cvt_pk_bf16_f32 v161, v166, v167\n"
    "v_cvt_pk_bf16_f32 v144, v144, v145\n"
    "ds_write_b64 v254, v[160:161] offset:43008\n"
    "v_cvt_pk_bf16_f32 v145, v146, v147\n"
    "v_cvt_pk_bf16_f32 v146, v148, v149\n"
    "v_cvt_pk_bf16_f32 v147, v150, v151\n"
    "ds_write_b128 v225, v[144:147] offset:49152\n"
    "v_cvt_pk_bf16_f32 v148, v152, v153\n"
    "v_cvt_pk_bf16_f32 v149, v154, v155\n"
    "ds_write_b64 v254, v[148:149] offset:49152\n"
    "v_mov_b32 v226, 0x27ec0\n"
    "v_mov_b32 v227, 1\n"
    "s_waitcnt vmcnt(0)\n"
    "s_mov_b64 exec, 1\n"
    "ds_add_rtn_u32 v225, v226, v227 offset:4\n"
    "s_mov_b64 exec, -1\n"
    "s_waitcnt lgkmcnt(0)\n"
    "v_readfirstlane_b32 s50, v225\n"
    "s_nop 0\n"
    "s_cmp_ge_u32 s50, 11\n"
    "s_cbranch_scc1 LSD4_%=\n"
    "LSP4_%=:\n"
    "ds_read_b32 v225, v226 offset:4\n"
    "s_waitcnt lgkmcnt(0)\n"
    "v_readfirstlane_b32 s50, v225\n"
    "s_nop 0\n"
    "s_cmp_ge_u32 s50, 12\n"
    "s_cbranch_scc0 LSP4_%=\n"
    "LSD4_%=:\n"
    "s_mul_i32 s54, s45, 28\n"
    "s_add_u32 s54, s54, 20\n"
    "s_mul_i32 s54, s54, 0x15000\n"
    "s_lshl_b32 s55, s44, 10\n"
    "s_add_u32 s55, s55, 0x83a0\n"
    "s_mov_b64 exec, s[56:57]\n"
    "s_mov_b32 m0, s55\n"
    "s_nop 0\n"
    "buffer_load_dwordx4 v177, s[20:23], s54 offen nt lds\n"
    "s_add_u32 s54, s54, 0x15000\n"
    "s_add_u32 s55, s55, 3744\n"
    "s_mov_b32 m0, s55\n"
    "s_nop 0\n"
    "buffer_load_dwordx4 v177, s[20:23], s54 offen nt lds\n"
    "s_add_u32 s54, s54, 0x15000\n"
    "s_add_u32 s55, s55, 3744\n"
    "s_mov_b32 m0, s55\n"
    "s_nop 0\n"
    "buffer_load_dwordx4 v177, s[20:23], s54 offen nt lds\n"
    "s_add_u32 s54, s54, 0x15000\n"
    "s_add_u32 s55, s55, 3744\n"
    "s_mov_b32 m0, s55\n"
    "s_nop 0\n"
    "buffer_load_dwordx4 v177, s[20:23], s54 offen nt lds\n"
    "s_add_u32 s54, s54, 0x15000\n"
    "s_add_u32 s55, s55, 3744\n"
    "s_mov_b32 m0, s55\n"
    "s_nop 0\n"
    "buffer_load_dwordx4 v177, s[20:23], s54 offen nt lds\n"
    "s_add_u32 s54, s54, 0x15000\n"
    "s_add_u32 s55, s55, 3744\n"
    "s_mov_b32 m0, s55\n"
    "s_nop 0\n"
    "buffer_load_dwordx4 v177, s[20:23], s54 offen nt lds\n"
    "s_add_u32 s54, s54, 0x15000\n"
    "s_add_u32 s55, s55, 3744\n"
    "s_mov_b32 m0, s55\n"
    "s_nop 0\n"
    "buffer_load_dwordx4 v177, s[20:23], s54 offen nt lds\n"
    "s_add_u32 s54, s54, 0x15000\n"
    "s_add_u32 s55, s55, 3744\n"
    "s_mov_b32 m0, s55\n"
    "s_nop 0\n"
    "buffer_load_dwordx4 v177, s[20:23], s54 offen nt lds\n"
    "s_add_u32 s54, s54, 0x15000\n"
    "s_add_u32 s55, s55, 3744\n"
    "s_mov_b32 m0, s55\n"
    "s_nop 0\n"
    "buffer_load_dwordx4 v177, s[20:23], s54 offen nt lds\n"
    "s_add_u32 s54, s54, 0x15000\n"
    "s_add_u32 s55, s55, 3744\n"
    "s_mov_b64 exec, -1\n"
    "s_mul_i32 s54, s45, 0x24c000\n"
    "s_mul_i32 s47, s46, 2688\n"
    "s_add_u32 s54, s54, s47\n"
    "s_mul_i32 s47, s44, 768\n"
    "s_add_u32 s54, s54, s47\n"
    "s_setprio 2\n"
    "ds_read_b128 v[226:229], v219 offset:3264\n"
    "ds_read_b128 v[230:233], v219 offset:3536\n"
    "v_add_u32 v225, v216, v223\n"
    "ds_read_b128 v[234:237], v225 offset:832\n"
    "ds_read_b128 v[246:249], v225 offset:864\n"
    "v_add_u32 v254, v217, v223\n"
    "ds_read_b128 v[238:241], v254 offset:832\n"
    "ds_read_b128 v[250:253], v254 offset:864\n"
    "v_add_u32 v255, v218, v223\n"
    "ds_read_b128 v[242:245], v255 offset:832\n"
    "s_waitcnt lgkmcnt(4)\n"
    "v_mfma_f32_16x16x32_bf16 v[144:147], v[226:229], v[234:237], 0\n"
    "ds_read_b128 v[234:237], v255 offset:864\n"
    "s_waitcnt lgkmcnt(4)\n"
    "v_mfma_f32_16x16x32_bf16 v[148:151], v[230:233], v[246:249], 0\n"
    "ds_read_b128 v[246:249], v255 offset:896\n"
    "v_add_u32 v255, v218, v223\n"
    "s_waitcnt lgkmcnt(4)\n"
    "v_mfma_f32_16x16x32_bf16 v[156:159], v[226:229], v[238:241], 0\n"
    "ds_read_b128 v[238:241], v225 offset:896\n"
    "s_setprio 0\n"
    "v_subrev_u32 v225, 0x186c0, v220\n"
    "v_and_b32 v225, 0xf0, v225\n"
    "s_setprio 2\n"
    "s_waitcnt lgkmcnt(3)\n"
    "v_mfma_f32_16x16x32_bf16 v[168:171], v[226:229], v[242:245], 0\n"
    "ds_read_b128 v[226:229], v219 offset:3808\n"
    "ds_read_b128 v[242:245], v254 offset:896\n"
    "s_setprio 0\n"
    "v_add_u32 v225, 0x276c0, v225\n"
    "s_setprio 2\n"
    "v_mfma_f32_16x16x32_bf16 v[160:163], v[230:233], v[250:253], 0\n"
    "ds_read_b128 v[250:253], v216 offset:0\n"
    "s_setprio 0\n"
    "v_add_u32 v254, v216, v224\n"
    "s_setprio 2\n"
    "s_waitcnt lgkmcnt(5)\n"
    "v_mfma_f32_16x16x32_bf16 v[172:175], v[230:233], v[234:237], 0\n"
    "ds_read_b128 v[230:233], v219 offset:0\n"
    "ds_read_b128 v[234:237], v217 offset:0\n"
    "s_waitcnt lgkmcnt(4)\n"
    "v_mfma_f32_16x16x32_bf16 v[152:155], v[226:229], v[238:241], 0\n"
    "ds_read_b128 v[238:241], v218 offset:0\n"
    "s_waitcnt lgkmcnt(4)\n"
    "v_mfma_f32_16x16x32_bf16 v[164:167], v[226:229], v[242:245], 0\n"
    "ds_read_b128 v[242:245], v216 offset:32\n"
    "v_mfma_f32_16x16x32_bf16 v[176:179], v[226:229], v[246:249], 0\n"
    "ds_read_b128 v[226:229], v219 offset:272\n"
    "ds_read_b128 v[246:249], v217 offset:32\n"
    "s_waitcnt lgkmcnt(5)\n"
    "v_mfma_f32_16x16x32_bf16 v[144:147], v[230:233], v[250:253], v[144:147]\n"
    "ds_read_b128 v[250:253], v218 offset:32\n"
    "s_waitcnt lgkmcnt(5)\n"
    "v_mfma_f32_16x16x32_bf16 v[156:159], v[230:233], v[234:237], v[156:159]\n"
    "ds_read_b128 v[234:237], v216 offset:64\n"
    "s_waitcnt lgkmcnt(5)\n"
    "v_mfma_f32_16x16x32_bf16 v[168:171], v[230:233], v[238:241], v[168:171]\n"
    "ds_read_b128 v[230:233], v219 offset:544\n"
    "ds_read_b128 v[238:241], v217 offset:64\n"
    "s_waitcnt lgkmcnt(5)\n"
    "v_mfma_f32_16x16x32_bf16 v[148:151], v[226:229], v[242:245], v[148:151]\n"
    "ds_read_b128 v[242:245], v218 offset:64\n"
    "s_waitcnt lgkmcnt(5)\n"
    "v_mfma_f32_16x16x32_bf16 v[160:163], v[226:229], v[246:249], v[160:163]\n"
    "ds_read_b128 v[246:249], v216 offset:3744\n"
    "s_waitcnt lgkmcnt(5)\n"
    "v_mfma_f32_16x16x32_bf16 v[172:175], v[226:229], v[250:253], v[172:175]\n"
    "ds_read_b128 v[226:229], v219 offset:816\n"
    "ds_read_b128 v[250:253], v217 offset:3744\n"
    "s_waitcnt lgkmcnt(5)\n"
    "v_mfma_f32_16x16x32_bf16 v[152:155], v[230:233], v[234:237], v[152:155]\n"
    "ds_read_b128 v[234:237], v218 offset:3744\n"
    "s_waitcnt lgkmcnt(5)\n"
    "v_mfma_f32_16x16x32_bf16 v[164:167], v[230:233], v[238:241], v[164:167]\n"
    "ds_read_b128 v[238:241], v216 offset:3776\n"
    "s_waitcnt lgkmcnt(5)\n"
    "v_mfma_f32_16x16x32_bf16 v[176:179], v[230:233], v[242:245], v[176:179]\n"
    "ds_read_b128 v[230:233], v219 offset:1088\n"
    "ds_read_b128 v[242:245], v217 offset:3776\n"
    "s_waitcnt lgkmcnt(5)\n"
    "v_mfma_f32_16x16x32_bf16 v[144:147], v[226:229], v[246:249], v[144:147]\n"
    "ds_read_b128 v[246:249], v218 offset:3776\n"
    "s_waitcnt lgkmcnt(5)\n"
    "v_mfma_f32_16x16x32_bf16 v[156:159], v[226:229], v[250:253], v[156:159]\n"
    "ds_read_b128 v[250:253], v216 offset:3808\n"
    "s_waitcnt lgkmcnt(5)\n"
    "v_mfma_f32_16x16x32_bf16 v[168:171], v[226:229], v[234:237], v[168:171]\n"
    "ds_read_b128 v[226:229], v219 offset:1360\n"
    "ds_read_b128 v[234:237], v217 offset:3808\n"
    "s_waitcnt lgkmcnt(5)\n"
    "v_mfma_f32_16x16x32_bf16 v[148:151], v[230:233], v[238:241], v[148:151]\n"
    "ds_read_b128 v[238:241], v218 offset:3808\n"
    "s_waitcnt lgkmcnt(5)\n"
    "v_mfma_f32_16x16x32_bf16 v[160:163], v[230:233], v[242:245], v[160:163]\n"
    "ds_read_b128 v[242:245], v216 offset:7488\n"
    "s_waitcnt lgkmcnt(5)\n"
    "v_mfma_f32_16x16x32_bf16 v[172:175], v[230:233], v[246:249], v[172:175]\n"
    "ds_read_b128 v[230:233], v219 offset:1632\n"
    "ds_read_b128 v[246:249], v217 offset:7488\n"
    "s_waitcnt lgkmcnt(5)\n"
    "v_mfma_f32_16x16x32_bf16 v[152:155], v[226:229], v[250:253], v[152:155]\n"
    "ds_read_b128 v[250:253], v218 offset:7488\n"
    "s_waitcnt lgkmcnt(5)\n"
    "v_mfma_f32_16x16x32_bf16 v[164:167], v[226:229], v[234:237], v[164:167]\n"
    "ds_read_b128 v[234:237], v216 offset:7520\n"
    "s_waitcnt lgkmcnt(5)\n"
    "v_mfma_f32_16x16x32_bf16 v[176:179], v[226:229], v[238:241], v[176:179]\n"
    "ds_read_b128 v[226:229], v219 offset:1904\n"
    "ds_read_b128 v[238:241], v217 offset:7520\n"
    "s_waitcnt lgkmcnt(5)\n"
    "v_mfma_f32_16x16x32_bf16 v[144:147], v[230:233], v[242:245], v[144:147]\n"
    "ds_read_b128 v[242:245], v218 offset:7520\n"
    "s_waitcnt lgkmcnt(5)\n"
    "v_mfma_f32_16x16x32_bf16 v[156:159], v[230:233], v[246:249], v[156:159]\n"
    "ds_read_b128 v[246:249], v216 offset:7552\n"
    "s_waitcnt lgkmcnt(5)\n"
    "v_mfma_f32_16x16x32_bf16 v[168:171], v[230:233], v[250:253], v[168:171]\n"
    "ds_read_b128 v[230:233], v219 offset:2176\n"
    "ds_read_b128 v[250:253], v217 offset:7552\n"
    "s_waitcnt lgkmcnt(5)\n"
    "v_mfma_f32_16x16x32_bf16 v[148:151], v[226:229], v[234:237], v[148:151]\n"
    "ds_read_b128 v[234:237], v218 offset:7552\n"
    "s_waitcnt lgkmcnt(5)\n"
    "v_mfma_f32_16x16x32_bf16 v[160:163], v[226:229], v[238:241], v[160:163]\n"
    "ds_read_b128 v[238:241], v216 offset:4160\n"
    "s_waitcnt lgkmcnt(5)\n"
    "v_mfma_f32_16x16x32_bf16 v[172:175], v[226:229], v[242:245], v[172:175]\n"
    "ds_read_b128 v[226:229], v219 offset:2448\n"
    "ds_read_b128 v[242:245], v217 offset:4160\n"
    "s_waitcnt lgkmcnt(5)\n"
    "v_mfma_f32_16x16x32_bf16 v[152:155], v[230:233], v[246:249], v[152:155]\n"
    "ds_read_b128 v[246:249], v218 offset:4160\n"
    "s_waitcnt lgkmcnt(5)\n"
    "v_mfma_f32_16x16x32_bf16 v[164:167], v[230:233], v[250:253], v[164:167]\n"
    "ds_read_b128 v[250:253], v216 offset:4192\n"
    "s_waitcnt lgkmcnt(5)\n"
    "v_mfma_f32_16x16x32_bf16 v[176:179], v[230:233], v[234:237], v[176:179]\n"
    "ds_read_b128 v[230:233], v219 offset:2720\n"
    "ds_read_b128 v[234:237], v217 offset:4192\n"
    "s_waitcnt lgkmcnt(5)\n"
    "v_mfma_f32_16x16x32_bf16 v[144:147], v[226:229], v[238:241], v[144:147]\n"
    "ds_read_b128 v[238:241], v218 offset:4192\n"
    "s_waitcnt lgkmcnt(5)\n"
    "v_mfma_f32_16x16x32_bf16 v[156:159], v[226:229], v[242:245], v[156:159]\n"
    "ds_read_b128 v[242:245], v216 offset:4224\n"
    "s_waitcnt lgkmcnt(5)\n"
    "v_mfma_f32_16x16x32_bf16 v[168:171], v[226:229], v[246:249], v[168:171]\n"
    "ds_read_b128 v[226:229], v219 offset:2992\n"
    "ds_read_b128 v[246:249], v217 offset:4224\n"
    "s_waitcnt lgkmcnt(5)\n"
    "v_mfma_f32_16x16x32_bf16 v[148:151], v[230:233], v[250:253], v[148:151]\n"
    "ds_read_b128 v[250:253], v218 offset:4224\n"
    "s_waitcnt lgkmcnt(5)\n"
    "v_mfma_f32_16x16x32_bf16 v[160:163], v[230:233], v[234:237], v[160:163]\n"
    "s_waitcnt lgkmcnt(4)\n"
    "v_mfma_f32_16x16x32_bf16 v[172:175], v[230:233], v[238:241], v[172:175]\n"
    "s_setprio 0\n"
    "v_mul_f32 v231, v156, v156\n"
    "v_mul_f32 v233, v157, v157\n"
    "s_setprio 2\n"
    "s_waitcnt lgkmcnt(2)\n"
    "v_mfma_f32_16x16x32_bf16 v[152:155], v[226:229], v[242:245], v[152:155]\n"
    "s_setprio 0\n"
    "v_add_f32 v230, v156, v157\n"
    "v_add_f32 v232, v158, v159\n"
    "s_setprio 2\n"
    "s_waitcnt lgkmcnt(1)\n"
    "v_mfma_f32_16x16x32_bf16 v[164:167], v[226:229], v[246:249], v[164:167]\n"
    "s_setprio 0\n"
    "v_fmac_f32 v231, v158, v158\n"
    "v_fmac_f32 v233, v159, v159\n"
    "s_setprio 2\n"
    "s_waitcnt lgkmcnt(0)\n"
    "v_mfma_f32_16x16x32_bf16 v[176:179], v[226:229], v[250:253], v[176:179]\n"
    "s_setprio 0\n"
    "v_mul_f32 v227, v144, v144\n"
    "v_mul_f32 v229, v145, v145\n"
    "v_add_f32 v226, v144, v145\n"
    "v_add_f32 v228, v146, v147\n"
    "v_fmac_f32 v227, v146, v146\n"
    "v_fmac_f32 v229, v147, v147\n"
    "v_mul_f32 v235, v168, v168\n"
    "v_mul_f32 v237, v169, v169\n"
    "v_add_f32 v226, v226, v148\n"
    "v_add_f32 v228, v228, v149\n"
    "v_fmac_f32 v227, v148, v148\n"
    "v_fmac_f32 v229, v149, v149\n"
    "v_add_f32 v230, v230, v160\n"
    "v_add_f32 v232, v232, v161\n"
    "v_fmac_f32 v231, v160, v160\n"
    "v_fmac_f32 v233, v161, v161\n"
    "v_add_f32 v234, v168, v169\n"
    "v_add_f32 v236, v170, v171\n"
    "v_fmac_f32 v235, v170, v170\n"
    "v_fmac_f32 v237, v171, v171\n"
    "v_add_f32 v226, v226, v150\n"
    "v_add_f32 v228, v228, v151\n"
    "v_fmac_f32 v227, v150, v150\n"
    "v_fmac_f32 v229, v151, v151\n"
    "v_add_f32 v230, v230, v162\n"
    "v_add_f32 v232, v232, v163\n"
    "v_fmac_f32 v231, v162, v162\n"
    "v_fmac_f32 v233, v163, v163\n"
    "v_add_f32 v234, v234, v172\n"
    "v_add_f32 v236, v236, v173\n"
    "v_fmac_f32 v235, v172, v172\n"
    "v_fmac_f32 v237, v173, v173\n"
    "v_add_f32 v226, v226, v152\n"
    "v_add_f32 v228, v228, v153\n"
    "v_fmac_f32 v227, v152, v152\n"
    "v_fmac_f32 v229, v153, v153\n"
    "v_add_f32 v230, v230, v164\n"
    "v_add_f32 v232, v232, v165\n"
    "v_fmac_f32 v231, v164, v164\n"
    "v_fmac_f32 v233, v165, v165\n"
    "v_add_f32 v234, v234, v174\n"
    "v_add_f32 v236, v236, v175\n"
    "v_fmac_f32 v235, v174, v174\n"
    "v_fmac_f32 v237, v175, v175\n"
    "v_add_f32 v226, v226, v154\n"
    "v_add_f32 v228, v228, v155\n"
    "v_fmac_f32 v227, v154, v154\n"
    "v_fmac_f32 v229, v155, v155\n"
    "v_add_f32 v230, v230, v166\n"
    "v_add_f32 v232, v232, v167\n"
    "v_fmac_f32 v231, v166, v166\n"
    "v_fmac_f32 v233, v167, v167\n"
    "v_add_f32 v234, v234, v176\n"
    "v_add_f32 v236, v236, v177\n"
    "v_fmac_f32 v235, v176, v176\n"
    "v_fmac_f32 v237, v177, v177\n"
    "v_add_f32 v226, v226, v228\n"
    "v_add_f32 v227, v227, v229\n"
    "v_add_f32 v230, v230, v232\n"
    "v_add_f32 v231, v231, v233\n"
    "v_add_f32 v234, v234, v178\n"
    "v_add_f32 v236, v236, v179\n"
    "v_fmac_f32 v235, v178, v178\n"
    "v_fmac_f32 v237, v179, v179\n"
    "v_add_f32 v234, v234, v236\n"
    "v_add_f32 v235, v235, v237\n"
    "v_permlane32_swap_b32 v226, v227\n"
    "v_permlane32_swap_b32 v230, v231\n"
    "v_permlane32_swap_b32 v234, v235\n"
    "v_add_f32 v226, v226, v227\n"
    "v_add_f32 v230, v230, v231\n"
    "v_add_f32 v234, v234, v235\n"
    "v_lshlrev_b32 v236, 16, v181\n"
    "v_permlane16_swap_b32 v226, v230\n"
    "v_permlane16_swap_b32 v234, v235\n"
    "v_add_f32 v226, v226, v230\n"
    "ds_write_b32 v221, v226 offset:0\n"
    "v_add_f32 v234, v234, v235\n"
    "ds_write_b32 v221, v234 offset:1024\n"
    "v_lshlrev_b32 v234, 16, v180\n"
    "v_and_b32 v235, 0xffff0000, v180\n"
    "v_and_b32 v237, 0xffff0000, v181\n"
    "v_cvt_pk_bf16_f32 v180, v144, v145\n"
    "v_cvt_pk_bf16_f32 v181, v146, v147\n"
    "v_add_u32 v232, s62, v220\n"
    "v_add_u32 v233, s63, v220\n"
    "s_waitcnt lgkmcnt(0)\n"
    "s_barrier\n"
    "ds_read_b128 v[238:241], v225 offset:0\n"
    "ds_read_b128 v[242:245], v225 offset:256\n"
    "ds_read_b128 v[246:249], v225 offset:512\n"
    "ds_read_b128 v[250:253], v225 offset:768\n"
    "s_waitcnt lgkmcnt(3)\n"
    "v_add_f32 v238, v238, v239\n"
    "v_add_f32 v240, v240, v241\n"
    "v_add_f32 v238, v238, v240\n"
    "s_waitcnt lgkmcnt(2)\n"
    "v_add_f32 v242, v242, v243\n"
    "v_add_f32 v244, v244, v245\n"
    "v_mul_f32 v238, s36, v238\n"
    "v_add_f32 v242, v242, v244\n"
    "v_fma_f32 v239, v238, v238, s37\n"
    "v_fma_f32 v242, v242, s36, -v239\n"
    "v_max_f32 v242, 0x3727c5ac, v242\n"
    "v_rsq_f32 v226, v242\n"
    "ds_read_b128 v[242:245], v225 offset:1280\n"
    "v_mul_f32_e64 v227, -v238, v226\n"
    "ds_read_b128 v[238:241], v225 offset:1024\n"
    "s_waitcnt lgkmcnt(3)\n"
    "v_add_f32 v246, v246, v247\n"
    "v_add_f32 v248, v248, v249\n"
    "v_add_f32 v246, v246, v248\n"
    "s_waitcnt lgkmcnt(2)\n"
    "v_add_f32 v250, v250, v251\n"
    "v_add_f32 v252, v252, v253\n"
    "v_mul_f32 v246, s36, v246\n"
    "v_add_f32 v250, v250, v252\n"
    "v_fma_f32 v247, v246, v246, s37\n"
    "v_fma_f32 v250, v250, s36, -v247\n"
    "v_max_f32 v250, 0x3727c5ac, v250\n"
    "v_rsq_f32 v228, v250\n"
    "ds_read_b128 v[250:253], v220 offset:19456\n"
    "s_waitcnt lgkmcnt(1)\n"
    "v_add_f32 v238, v238, v239\n"
    "v_add_f32 v240, v240, v241\n"
    "v_add_f32 v238, v238, v240\n"
    "v_add_f32 v242, v242, v243\n"
    "v_add_f32 v244, v244, v245\n"
    "v_mul_f32 v238, s36, v238\n"
    "v_mul_f32_e64 v229, -v246, v228\n"
    "ds_read_b128 v[246:249], v220 offset:18432\n"
    "v_add_f32 v242, v242, v244\n"
    "v_fma_f32 v239, v238, v238, s37\n"
    "v_fma_f32 v242, v242, s36, -v239\n"
    "v_max_f32 v242, 0x3727c5ac, v242\n"
    "v_rsq_f32 v230, v242\n"
    "v_and_b32 v239, 0xffff0000, v182\n"
    "v_mul_f32_e64 v231, -v238, v230\n"
    "v_lshlrev_b32 v238, 16, v182\n"
    "v_lshlrev_b32 v240, 16, v183\n"
    "v_and_b32 v241, 0xffff0000, v183\n"
    "s_waitcnt lgkmcnt(0)\n"
    "v_mfma_f32_16x16x32_bf16 v[234:237], v[72:75], v[246:249], v[234:237]\n"
    "v_lshlrev_b32 v242, 16, v184\n"
    "v_and_b32 v243, 0xffff0000, v184\n"
    "v_mfma_f32_16x16x32_bf16 v[238:241], v[76:79], v[246:249], v[238:241]\n"
    "v_lshlrev_b32 v244, 16, v185\n"
    "v_and_b32 v245, 0xffff0000, v185\n"
    "v_mfma_f32_16x16x32_bf16 v[234:237], v[84:87], v[250:253], v[234:237]\n"
    "v_fma_f32 v144, v144, v226, v227\n"
    "v_fma_f32 v145, v145, v226, v227\n"
    "v_mfma_f32_16x16x32_bf16 v[242:245], v[80:83], v[246:249], v[242:245]\n"
    "ds_read_b128 v[246:249], v220 offset:20480\n"
    "v_cvt_pk_bf16_f32 v182, v148, v149\n"
    "v_cvt_pk_bf16_f32 v183, v150, v151\n"
    "v_mfma_f32_16x16x32_bf16 v[238:241], v[88:91], v[250:253], v[238:241]\n"
    "v_fma_f32 v146, v146, v226, v227\n"
    "v_fma_f32 v147, v147, v226, v227\n"
    "v_mfma_f32_16x16x32_bf16 v[242:245], v[92:95], v[250:253], v[242:245]\n"
    "ds_read_b128 v[250:253], v220 offset:21504\n"
    "v_fma_f32 v148, v148, v226, v227\n"
    "v_fma_f32 v149, v149, v226, v227\n"
    "v_fma_f32 v150, v150, v226, v227\n"
    "v_fma_f32 v151, v151, v226, v227\n"
    "v_cvt_pk_bf16_f32 v144, v144, v145\n"
    "s_waitcnt lgkmcnt(1)\n"
    "v_mfma_f32_16x16x32_bf16 v[234:237], v[96:99], v[246:249], v[234:237]\n"
    "v_cvt_pk_bf16_f32 v145, v146, v147\n"
    "v_cvt_pk_bf16_f32 v184, v152, v153\n"
    "v_mfma_f32_16x16x32_bf16 v[238:241], v[100:103], v[246:249], v[238:241]\n"
    "v_cvt_pk_bf16_f32 v185, v154, v155\n"
    "v_cvt_pk_bf16_f32 v146, v148, v149\n"
    "v_mfma_f32_16x16x32_bf16 v[242:245], v[104:107], v[246:249], v[242:245]\n"
    "ds_read_b128 v[246:249], v220 offset:22528\n"
    "v_cvt_pk_bf16_f32 v147, v150, v151\n"
    "v_fma_f32 v152, v152, v226, v227\n"
    "s_waitcnt lgkmcnt(1)\n"
    "v_mfma_f32_16x16x32_bf16 v[234:237], v[108:111], v[250:253], v[234:237]\n"
    "ds_write_b128 v232, v[144:147] offset:0\n"
    "v_mfma_f32_16x16x32_bf16 v[238:241], v[112:115], v[250:253], v[238:241]\n"
    "v_fma_f32 v153, v153, v226, v227\n"
    "v_fma_f32 v154, v154, v226, v227\n"
    "v_mfma_f32_16x16x32_bf16 v[242:245], v[116:119], v[250:253], v[242:245]\n"
    "ds_read_b128 v[250:253], v220 offset:23552\n"
    "v_fma_f32 v155, v155, v226, v227\n"
    "v_cvt_pk_bf16_f32 v148, v152, v153\n"
    "s_waitcnt lgkmcnt(2)\n"
    "v_mfma_f32_16x16x32_bf16 v[234:237], v[120:123], v[246:249], v[234:237]\n"
    "v_cvt_pk_bf16_f32 v149, v154, v155\n"
    "ds_write_b64 v233, v[148:149] offset:0\n"
    "v_mfma_f32_16x16x32_bf16 v[238:241], v[124:127], v[246:249], v[238:241]\n"
    "v_lshlrev_b32 v144, 16, v186\n"
    "v_and_b32 v145, 0xffff0000, v186\n"
    "v_mfma_f32_16x16x32_bf16 v[242:245], v[128:131], v[246:249], v[242:245]\n"
    "ds_read_b128 v[246:249], v220 offset:24576\n"
    "v_lshlrev_b32 v146, 16, v187\n"
    "v_and_b32 v147, 0xffff0000, v187\n"
    "s_waitcnt lgkmcnt(2)\n"
    "v_mfma_f32_16x16x32_bf16 v[234:237], v[132:135], v[250:253], v[234:237]\n"
    "v_lshlrev_b32 v148, 16, v188\n"
    "v_and_b32 v149, 0xffff0000, v188\n"
    "v_mfma_f32_16x16x32_bf16 v[238:241], v[136:139], v[250:253], v[238:241]\n"
    "v_lshlrev_b32 v150, 16, v189\n"
    "v_and_b32 v151, 0xffff0000, v189\n"
    "v_mfma_f32_16x16x32_bf16 v[242:245], v[140:143], v[250:253], v[242:245]\n"
    "ds_read_b128 v[250:253], v220 offset:25600\n"
    "v_lshlrev_b32 v152, 16, v190\n"
    "v_and_b32 v153, 0xffff0000, v190\n"
    "s_waitcnt lgkmcnt(1)\n"
    "v_mfma_f32_16x16x32_bf16 v[144:147], v[72:75], v[246:249], v[144:147]\n"
    "v_lshlrev_b32 v154, 16, v191\n"
    "v_and_b32 v155, 0xffff0000, v191\n"
    "v_mfma_f32_16x16x32_bf16 v[148:151], v[76:79], v[246:249], v[148:151]\n"
    "v_cvt_pk_bf16_f32 v186, v156, v157\n"
    "v_cvt_pk_bf16_f32 v234, v234, v235\n"
    "v_mfma_f32_16x16x32_bf16 v[152:155], v[80:83], v[246:249], v[152:155]\n"
    "ds_read_b128 v[246:249], v220 offset:26624\n"
    "v_cvt_pk_bf16_f32 v187, v158, v159\n"
    "v_fma_f32 v156, v156, v228, v229\n"
    "s_waitcnt lgkmcnt(1)\n"
    "v_mfma_f32_16x16x32_bf16 v[144:147], v[84:87], v[250:253], v[144:147]\n"
    "v_fma_f32 v157, v157, v228, v229\n"
    "v_cvt_pk_bf16_f32 v235, v236, v237\n"
    "v_mfma_f32_16x16x32_bf16 v[148:151], v[88:91], v[250:253], v[148:151]\n"
    "ds_write_b64 v254, v[234:235] offset:0\n"
    "v_mfma_f32_16x16x32_bf16 v[152:155], v[92:95], v[250:253], v[152:155]\n"
    "ds_read_b128 v[250:253], v220 offset:27648\n"
    "v_cvt_pk_bf16_f32 v188, v160, v161\n"
    "v_cvt_pk_bf16_f32 v189, v162, v163\n"
    "s_waitcnt lgkmcnt(2)\n"
    "v_mfma_f32_16x16x32_bf16 v[144:147], v[96:99], v[246:249], v[144:147]\n"
    "v_fma_f32 v158, v158, v228, v229\n"
    "v_fma_f32 v159, v159, v228, v229\n"
    "v_mfma_f32_16x16x32_bf16 v[148:151], v[100:103], v[246:249], v[148:151]\n"
    "v_fma_f32 v160, v160, v228, v229\n"
    "v_fma_f32 v161, v161, v228, v229\n"
    "v_mfma_f32_16x16x32_bf16 v[152:155], v[104:107], v[246:249], v[152:155]\n"
    "ds_read_b128 v[246:249], v220 offset:28672\n"
    "v_fma_f32 v162, v162, v228, v229\n"
    "v_fma_f32 v163, v163, v228, v229\n"
    "s_waitcnt lgkmcnt(1)\n"
    "v_mfma_f32_16x16x32_bf16 v[144:147], v[108:111], v[250:253], v[144:147]\n"
    "v_cvt_pk_bf16_f32 v156, v156, v157\n"
    "v_cvt_pk_bf16_f32 v157, v158, v159\n"
    "v_mfma_f32_16x16x32_bf16 v[148:151], v[112:115], v[250:253], v[148:151]\n"
    "v_cvt_pk_bf16_f32 v236, v238, v239\n"
    "v_cvt_pk_bf16_f32 v237, v240, v241\n"
    "v_mfma_f32_16x16x32_bf16 v[152:155], v[116:119], v[250:253], v[152:155]\n"
    "ds_read_b128 v[250:253], v220 offset:29696\n"
    "v_cvt_pk_bf16_f32 v158, v160, v161\n"
    "v_cvt_pk_bf16_f32 v159, v162, v163\n"
    "s_waitcnt lgkmcnt(1)\n"
    "v_mfma_f32_16x16x32_bf16 v[144:147], v[120:123], v[246:249], v[144:147]\n"
    "ds_write_b64 v254, v[236:237] offset:32\n"
    "v_mfma_f32_16x16x32_bf16 v[148:151], v[124:127], v[246:249], v[148:151]\n"
    "ds_write_b128 v232, v[156:159] offset:6144\n"
    "v_mfma_f32_16x16x32_bf16 v[152:155], v[128:131], v[246:249], v[152:155]\n"
    "ds_read_b128 v[246:249], v220 offset:30720\n"
    "ds_read_b128 v[156:159], v220 offset:32768\n"
    "v_cvt_pk_bf16_f32 v190, v164, v165\n"
    "s_waitcnt lgkmcnt(4)\n"
    "v_mfma_f32_16x16x32_bf16 v[144:147], v[132:135], v[250:253], v[144:147]\n"
    "v_cvt_pk_bf16_f32 v191, v166, v167\n"
    "v_cvt_pk_bf16_f32 v238, v242, v243\n"
    "v_mfma_f32_16x16x32_bf16 v[148:151], v[136:139], v[250:253], v[148:151]\n"
    "v_cvt_pk_bf16_f32 v239, v244, v245\n"
    "v_fma_f32 v164, v164, v228, v229\n"
    "v_mfma_f32_16x16x32_bf16 v[152:155], v[140:143], v[250:253], v[152:155]\n"
    "ds_read_b128 v[250:253], v220 offset:31744\n"
    "v_fma_f32 v165, v165, v228, v229\n"
    "v_fma_f32 v166, v166, v228, v229\n"
    "v_fma_f32 v167, v167, v228, v229\n"
    "v_lshlrev_b32 v234, 16, v192\n"
    "v_and_b32 v235, 0xffff0000, v192\n"
    "v_lshlrev_b32 v236, 16, v193\n"
    "v_and_b32 v237, 0xffff0000, v193\n"
    "ds_write_b64 v254, v[238:239] offset:64\n"
    "v_cvt_pk_bf16_f32 v160, v164, v165\n"
    "s_waitcnt lgkmcnt(3)\n"
    "v_mfma_f32_16x16x32_bf16 v[234:237], v[72:75], v[246:249], v[234:237]\n"
    "v_cvt_pk_bf16_f32 v161, v166, v167\n"
    "ds_read_b128 v[164:167], v220 offset:34816\n"
    "s_waitcnt lgkmcnt(2)\n"
    "v_mfma_f32_16x16x32_bf16 v[234:237], v[84:87], v[250:253], v[234:237]\n"
    "ds_write_b64 v233, v[160:161] offset:6144\n"
    "v_mfma_f32_16x16x32_bf16 v[234:237], v[96:99], v[156:159], v[234:237]\n"
    "ds_read_b128 v[160:163], v220 offset:33792\n"
    "v_lshlrev_b32 v238, 16, v194\n"
    "v_and_b32 v239, 0xffff0000, v194\n"
    "v_lshlrev_b32 v240, 16, v195\n"
    "v_and_b32 v241, 0xffff0000, v195\n"
    "v_lshlrev_b32 v242, 16, v196\n"
    "v_and_b32 v243, 0xffff0000, v196\n"
    "v_mfma_f32_16x16x32_bf16 v[238:241], v[76:79], v[246:249], v[238:241]\n"
    "v_lshlrev_b32 v244, 16, v197\n"
    "v_and_b32 v245, 0xffff0000, v197\n"
    "v_mfma_f32_16x16x32_bf16 v[238:241], v[88:91], v[250:253], v[238:241]\n"
    "v_cvt_pk_bf16_f32 v144, v144, v145\n"
    "v_add_u32 v254, v217, v224\n"
    "v_mfma_f32_16x16x32_bf16 v[242:245], v[80:83], v[246:249], v[242:245]\n"
    "ds_read_b128 v[246:249], v220 offset:35840\n"
    "v_cvt_pk_bf16_f32 v145, v146, v147\n"
    "ds_write_b64 v254, v[144:145] offset:0\n"
    "v_mfma_f32_16x16x32_bf16 v[242:245], v[92:95], v[250:253], v[242:245]\n"
    "v_cvt_pk_bf16_f32 v146, v148, v149\n"
    "v_cvt_pk_bf16_f32 v147, v150, v151\n"
    "v_mfma_f32_16x16x32_bf16 v[238:241], v[100:103], v[156:159], v[238:241]\n"
    "ds_write_b64 v254, v[146:147] offset:32\n"
    "s_waitcnt lgkmcnt(3)\n"
    "v_mfma_f32_16x16x32_bf16 v[234:237], v[108:111], v[160:163], v[234:237]\n"
    "v_cvt_pk_bf16_f32 v148, v152, v153\n"
    "v_cvt_pk_bf16_f32 v149, v154, v155\n"
    "v_mfma_f32_16x16x32_bf16 v[242:245], v[104:107], v[156:159], v[242:245]\n"
    "ds_write_b64 v254, v[148:149] offset:64\n"
    "v_mfma_f32_16x16x32_bf16 v[238:241], v[112:115], v[160:163], v[238:241]\n"
    "v_cvt_pk_bf16_f32 v192, v168, v169\n"
    "v_cvt_pk_bf16_f32 v193, v170, v171\n"
    "v_mfma_f32_16x16x32_bf16 v[234:237], v[120:123], v[164:167], v[234:237]\n"
    "v_fma_f32 v168, v168, v230, v231\n"
    "v_fma_f32 v169, v169, v230, v231\n"
    "v_mfma_f32_16x16x32_bf16 v[242:245], v[116:119], v[160:163], v[242:245]\n"
    "v_cvt_pk_bf16_f32 v194, v172, v173\n"
    "v_cvt_pk_bf16_f32 v195, v174, v175\n"
    "v_mfma_f32_16x16x32_bf16 v[238:241], v[124:127], v[164:167], v[238:241]\n"
    "v_fma_f32 v170, v170, v230, v231\n"
    "v_fma_f32 v171, v171, v230, v231\n"
    "s_waitcnt lgkmcnt(3)\n"
    "v_mfma_f32_16x16x32_bf16 v[234:237], v[132:135], v[246:249], v[234:237]\n"
    "v_add_u32 v254, v218, v224\n"
    "v_fma_f32 v172, v172, v230, v231\n"
    "v_mfma_f32_16x16x32_bf16 v[242:245], v[128:131], v[164:167], v[242:245]\n"
    "v_fma_f32 v173, v173, v230, v231\n"
    "v_fma_f32 v174, v174, v230, v231\n"
    "v_mfma_f32_16x16x32_bf16 v[238:241], v[136:139], v[246:249], v[238:241]\n"
    "v_fma_f32 v175, v175, v230, v231\n"
    "v_cvt_pk_bf16_f32 v168, v168, v169\n"
    "v_mfma_f32_16x16x32_bf16 v[242:245], v[140:143], v[246:249], v[242:245]\n"
    "v_cvt_pk_bf16_f32 v234, v234, v235\n"
    "v_cvt_pk_bf16_f32 v235, v236, v237\n"
    "v_cvt_pk_bf16_f32 v169, v170, v171\n"
    "ds_write_b64 v254, v[234:235] offset:0\n"
    "v_cvt_pk_bf16_f32 v196, v176, v177\n"
    "v_cvt_pk_bf16_f32 v197, v178, v179\n"
    "v_cvt_pk_bf16_f32 v170, v172, v173\n"
    "v_cvt_pk_bf16_f32 v171, v174, v175\n"
    "v_cvt_pk_bf16_f32 v236, v238, v239\n"
    "v_cvt_pk_bf16_f32 v237, v240, v241\n"
    "v_fma_f32 v176, v176, v230, v231\n"
    "v_fma_f32 v177, v177, v230, v231\n"
    "v_fma_f32 v178, v178, v230, v231\n"
    "v_fma_f32 v179, v179, v230, v231\n"
    "ds_write_b128 v232, v[168:171] offset:12288\n"
    "ds_write_b64 v254, v[236:237] offset:32\n"
    "v_cvt_pk_bf16_f32 v172, v176, v177\n"
    "v_cvt_pk_bf16_f32 v173, v178, v179\n"
    "v_cvt_pk_bf16_f32 v238, v242, v243\n"
    "v_cvt_pk_bf16_f32 v239, v244, v245\n"
    "ds_write_b64 v233, v[172:173] offset:12288\n"
    "ds_write_b64 v254, v[238:239] offset:64\n"
    "s_setprio 1\n"
    "v_add_u32 v225, s62, v220\n"
    "v_add_u32 v254, s63, v220\n"
    "s_waitcnt vmcnt(0) lgkmcnt(0)\n"
    "s_barrier\n"
    "ds_read_b128 v[226:229], v220 offset:0\n"
    "ds_read_b128 v[230:233], v220 offset:1024\n"
    "ds_read_b128 v[168:171], v222 offset:0\n"
    "ds_read_b128 v[234:237], v220 offset:2048\n"
    "ds_read_b128 v[238:241], v220 offset:3072\n"
    "ds_read_b128 v[172:175], v222 offset:3744\n"
    "ds_read_b128 v[176:179], v222 offset:7488\n"
    "ds_read_b128 v[242:245], v222 offset:11232\n"
    "ds_read_b128 v[246:249], v222 offset:14976\n"
    "ds_read_b128 v[250:253], v222 offset:18720\n"
    "s_waitcnt lgkmcnt(9)\n"
    "v_mfma_f32_16x16x32_bf16 v[144:147], v[0:3], v[226:229], 0\n"
    "s_waitcnt lgkmcnt(7)\n"
    "s_mov_b64 exec, s[24:25]\n"
    "buffer_store_dwordx4 v[168:171], v220, s[28:31], s54 offen sc0 sc1 nt\n"
    "s_mov_b64 exec, -1\n"
    "v_mfma_f32_16x16x32_bf16 v[148:151], v[4:7], v[226:229], 0\n"
    "s_add_u32 s54, s54, 0x15000\n"
    "ds_read_b128 v[168:171], v222 offset:22464\n"
    "v_mfma_f32_16x16x32_bf16 v[152:155], v[8:11], v[226:229], 0\n"
    "ds_read_b128 v[226:229], v220 offset:4096\n"
    "v_mfma_f32_16x16x32_bf16 v[144:147], v[12:15], v[230:233], v[144:147]\n"
    "v_mfma_f32_16x16x32_bf16 v[148:151], v[16:19], v[230:233], v[148:151]\n"
    "s_waitcnt lgkmcnt(6)\n"
    "s_mov_b64 exec, s[24:25]\n"
    "buffer_store_dwordx4 v[172:175], v220, s[28:31], s54 offen sc0 sc1 nt\n"
    "s_mov_b64 exec, -1\n"
    "v_mfma_f32_16x16x32_bf16 v[152:155], v[20:23], v[230:233], v[152:155]\n"
    "ds_read_b128 v[230:233], v220 offset:5120\n"
    "s_add_u32 s54, s54, 0x15000\n"
    "v_mfma_f32_16x16x32_bf16 v[144:147], v[24:27], v[234:237], v[144:147]\n"
    "v_mfma_f32_16x16x32_bf16 v[148:151], v[28:31], v[234:237], v[148:151]\n"
    "v_mfma_f32_16x16x32_bf16 v[152:155], v[32:35], v[234:237], v[152:155]\n"
    "ds_read_b128 v[234:237], v220 offset:6144\n"
    "s_waitcnt lgkmcnt(7)\n"
    "s_mov_b64 exec, s[24:25]\n"
    "buffer_store_dwordx4 v[176:179], v220, s[28:31], s54 offen sc0 sc1 nt\n"
    "s_mov_b64 exec, -1\n"
    "v_mfma_f32_16x16x32_bf16 v[144:147], v[36:39], v[238:241], v[144:147]\n"
    "s_add_u32 s54, s54, 0x15000\n"
    "v_mfma_f32_16x16x32_bf16 v[148:151], v[40:43], v[238:241], v[148:151]\n"
    "v_mfma_f32_16x16x32_bf16 v[152:155], v[44:47], v[238:241], v[152:155]\n"
    "ds_read_b128 v[238:241], v220 offset:7168\n"
    "s_waitcnt lgkmcnt(3)\n"
    "v_mfma_f32_16x16x32_bf16 v[144:147], v[48:51], v[226:229], v[144:147]\n"
    "s_mov_b64 exec, s[24:25]\n"
    "buffer_store_dwordx4 v[242:245], v220, s[28:31], s54 offen sc0 sc1 nt\n"
    "s_mov_b64 exec, -1\n"
    "v_mfma_f32_16x16x32_bf16 v[148:151], v[52:55], v[226:229], v[148:151]\n"
    "s_add_u32 s54, s54, 0x15000\n"
    "s_setprio 2\n"
    "ds_read_b128 v[242:245], v255 offset:34528\n"
    "s_setprio 1\n"
    "v_mfma_f32_16x16x32_bf16 v[152:155], v[56:59], v[226:229], v[152:155]\n"
    "ds_read_b128 v[226:229], v220 offset:8192\n"
    "s_waitcnt lgkmcnt(3)\n"
    "v_mfma_f32_16x16x32_bf16 v[156:159], v[0:3], v[234:237], 0\n"
    "v_mfma_f32_16x16x32_bf16 v[160:163], v[4:7], v[234:237], 0\n"
    "s_mov_b64 exec, s[24:25]\n"
    "buffer_store_dwordx4 v[246:249], v220, s[28:31], s54 offen sc0 sc1 nt\n"
    "s_mov_b64 exec, -1\n"
    "v_mfma_f32_16x16x32_bf16 v[164:167], v[8:11], v[234:237], 0\n"
    "ds_read_b128 v[234:237], v220 offset:10240\n"
    "s_add_u32 s54, s54, 0x15000\n"
    "v_mfma_f32_16x16x32_bf16 v[144:147], v[60:63], v[230:233], v[144:147]\n"
    "v_mfma_f32_16x16x32_bf16 v[148:151], v[64:67], v[230:233], v[148:151]\n"
    "v_mfma_f32_16x16x32_bf16 v[152:155], v[68:71], v[230:233], v[152:155]\n"
    "ds_read_b128 v[230:233], v220 offset:9216\n"
    "s_mov_b64 exec, s[24:25]\n"
    "buffer_store_dwordx4 v[250:253], v220, s[28:31], s54 offen sc0 sc1 nt\n"
    "s_mov_b64 exec, -1\n"
    "s_waitcnt lgkmcnt(4)\n"
    "v_mfma_f32_16x16x32_bf16 v[156:159], v[12:15], v[238:241], v[156:159]\n"
    "s_add_u32 s54, s54, 0x15000\n"
    "v_mfma_f32_16x16x32_bf16 v[160:163], v[16:19], v[238:241], v[160:163]\n"
    "v_cvt_pk_bf16_f32 v144, v144, v145\n"
    "v_mfma_f32_16x16x32_bf16 v[164:167], v[20:23], v[238:241], v[164:167]\n"
    "ds_read_b128 v[238:241], v220 offset:11264\n"
    "v_cvt_pk_bf16_f32 v145, v146, v147\n"
    "v_cvt_pk_bf16_f32 v146, v148, v149\n"
    "s_waitcnt lgkmcnt(3)\n"
    "v_mfma_f32_16x16x32_bf16 v[156:159], v[24:27], v[226:229], v[156:159]\n"
    "v_cvt_pk_bf16_f32 v147, v150, v151\n"
    "ds_write_b128 v225, v[144:147] offset:18432\n"
    "v_mfma_f32_16x16x32_bf16 v[160:163], v[28:31], v[226:229], v[160:163]\n"
    "s_mov_b64 exec, s[26:27]\n"
    "buffer_store_dwordx4 v[168:171], v220, s[28:31], s54 offen sc0 sc1 nt\n"
    "s_mov_b64 exec, -1\n"
    "v_cvt_pk_bf16_f32 v148, v152, v153\n"
    "v_mfma_f32_16x16x32_bf16 v[164:167], v[32:35], v[226:229], v[164:167]\n"
    "ds_read_b128 v[226:229], v220 offset:12288\n"
    "v_cvt_pk_bf16_f32 v149, v154, v155\n"
    "ds_write_b64 v254, v[148:149] offset:18432\n"
    "s_waitcnt lgkmcnt(4)\n"
    "v_mfma_f32_16x16x32_bf16 v[156:159], v[36:39], v[230:233], v[156:159]\n"
    "s_add_u32 s54, s54, 0x15000\n"
    "v_mfma_f32_16x16x32_bf16 v[160:163], v[40:43], v[230:233], v[160:163]\n"
    "v_mfma_f32_16x16x32_bf16 v[164:167], v[44:47], v[230:233], v[164:167]\n"
    "ds_read_b128 v[230:233], v220 offset:13312\n"
    "v_mfma_f32_16x16x32_bf16 v[156:159], v[48:51], v[234:237], v[156:159]\n"
    "v_mfma_f32_16x16x32_bf16 v[160:163], v[52:55], v[234:237], v[160:163]\n"
    "v_mfma_f32_16x16x32_bf16 v[164:167], v[56:59], v[234:237], v[164:167]\n"
    "ds_read_b128 v[234:237], v220 offset:14336\n"
    "s_waitcnt lgkmcnt(3)\n"
    "v_mfma_f32_16x16x32_bf16 v[144:147], v[0:3], v[226:229], 0\n"
    "v_mfma_f32_16x16x32_bf16 v[156:159], v[60:63], v[238:241], v[156:159]\n"
    "v_mfma_f32_16x16x32_bf16 v[160:163], v[64:67], v[238:241], v[160:163]\n"
    "v_mfma_f32_16x16x32_bf16 v[164:167], v[68:71], v[238:241], v[164:167]\n"
    "ds_read_b128 v[238:241], v220 offset:15360\n"
    "v_mfma_f32_16x16x32_bf16 v[148:151], v[4:7], v[226:229], 0\n"
    "v_mfma_f32_16x16x32_bf16 v[152:155], v[8:11], v[226:229], 0\n"
    "ds_read_b128 v[226:229], v220 offset:16384\n"
    "s_waitcnt lgkmcnt(3)\n"
    "v_mfma_f32_16x16x32_bf16 v[144:147], v[12:15], v[230:233], v[144:147]\n"
    "v_cvt_pk_bf16_f32 v156, v156, v157\n"
    "v_mfma_f32_16x16x32_bf16 v[148:151], v[16:19], v[230:233], v[148:151]\n"
    "v_cvt_pk_bf16_f32 v157, v158, v159\n"
    "v_cvt_pk_bf16_f32 v158, v160, v161\n"
    "v_mfma_f32_16x16x32_bf16 v[152:155], v[20:23], v[230:233], v[152:155]\n"
    "ds_read_b128 v[230:233], v220 offset:17408\n"
    "v_cvt_pk_bf16_f32 v159, v162, v163\n"
    "ds_write_b128 v225, v[156:159] offset:24576\n"
    "s_waitcnt lgkmcnt(4)\n"
    "v_mfma_f32_16x16x32_bf16 v[144:147], v[24:27], v[234:237], v[144:147]\n"
    "v_cvt_pk_bf16_f32 v160, v164, v165\n"
    "v_cvt_pk_bf16_f32 v161, v166, v167\n"
    "v_mfma_f32_16x16x32_bf16 v[148:151], v[28:31], v[234:237], v[148:151]\n"
    "ds_write_b64 v254, v[160:161] offset:24576\n"
    "v_mfma_f32_16x16x32_bf16 v[152:155], v[32:35], v[234:237], v[152:155]\n"
    "s_waitcnt lgkmcnt(4)\n"
    "v_mfma_f32_16x16x32_bf16 v[144:147], v[36:39], v[238:241], v[144:147]\n"
    "v_mfma_f32_16x16x32_bf16 v[148:151], v[40:43], v[238:241], v[148:151]\n"
    "v_mfma_f32_16x16x32_bf16 v[152:155], v[44:47], v[238:241], v[152:155]\n"
    "s_waitcnt lgkmcnt(3)\n"
    "v_mfma_f32_16x16x32_bf16 v[144:147], v[48:51], v[226:229], v[144:147]\n"
    "v_mfma_f32_16x16x32_bf16 v[148:151], v[52:55], v[226:229], v[148:151]\n"
    "v_mfma_f32_16x16x32_bf16 v[152:155], v[56:59], v[226:229], v[152:155]\n"
    "s_setprio 2\n"
    "ds_read_b128 v[226:229], v219 offset:3264\n"
    "s_setprio 1\n"
    "s_waitcnt lgkmcnt(3)\n"
    "v_mfma_f32_16x16x32_bf16 v[144:147], v[60:63], v[230:233], v[144:147]\n"
    "v_mfma_f32_16x16x32_bf16 v[148:151], v[64:67], v[230:233], v[148:151]\n"
    "v_mfma_f32_16x16x32_bf16 v[152:155], v[68:71], v[230:233], v[152:155]\n"
    "s_setprio 2\n"
    "ds_read_b128 v[230:233], v219 offset:3536\n"
    "s_waitcnt lgkmcnt(1)\n"
    "v_mfma_f32_16x16x32_bf16 v[168:171], v[226:229], v[242:245], 0\n"
    "s_setprio 1\n"
    "s_nop 0\n"
    "v_cvt_pk_bf16_f32 v144, v144, v145\n"
    "v_cvt_pk_bf16_f32 v145, v146, v147\n"
    "v_cvt_pk_bf16_f32 v146, v148, v149\n"
    "v_cvt_pk_bf16_f32 v147, v150, v151\n"
    "ds_write_b128 v225, v[144:147] offset:30720\n"
    "v_cvt_pk_bf16_f32 v148, v152, v153\n"
    "v_cvt_pk_bf16_f32 v149, v154, v155\n"
    "ds_write_b64 v254, v[148:149] offset:30720\n"
    "s_setprio 2\n"
    "v_add_u32 v225, v216, v223\n"
    "ds_read_b128 v[234:237], v225 offset:34528\n"
    "ds_read_b128 v[246:249], v225 offset:34560\n"
    "v_add_u32 v254, v217, v223\n"
    "ds_read_b128 v[238:241], v254 offset:34528\n"
    "ds_read_b128 v[250:253], v254 offset:34560\n"
    "ds_read_b128 v[242:245], v254 offset:34592\n"
    "s_setprio 0\n"
    "v_add_u32 v254, v216, v224\n"
    "s_setprio 2\n"
    "s_waitcnt lgkmcnt(4)\n"
    "v_mfma_f32_16x16x32_bf16 v[144:147], v[226:229], v[234:237], 0\n"
    "ds_read_b128 v[234:237], v255 offset:34560\n"
    "s_waitcnt lgkmcnt(4)\n"
    "v_mfma_f32_16x16x32_bf16 v[148:151], v[230:233], v[246:249], 0\n"
    "ds_read_b128 v[246:249], v255 offset:34592\n"
    "s_waitcnt lgkmcnt(4)\n"
    "v_mfma_f32_16x16x32_bf16 v[156:159], v[226:229], v[238:241], 0\n"
    "ds_read_b128 v[226:229], v219 offset:3808\n"
    "ds_read_b128 v[238:241], v225 offset:34592\n"
    "s_setprio 0\n"
    "v_subrev_u32 v225, 0x186c0, v220\n"
    "s_setprio 2\n"
    "s_waitcnt lgkmcnt(5)\n"
    "v_mfma_f32_16x16x32_bf16 v[160:163], v[230:233], v[250:253], 0\n"
    "ds_read_b128 v[250:253], v216 offset:33696\n"
    "s_setprio 0\n"
    "v_and_b32 v225, 0xf0, v225\n"
    "v_add_u32 v225, 0x276c0, v225\n"
    "s_setprio 2\n"
    "s_waitcnt lgkmcnt(4)\n"
    "v_mfma_f32_16x16x32_bf16 v[172:175], v[230:233], v[234:237], 0\n"
    "ds_read_b128 v[230:233], v219 offset:0\n"
    "ds_read_b128 v[234:237], v217 offset:33696\n"
    "s_waitcnt lgkmcnt(4)\n"
    "v_mfma_f32_16x16x32_bf16 v[164:167], v[226:229], v[242:245], 0\n"
    "ds_read_b128 v[242:245], v216 offset:33728\n"
    "s_waitcnt lgkmcnt(4)\n"
    "v_mfma_f32_16x16x32_bf16 v[152:155], v[226:229], v[238:241], 0\n"
    "ds_read_b128 v[238:241], v218 offset:33696\n"
    "v_mfma_f32_16x16x32_bf16 v[176:179], v[226:229], v[246:249], 0\n"
    "ds_read_b128 v[226:229], v219 offset:272\n"
    "ds_read_b128 v[246:249], v217 offset:33728\n"
    "s_waitcnt lgkmcnt(5)\n"
    "v_mfma_f32_16x16x32_bf16 v[144:147], v[230:233], v[250:253], v[144:147]\n"
    "ds_read_b128 v[250:253], v218 offset:33728\n"
    "s_waitcnt lgkmcnt(5)\n"
    "v_mfma_f32_16x16x32_bf16 v[156:159], v[230:233], v[234:237], v[156:159]\n"
    "ds_read_b128 v[234:237], v216 offset:33760\n"
    "s_waitcnt lgkmcnt(4)\n"
    "v_mfma_f32_16x16x32_bf16 v[168:171], v[230:233], v[238:241], v[168:171]\n"
    "ds_read_b128 v[230:233], v219 offset:544\n"
    "ds_read_b128 v[238:241], v217 offset:33760\n"
    "s_waitcnt lgkmcnt(5)\n"
    "v_mfma_f32_16x16x32_bf16 v[148:151], v[226:229], v[242:245], v[148:151]\n"
    "ds_read_b128 v[242:245], v218 offset:33760\n"
    "s_waitcnt lgkmcnt(5)\n"
    "v_mfma_f32_16x16x32_bf16 v[160:163], v[226:229], v[246:249], v[160:163]\n"
    "ds_read_b128 v[246:249], v216 offset:37440\n"
    "s_waitcnt lgkmcnt(5)\n"
    "v_mfma_f32_16x16x32_bf16 v[172:175], v[226:229], v[250:253], v[172:175]\n"
    "ds_read_b128 v[226:229], v219 offset:816\n"
    "ds_read_b128 v[250:253], v217 offset:37440\n"
    "s_waitcnt lgkmcnt(5)\n"
    "v_mfma_f32_16x16x32_bf16 v[152:155], v[230:233], v[234:237], v[152:155]\n"
    "ds_read_b128 v[234:237], v218 offset:37440\n"
    "s_waitcnt lgkmcnt(5)\n"
    "v_mfma_f32_16x16x32_bf16 v[164:167], v[230:233], v[238:241], v[164:167]\n"
    "ds_read_b128 v[238:241], v216 offset:37472\n"
    "s_waitcnt lgkmcnt(5)\n"
    "v_mfma_f32_16x16x32_bf16 v[176:179], v[230:233], v[242:245], v[176:179]\n"
    "ds_read_b128 v[230:233], v219 offset:1088\n"
    "ds_read_b128 v[242:245], v217 offset:37472\n"
    "s_waitcnt lgkmcnt(5)\n"
    "v_mfma_f32_16x16x32_bf16 v[144:147], v[226:229], v[246:249], v[144:147]\n"
    "ds_read_b128 v[246:249], v218 offset:37472\n"
    "s_waitcnt lgkmcnt(5)\n"
    "v_mfma_f32_16x16x32_bf16 v[156:159], v[226:229], v[250:253], v[156:159]\n"
    "ds_read_b128 v[250:253], v216 offset:37504\n"
    "s_waitcnt lgkmcnt(5)\n"
    "v_mfma_f32_16x16x32_bf16 v[168:171], v[226:229], v[234:237], v[168:171]\n"
    "ds_read_b128 v[226:229], v219 offset:1360\n"
    "ds_read_b128 v[234:237], v217 offset:37504\n"
    "s_waitcnt lgkmcnt(5)\n"
    "v_mfma_f32_16x16x32_bf16 v[148:151], v[230:233], v[238:241], v[148:151]\n"
    "ds_read_b128 v[238:241], v218 offset:37504\n"
    "s_waitcnt lgkmcnt(5)\n"
    "v_mfma_f32_16x16x32_bf16 v[160:163], v[230:233], v[242:245], v[160:163]\n"
    "ds_read_b128 v[242:245], v216 offset:41184\n"
    "s_waitcnt lgkmcnt(5)\n"
    "v_mfma_f32_16x16x32_bf16 v[172:175], v[230:233], v[246:249], v[172:175]\n"
    "ds_read_b128 v[230:233], v219 offset:1632\n"
    "ds_read_b128 v[246:249], v217 offset:41184\n"
    "s_waitcnt lgkmcnt(5)\n"
    "v_mfma_f32_16x16x32_bf16 v[152:155], v[226:229], v[250:253], v[152:155]\n"
    "ds_read_b128 v[250:253], v218 offset:41184\n"
    "s_waitcnt lgkmcnt(5)\n"
    "v_mfma_f32_16x16x32_bf16 v[164:167], v[226:229], v[234:237], v[164:167]\n"
    "ds_read_b128 v[234:237], v216 offset:41216\n"
    "s_waitcnt lgkmcnt(5)\n"
    "v_mfma_f32_16x16x32_bf16 v[176:179], v[226:229], v[238:241], v[176:179]\n"
    "ds_read_b128 v[226:229], v219 offset:1904\n"
    "ds_read_b128 v[238:241], v217 offset:41216\n"
    "s_waitcnt lgkmcnt(5)\n"
    "v_mfma_f32_16x16x32_bf16 v[144:147], v[230:233], v[242:245], v[144:147]\n"
    "ds_read_b128 v[242:245], v218 offset:41216\n"
    "s_waitcnt lgkmcnt(5)\n"
    "v_mfma_f32_16x16x32_bf16 v[156:159], v[230:233], v[246:249], v[156:159]\n"
    "ds_read_b128 v[246:249], v216 offset:41248\n"
    "s_waitcnt lgkmcnt(5)\n"
    "v_mfma_f32_16x16x32_bf16 v[168:171], v[230:233], v[250:253], v[168:171]\n"
    "ds_read_b128 v[230:233], v219 offset:2176\n"
    "ds_read_b128 v[250:253], v217 offset:41248\n"
    "s_waitcnt lgkmcnt(5)\n"
    "v_mfma_f32_16x16x32_bf16 v[148:151], v[226:229], v[234:237], v[148:151]\n"
    "ds_read_b128 v[234:237], v218 offset:41248\n"
    "s_waitcnt lgkmcnt(5)\n"
    "v_mfma_f32_16x16x32_bf16 v[160:163], v[226:229], v[238:241], v[160:163]\n"
    "ds_read_b128 v[238:241], v216 offset:37856\n"
    "s_waitcnt lgkmcnt(5)\n"
    "v_mfma_f32_16x16x32_bf16 v[172:175], v[226:229], v[242:245], v[172:175]\n"
    "ds_read_b128 v[226:229], v219 offset:2448\n"
    "ds_read_b128 v[242:245], v217 offset:37856\n"
    "s_waitcnt lgkmcnt(5)\n"
    "v_mfma_f32_16x16x32_bf16 v[152:155], v[230:233], v[246:249], v[152:155]\n"
    "ds_read_b128 v[246:249], v218 offset:37856\n"
    "s_waitcnt lgkmcnt(5)\n"
    "v_mfma_f32_16x16x32_bf16 v[164:167], v[230:233], v[250:253], v[164:167]\n"
    "ds_read_b128 v[250:253], v216 offset:37888\n"
    "s_waitcnt lgkmcnt(5)\n"
    "v_mfma_f32_16x16x32_bf16 v[176:179], v[230:233], v[234:237], v[176:179]\n"
    "ds_read_b128 v[230:233], v219 offset:2720\n"
    "ds_read_b128 v[234:237], v217 offset:37888\n"
    "s_waitcnt lgkmcnt(5)\n"
    "v_mfma_f32_16x16x32_bf16 v[144:147], v[226:229], v[238:241], v[144:147]\n"
    "ds_read_b128 v[238:241], v218 offset:37888\n"
    "s_waitcnt lgkmcnt(5)\n"
    "v_mfma_f32_16x16x32_bf16 v[156:159], v[226:229], v[242:245], v[156:159]\n"
    "ds_read_b128 v[242:245], v216 offset:37920\n"
    "s_waitcnt lgkmcnt(5)\n"
    "v_mfma_f32_16x16x32_bf16 v[168:171], v[226:229], v[246:249], v[168:171]\n"
    "ds_read_b128 v[226:229], v219 offset:2992\n"
    "ds_read_b128 v[246:249], v217 offset:37920\n"
    "s_waitcnt lgkmcnt(5)\n"
    "v_mfma_f32_16x16x32_bf16 v[148:151], v[230:233], v[250:253], v[148:151]\n"
    "ds_read_b128 v[250:253], v218 offset:37920\n"
    "s_waitcnt lgkmcnt(5)\n"
    "v_mfma_f32_16x16x32_bf16 v[160:163], v[230:233], v[234:237], v[160:163]\n"
    "s_waitcnt lgkmcnt(4)\n"
    "v_mfma_f32_16x16x32_bf16 v[172:175], v[230:233], v[238:241], v[172:175]\n"
    "s_setprio 0\n"
    "v_mul_f32 v231, v156, v156\n"
    "v_mul_f32 v233, v157, v157\n"
    "s_setprio 2\n"
    "s_waitcnt lgkmcnt(2)\n"
    "v_mfma_f32_16x16x32_bf16 v[152:155], v[226:229], v[242:245], v[152:155]\n"
    "s_setprio 0\n"
    "v_add_f32 v230, v156, v157\n"
    "v_add_f32 v232, v158, v159\n"
    "s_setprio 2\n"
    "s_waitcnt lgkmcnt(1)\n"
    "v_mfma_f32_16x16x32_bf16 v[164:167], v[226:229], v[246:249], v[164:167]\n"
    "s_setprio 0\n"
    "v_fmac_f32 v231, v158, v158\n"
    "v_fmac_f32 v233, v159, v159\n"
    "s_setprio 2\n"
    "s_waitcnt lgkmcnt(0)\n"
    "v_mfma_f32_16x16x32_bf16 v[176:179], v[226:229], v[250:253], v[176:179]\n"
    "s_setprio 0\n"
    "v_mul_f32 v227, v144, v144\n"
    "v_mul_f32 v229, v145, v145\n"
    "v_add_f32 v226, v144, v145\n"
    "v_add_f32 v228, v146, v147\n"
    "v_fmac_f32 v227, v146, v146\n"
    "v_fmac_f32 v229, v147, v147\n"
    "v_mul_f32 v235, v168, v168\n"
    "v_mul_f32 v237, v169, v169\n"
    "v_add_f32 v226, v226, v148\n"
    "v_add_f32 v228, v228, v149\n"
    "v_fmac_f32 v227, v148, v148\n"
    "v_fmac_f32 v229, v149, v149\n"
    "v_add_f32 v230, v230, v160\n"
    "v_add_f32 v232, v232, v161\n"
    "v_fmac_f32 v231, v160, v160\n"
    "v_fmac_f32 v233, v161, v161\n"
    "v_add_f32 v234, v168, v169\n"
    "v_add_f32 v236, v170, v171\n"
    "v_fmac_f32 v235, v170, v170\n"
    "v_fmac_f32 v237, v171, v171\n"
    "v_add_f32 v226, v226, v150\n"
    "v_add_f32 v228, v228, v151\n"
    "v_fmac_f32 v227, v150, v150\n"
    "v_fmac_f32 v229, v151, v151\n"
    "v_add_f32 v230, v230, v162\n"
    "v_add_f32 v232, v232, v163\n"
    "v_fmac_f32 v231, v162, v162\n"
    "v_fmac_f32 v233, v163, v163\n"
    "v_add_f32 v234, v234, v172\n"
    "v_add_f32 v236, v236, v173\n"
    "v_fmac_f32 v235, v172, v172\n"
    "v_fmac_f32 v237, v173, v173\n"
    "v_add_f32 v226, v226, v152\n"
    "v_add_f32 v228, v228, v153\n"
    "v_fmac_f32 v227, v152, v152\n"
    "v_fmac_f32 v229, v153, v153\n"
    "v_add_f32 v230, v230, v164\n"
    "v_add_f32 v232, v232, v165\n"
    "v_fmac_f32 v231, v164, v164\n"
    "v_fmac_f32 v233, v165, v165\n"
    "v_add_f32 v234, v234, v174\n"
    "v_add_f32 v236, v236, v175\n"
    "v_fmac_f32 v235, v174, v174\n"
    "v_fmac_f32 v237, v175, v175\n"
    "v_add_f32 v226, v226, v154\n"
    "v_add_f32 v228, v228, v155\n"
    "v_fmac_f32 v227, v154, v154\n"
    "v_fmac_f32 v229, v155, v155\n"
    "v_add_f32 v230, v230, v166\n"
    "v_add_f32 v232, v232, v167\n"
    "v_fmac_f32 v231, v166, v166\n"
    "v_fmac_f32 v233, v167, v167\n"
    "v_add_f32 v234, v234, v176\n"
    "v_add_f32 v236, v236, v177\n"
    "v_fmac_f32 v235, v176, v176\n"
    "v_fmac_f32 v237, v177, v177\n"
    "v_add_f32 v226, v226, v228\n"
    "v_add_f32 v227, v227, v229\n"
    "v_add_f32 v230, v230, v232\n"
    "v_add_f32 v231, v231, v233\n"
    "v_add_f32 v234, v234, v178\n"
    "v_add_f32 v236, v236, v179\n"
    "v_fmac_f32 v235, v178, v178\n"
    "v_fmac_f32 v237, v179, v179\n"
    "v_add_f32 v234, v234, v236\n"
    "v_add_f32 v235, v235, v237\n"
    "v_permlane32_swap_b32 v226, v227\n"
    "v_permlane32_swap_b32 v230, v231\n"
    "v_permlane32_swap_b32 v234, v235\n"
    "v_add_f32 v226, v226, v227\n"
    "v_add_f32 v230, v230, v231\n"
    "v_add_f32 v234, v234, v235\n"
    "v_lshlrev_b32 v236, 16, v199\n"
    "v_permlane16_swap_b32 v226, v230\n"
    "v_permlane16_swap_b32 v234, v235\n"
    "v_add_f32 v226, v226, v230\n"
    "ds_write_b32 v221, v226 offset:0\n"
    "v_add_f32 v234, v234, v235\n"
    "ds_write_b32 v221, v234 offset:1024\n"
    "v_lshlrev_b32 v234, 16, v198\n"
    "v_and_b32 v235, 0xffff0000, v198\n"
    "v_and_b32 v237, 0xffff0000, v199\n"
    "v_cvt_pk_bf16_f32 v198, v144, v145\n"
    "v_cvt_pk_bf16_f32 v199, v146, v147\n"
    "v_add_u32 v232, s62, v220\n"
    "v_add_u32 v233, s63, v220\n"
    "s_waitcnt lgkmcnt(0)\n"
    "s_barrier\n"
    "ds_read_b128 v[238:241], v225 offset:0\n"
    "ds_read_b128 v[242:245], v225 offset:256\n"
    "ds_read_b128 v[246:249], v225 offset:512\n"
    "ds_read_b128 v[250:253], v225 offset:768\n"
    "s_waitcnt lgkmcnt(3)\n"
    "v_add_f32 v238, v238, v239\n"
    "v_add_f32 v240, v240, v241\n"
    "v_add_f32 v238, v238, v240\n"
    "s_waitcnt lgkmcnt(2)\n"
    "v_add_f32 v242, v242, v243\n"
    "v_add_f32 v244, v244, v245\n"
    "v_mul_f32 v238, s36, v238\n"
    "v_add_f32 v242, v242, v244\n"
    "v_fma_f32 v239, v238, v238, s37\n"
    "v_fma_f32 v242, v242, s36, -v239\n"
    "v_max_f32 v242, 0x3727c5ac, v242\n"
    "v_rsq_f32 v226, v242\n"
    "ds_read_b128 v[242:245], v225 offset:1280\n"
    "v_mul_f32_e64 v227, -v238, v226\n"
    "ds_read_b128 v[238:241], v225 offset:1024\n"
    "s_waitcnt lgkmcnt(3)\n"
    "v_add_f32 v246, v246, v247\n"
    "v_add_f32 v248, v248, v249\n"
    "v_add_f32 v246, v246, v248\n"
    "s_waitcnt lgkmcnt(2)\n"
    "v_add_f32 v250, v250, v251\n"
    "v_add_f32 v252, v252, v253\n"
    "v_mul_f32 v246, s36, v246\n"
    "v_add_f32 v250, v250, v252\n"
    "v_fma_f32 v247, v246, v246, s37\n"
    "v_fma_f32 v250, v250, s36, -v247\n"
    "v_max_f32 v250, 0x3727c5ac, v250\n"
    "v_rsq_f32 v228, v250\n"
    "ds_read_b128 v[250:253], v220 offset:37888\n"
    "s_waitcnt lgkmcnt(1)\n"
    "v_add_f32 v238, v238, v239\n"
    "v_add_f32 v240, v240, v241\n"
    "v_add_f32 v238, v238, v240\n"
    "v_add_f32 v242, v242, v243\n"
    "v_add_f32 v244, v244, v245\n"
    "v_mul_f32 v238, s36, v238\n"
    "v_mul_f32_e64 v229, -v246, v228\n"
    "ds_read_b128 v[246:249], v220 offset:36864\n"
    "v_add_f32 v242, v242, v244\n"
    "v_fma_f32 v239, v238, v238, s37\n"
    "v_fma_f32 v242, v242, s36, -v239\n"
    "v_max_f32 v242, 0x3727c5ac, v242\n"
    "v_rsq_f32 v230, v242\n"
    "v_and_b32 v239, 0xffff0000, v200\n"
    "v_mul_f32_e64 v231, -v238, v230\n"
    "v_lshlrev_b32 v238, 16, v200\n"
    "v_lshlrev_b32 v240, 16, v201\n"
    "v_and_b32 v241, 0xffff0000, v201\n"
    "s_waitcnt lgkmcnt(0)\n"
    "v_mfma_f32_16x16x32_bf16 v[234:237], v[72:75], v[246:249], v[234:237]\n"
    "v_lshlrev_b32 v242, 16, v202\n"
    "v_and_b32 v243, 0xffff0000, v202\n"
    "v_mfma_f32_16x16x32_bf16 v[238:241], v[76:79], v[246:249], v[238:241]\n"
    "v_lshlrev_b32 v244, 16, v203\n"
    "v_and_b32 v245, 0xffff0000, v203\n"
    "v_mfma_f32_16x16x32_bf16 v[234:237], v[84:87], v[250:253], v[234:237]\n"
    "v_fma_f32 v144, v144, v226, v227\n"
    "v_fma_f32 v145, v145, v226, v227\n"
    "v_mfma_f32_16x16x32_bf16 v[242:245], v[80:83], v[246:249], v[242:245]\n"
    "ds_read_b128 v[246:249], v220 offset:38912\n"
    "v_cvt_pk_bf16_f32 v200, v148, v149\n"
    "v_cvt_pk_bf16_f32 v201, v150, v151\n"
    "v_mfma_f32_16x16x32_bf16 v[238:241], v[88:91], v[250:253], v[238:241]\n"
    "v_fma_f32 v146, v146, v226, v227\n"
    "v_fma_f32 v147, v147, v226, v227\n"
    "v_mfma_f32_16x16x32_bf16 v[242:245], v[92:95], v[250:253], v[242:245]\n"
    "ds_read_b128 v[250:253], v220 offset:39936\n"
    "v_fma_f32 v148, v148, v226, v227\n"
    "v_fma_f32 v149, v149, v226, v227\n"
    "v_fma_f32 v150, v150, v226, v227\n"
    "v_fma_f32 v151, v151, v226, v227\n"
    "v_cvt_pk_bf16_f32 v144, v144, v145\n"
    "s_waitcnt lgkmcnt(1)\n"
    "v_mfma_f32_16x16x32_bf16 v[234:237], v[96:99], v[246:249], v[234:237]\n"
    "v_cvt_pk_bf16_f32 v145, v146, v147\n"
    "v_cvt_pk_bf16_f32 v202, v152, v153\n"
    "v_mfma_f32_16x16x32_bf16 v[238:241], v[100:103], v[246:249], v[238:241]\n"
    "v_cvt_pk_bf16_f32 v203, v154, v155\n"
    "v_cvt_pk_bf16_f32 v146, v148, v149\n"
    "v_mfma_f32_16x16x32_bf16 v[242:245], v[104:107], v[246:249], v[242:245]\n"
    "ds_read_b128 v[246:249], v220 offset:40960\n"
    "v_cvt_pk_bf16_f32 v147, v150, v151\n"
    "v_fma_f32 v152, v152, v226, v227\n"
    "s_waitcnt lgkmcnt(1)\n"
    "v_mfma_f32_16x16x32_bf16 v[234:237], v[108:111], v[250:253], v[234:237]\n"
    "ds_write_b128 v232, v[144:147] offset:0\n"
    "v_mfma_f32_16x16x32_bf16 v[238:241], v[112:115], v[250:253], v[238:241]\n"
    "v_fma_f32 v153, v153, v226, v227\n"
    "v_fma_f32 v154, v154, v226, v227\n"
    "v_mfma_f32_16x16x32_bf16 v[242:245], v[116:119], v[250:253], v[242:245]\n"
    "ds_read_b128 v[250:253], v220 offset:41984\n"
    "v_fma_f32 v155, v155, v226, v227\n"
    "v_cvt_pk_bf16_f32 v148, v152, v153\n"
    "s_waitcnt lgkmcnt(2)\n"
    "v_mfma_f32_16x16x32_bf16 v[234:237], v[120:123], v[246:249], v[234:237]\n"
    "v_cvt_pk_bf16_f32 v149, v154, v155\n"
    "ds_write_b64 v233, v[148:149] offset:0\n"
    "v_mfma_f32_16x16x32_bf16 v[238:241], v[124:127], v[246:249], v[238:241]\n"
    "v_lshlrev_b32 v144, 16, v204\n"
    "v_and_b32 v145, 0xffff0000, v204\n"
    "v_mfma_f32_16x16x32_bf16 v[242:245], v[128:131], v[246:249], v[242:245]\n"
    "ds_read_b128 v[246:249], v220 offset:43008\n"
    "v_lshlrev_b32 v146, 16, v205\n"
    "v_and_b32 v147, 0xffff0000, v205\n"
    "s_waitcnt lgkmcnt(2)\n"
    "v_mfma_f32_16x16x32_bf16 v[234:237], v[132:135], v[250:253], v[234:237]\n"
    "v_lshlrev_b32 v148, 16, v206\n"
    "v_and_b32 v149, 0xffff0000, v206\n"
    "v_mfma_f32_16x16x32_bf16 v[238:241], v[136:139], v[250:253], v[238:241]\n"
    "v_lshlrev_b32 v150, 16, v207\n"
    "v_and_b32 v151, 0xffff0000, v207\n"
    "v_mfma_f32_16x16x32_bf16 v[242:245], v[140:143], v[250:253], v[242:245]\n"
    "ds_read_b128 v[250:253], v220 offset:44032\n"
    "v_lshlrev_b32 v152, 16, v208\n"
    "v_and_b32 v153, 0xffff0000, v208\n"
    "s_waitcnt lgkmcnt(1)\n"
    "v_mfma_f32_16x16x32_bf16 v[144:147], v[72:75], v[246:249], v[144:147]\n"
    "v_lshlrev_b32 v154, 16, v209\n"
    "v_and_b32 v155, 0xffff0000, v209\n"
    "v_mfma_f32_16x16x32_bf16 v[148:151], v[76:79], v[246:249], v[148:151]\n"
    "v_cvt_pk_bf16_f32 v204, v156, v157\n"
    "v_cvt_pk_bf16_f32 v234, v234, v235\n"
    "v_mfma_f32_16x16x32_bf16 v[152:155], v[80:83], v[246:249], v[152:155]\n"
    "ds_read_b128 v[246:249], v220 offset:45056\n"
    "v_cvt_pk_bf16_f32 v205, v158, v159\n"
    "v_fma_f32 v156, v156, v228, v229\n"
    "s_waitcnt lgkmcnt(1)\n"
    "v_mfma_f32_16x16x32_bf16 v[144:147], v[84:87], v[250:253], v[144:147]\n"
    "v_fma_f32 v157, v157, v228, v229\n"
    "v_cvt_pk_bf16_f32 v235, v236, v237\n"
    "v_mfma_f32_16x16x32_bf16 v[148:151], v[88:91], v[250:253], v[148:151]\n"
    "ds_write_b64 v254, v[234:235] offset:33696\n"
    "v_mfma_f32_16x16x32_bf16 v[152:155], v[92:95], v[250:253], v[152:155]\n"
    "ds_read_b128 v[250:253], v220 offset:46080\n"
    "v_cvt_pk_bf16_f32 v206, v160, v161\n"
    "v_cvt_pk_bf16_f32 v207, v162, v163\n"
    "s_waitcnt lgkmcnt(2)\n"
    "v_mfma_f32_16x16x32_bf16 v[144:147], v[96:99], v[246:249], v[144:147]\n"
    "v_fma_f32 v158, v158, v228, v229\n"
    "v_fma_f32 v159, v159, v228, v229\n"
    "v_mfma_f32_16x16x32_bf16 v[148:151], v[100:103], v[246:249], v[148:151]\n"
    "v_fma_f32 v160, v160, v228, v229\n"
    "v_fma_f32 v161, v161, v228, v229\n"
    "v_mfma_f32_16x16x32_bf16 v[152:155], v[104:107], v[246:249], v[152:155]\n"
    "ds_read_b128 v[246:249], v220 offset:47104\n"
    "v_fma_f32 v162, v162, v228, v229\n"
    "v_fma_f32 v163, v163, v228, v229\n"
    "s_waitcnt lgkmcnt(1)\n"
    "v_mfma_f32_16x16x32_bf16 v[144:147], v[108:111], v[250:253], v[144:147]\n"
    "v_cvt_pk_bf16_f32 v156, v156, v157\n"
    "v_cvt_pk_bf16_f32 v157, v158, v159\n"
    "v_mfma_f32_16x16x32_bf16 v[148:151], v[112:115], v[250:253], v[148:151]\n"
    "v_cvt_pk_bf16_f32 v236, v238, v239\n"
    "v_cvt_pk_bf16_f32 v237, v240, v241\n"
    "v_mfma_f32_16x16x32_bf16 v[152:155], v[116:119], v[250:253], v[152:155]\n"
    "ds_read_b128 v[250:253], v220 offset:48128\n"
    "v_cvt_pk_bf16_f32 v158, v160, v161\n"
    "v_cvt_pk_bf16_f32 v159, v162, v163\n"
    "s_waitcnt lgkmcnt(1)\n"
    "v_mfma_f32_16x16x32_bf16 v[144:147], v[120:123], v[246:249], v[144:147]\n"
    "ds_write_b64 v254, v[236:237] offset:33728\n"
    "v_mfma_f32_16x16x32_bf16 v[148:151], v[124:127], v[246:249], v[148:151]\n"
    "ds_write_b128 v232, v[156:159] offset:6144\n"
    "v_mfma_f32_16x16x32_bf16 v[152:155], v[128:131], v[246:249], v[152:155]\n"
    "ds_read_b128 v[246:249], v220 offset:49152\n"
    "ds_read_b128 v[156:159], v220 offset:51200\n"
    "v_cvt_pk_bf16_f32 v208, v164, v165\n"
    "s_waitcnt lgkmcnt(4)\n"
    "v_mfma_f32_16x16x32_bf16 v[144:147], v[132:135], v[250:253], v[144:147]\n"
    "v_cvt_pk_bf16_f32 v209, v166, v167\n"
    "v_cvt_pk_bf16_f32 v238, v242, v243\n"
    "v_mfma_f32_16x16x32_bf16 v[148:151], v[136:139], v[250:253], v[148:151]\n"
    "v_cvt_pk_bf16_f32 v239, v244, v245\n"
    "v_fma_f32 v164, v164, v228, v229\n"
    "v_mfma_f32_16x16x32_bf16 v[152:155], v[140:143], v[250:253], v[152:155]\n"
    "ds_read_b128 v[250:253], v220 offset:50176\n"
    "v_fma_f32 v165, v165, v228, v229\n"
    "v_fma_f32 v166, v166, v228, v229\n"
    "v_fma_f32 v167, v167, v228, v229\n"
    "v_lshlrev_b32 v234, 16, v210\n"
    "v_and_b32 v235, 0xffff0000, v210\n"
    "v_lshlrev_b32 v236, 16, v211\n"
    "v_and_b32 v237, 0xffff0000, v211\n"
    "ds_write_b64 v254, v[238:239] offset:33760\n"
    "v_cvt_pk_bf16_f32 v160, v164, v165\n"
    "s_waitcnt lgkmcnt(3)\n"
    "v_mfma_f32_16x16x32_bf16 v[234:237], v[72:75], v[246:249], v[234:237]\n"
    "v_cvt_pk_bf16_f32 v161, v166, v167\n"
    "ds_read_b128 v[164:167], v220 offset:53248\n"
    "s_waitcnt lgkmcnt(2)\n"
    "v_mfma_f32_16x16x32_bf16 v[234:237], v[84:87], v[250:253], v[234:237]\n"
    "ds_write_b64 v233, v[160:161] offset:6144\n"
    "v_mfma_f32_16x16x32_bf16 v[234:237], v[96:99], v[156:159], v[234:237]\n"
    "ds_read_b128 v[160:163], v220 offset:52224\n"
    "v_lshlrev_b32 v238, 16, v212\n"
    "v_and_b32 v239, 0xffff0000, v212\n"
    "v_lshlrev_b32 v240, 16, v213\n"
    "v_and_b32 v241, 0xffff0000, v213\n"
    "v_lshlrev_b32 v242, 16, v214\n"
    "v_and_b32 v243, 0xffff0000, v214\n"
    "v_mfma_f32_16x16x32_bf16 v[238:241], v[76:79], v[246:249], v[238:241]\n"
    "v_lshlrev_b32 v244, 16, v215\n"
    "v_and_b32 v245, 0xffff0000, v215\n"
    "v_mfma_f32_16x16x32_bf16 v[238:241], v[88:91], v[250:253], v[238:241]\n"
    "v_cvt_pk_bf16_f32 v144, v144, v145\n"
    "v_add_u32 v254, v217, v224\n"
    "v_mfma_f32_16x16x32_bf16 v[242:245], v[80:83], v[246:249], v[242:245]\n"
    "ds_read_b128 v[246:249], v220 offset:54272\n"
    "v_cvt_pk_bf16_f32 v145, v146, v147\n"
    "ds_write_b64 v254, v[144:145] offset:33696\n"
    "v_mfma_f32_16x16x32_bf16 v[242:245], v[92:95], v[250:253], v[242:245]\n"
    "v_cvt_pk_bf16_f32 v146, v148, v149\n"
    "v_cvt_pk_bf16_f32 v147, v150, v151\n"
    "v_mfma_f32_16x16x32_bf16 v[238:241], v[100:103], v[156:159], v[238:241]\n"
    "ds_write_b64 v254, v[146:147] offset:33728\n"
    "s_waitcnt lgkmcnt(3)\n"
    "v_mfma_f32_16x16x32_bf16 v[234:237], v[108:111], v[160:163], v[234:237]\n"
    "v_cvt_pk_bf16_f32 v148, v152, v153\n"
    "v_cvt_pk_bf16_f32 v149, v154, v155\n"
    "v_mfma_f32_16x16x32_bf16 v[242:245], v[104:107], v[156:159], v[242:245]\n"
    "ds_write_b64 v254, v[148:149] offset:33760\n"
    "v_mfma_f32_16x16x32_bf16 v[238:241], v[112:115], v[160:163], v[238:241]\n"
    "v_cvt_pk_bf16_f32 v210, v168, v169\n"
    "v_cvt_pk_bf16_f32 v211, v170, v171\n"
    "v_mfma_f32_16x16x32_bf16 v[234:237], v[120:123], v[164:167], v[234:237]\n"
    "v_fma_f32 v168, v168, v230, v231\n"
    "v_fma_f32 v169, v169, v230, v231\n"
    "v_mfma_f32_16x16x32_bf16 v[242:245], v[116:119], v[160:163], v[242:245]\n"
    "v_cvt_pk_bf16_f32 v212, v172, v173\n"
    "v_cvt_pk_bf16_f32 v213, v174, v175\n"
    "v_mfma_f32_16x16x32_bf16 v[238:241], v[124:127], v[164:167], v[238:241]\n"
    "v_fma_f32 v170, v170, v230, v231\n"
    "v_fma_f32 v171, v171, v230, v231\n"
    "s_waitcnt lgkmcnt(3)\n"
    "v_mfma_f32_16x16x32_bf16 v[234:237], v[132:135], v[246:249], v[234:237]\n"
    "v_add_u32 v254, v218, v224\n"
    "v_fma_f32 v172, v172, v230, v231\n"
    "v_mfma_f32_16x16x32_bf16 v[242:245], v[128:131], v[164:167], v[242:245]\n"
    "v_fma_f32 v173, v173, v230, v231\n"
    "v_fma_f32 v174, v174, v230, v231\n"
    "v_mfma_f32_16x16x32_bf16 v[238:241], v[136:139], v[246:249], v[238:241]\n"
    "v_fma_f32 v175, v175, v230, v231\n"
    "v_cvt_pk_bf16_f32 v168, v168, v169\n"
    "v_mfma_f32_16x16x32_bf16 v[242:245], v[140:143], v[246:249], v[242:245]\n"
    "v_cvt_pk_bf16_f32 v234, v234, v235\n"
    "v_cvt_pk_bf16_f32 v235, v236, v237\n"
    "v_cvt_pk_bf16_f32 v169, v170, v171\n"
    "ds_write_b64 v254, v[234:235] offset:33696\n"
    "v_cvt_pk_bf16_f32 v214, v176, v177\n"
    "v_cvt_pk_bf16_f32 v215, v178, v179\n"
    "v_cvt_pk_bf16_f32 v170, v172, v173\n"
    "v_cvt_pk_bf16_f32 v171, v174, v175\n"
    "v_cvt_pk_bf16_f32 v236, v238, v239\n"
    "v_cvt_pk_bf16_f32 v237, v240, v241\n"
    "v_fma_f32 v176, v176, v230, v231\n"
    "v_fma_f32 v177, v177, v230, v231\n"
    "v_fma_f32 v178, v178, v230, v231\n"
    "v_fma_f32 v179, v179, v230, v231\n"
    "ds_write_b128 v232, v[168:171] offset:12288\n"
    "ds_write_b64 v254, v[236:237] offset:33728\n"
    "v_cvt_pk_bf16_f32 v172, v176, v177\n"
    "v_cvt_pk_bf16_f32 v173, v178, v179\n"
    "v_cvt_pk_bf16_f32 v238, v242, v243\n"
    "v_cvt_pk_bf16_f32 v239, v244, v245\n"
    "ds_write_b64 v233, v[172:173] offset:12288\n"
    "ds_write_b64 v254, v[238:239] offset:33760\n"
    "s_setprio 1\n"
    "v_add_u32 v225, s62, v220\n"
    "v_add_u32 v254, s63, v220\n"
    "s_waitcnt lgkmcnt(0)\n"
    "s_barrier\n"
    "ds_read_b128 v[226:229], v220 offset:0\n"
    "ds_read_b128 v[168:171], v222 offset:33696\n"
    "ds_read_b128 v[230:233], v220 offset:1024\n"
    "ds_read_b128 v[234:237], v220 offset:2048\n"
    "ds_read_b128 v[238:241], v220 offset:3072\n"
    "ds_read_b128 v[172:175], v222 offset:37440\n"
    "ds_read_b128 v[176:179], v222 offset:41184\n"
    "ds_read_b128 v[242:245], v222 offset:44928\n"
    "ds_read_b128 v[246:249], v222 offset:48672\n"
    "ds_read_b128 v[250:253], v222 offset:52416\n"
    "s_waitcnt lgkmcnt(9)\n"
    "v_mfma_f32_16x16x32_bf16 v[144:147], v[0:3], v[226:229], 0\n"
    "s_waitcnt lgkmcnt(8)\n"
    "s_mov_b64 exec, s[24:25]\n"
    "buffer_store_dwordx4 v[168:171], v220, s[28:31], s54 offen sc0 sc1 nt\n"
    "s_mov_b64 exec, -1\n"
    "s_nop 1\n"
    "ds_read_b128 v[168:171], v222 offset:56160\n"
    "v_mfma_f32_16x16x32_bf16 v[148:151], v[4:7], v[226:229], 0\n"
    "s_add_u32 s54, s54, 0x15000\n"
    "v_mfma_f32_16x16x32_bf16 v[152:155], v[8:11], v[226:229], 0\n"
    "ds_read_b128 v[226:229], v220 offset:4096\n"
    "s_waitcnt lgkmcnt(9)\n"
    "v_mfma_f32_16x16x32_bf16 v[144:147], v[12:15], v[230:233], v[144:147]\n"
    "v_mfma_f32_16x16x32_bf16 v[148:151], v[16:19], v[230:233], v[148:151]\n"
    "s_waitcnt lgkmcnt(6)\n"
    "s_mov_b64 exec, s[24:25]\n"
    "buffer_store_dwordx4 v[172:175], v220, s[28:31], s54 offen sc0 sc1 nt\n"
    "s_mov_b64 exec, -1\n"
    "v_mfma_f32_16x16x32_bf16 v[152:155], v[20:23], v[230:233], v[152:155]\n"
    "ds_read_b128 v[230:233], v220 offset:5120\n"
    "s_add_u32 s54, s54, 0x15000\n"
    "v_mfma_f32_16x16x32_bf16 v[144:147], v[24:27], v[234:237], v[144:147]\n"
    "v_mfma_f32_16x16x32_bf16 v[148:151], v[28:31], v[234:237], v[148:151]\n"
    "v_mfma_f32_16x16x32_bf16 v[152:155], v[32:35], v[234:237], v[152:155]\n"
    "ds_read_b128 v[234:237], v220 offset:6144\n"
    "s_waitcnt lgkmcnt(7)\n"
    "s_mov_b64 exec, s[24:25]\n"
    "buffer_store_dwordx4 v[176:179], v220, s[28:31], s54 offen sc0 sc1 nt\n"
    "s_mov_b64 exec, -1\n"
    "v_mfma_f32_16x16x32_bf16 v[144:147], v[36:39], v[238:241], v[144:147]\n"
    "s_add_u32 s54, s54, 0x15000\n"
    "v_mfma_f32_16x16x32_bf16 v[148:151], v[40:43], v[238:241], v[148:151]\n"
    "v_mfma_f32_16x16x32_bf16 v[152:155], v[44:47], v[238:241], v[152:155]\n"
    "ds_read_b128 v[238:241], v220 offset:7168\n"
    "s_waitcnt lgkmcnt(3)\n"
    "v_mfma_f32_16x16x32_bf16 v[144:147], v[48:51], v[226:229], v[144:147]\n"
    "s_mov_b64 exec, s[24:25]\n"
    "buffer_store_dwordx4 v[242:245], v220, s[28:31], s54 offen sc0 sc1 nt\n"
    "s_mov_b64 exec, -1\n"
    "v_mfma_f32_16x16x32_bf16 v[148:151], v[52:55], v[226:229], v[148:151]\n"
    "s_add_u32 s54, s54, 0x15000\n"
    "v_mfma_f32_16x16x32_bf16 v[152:155], v[56:59], v[226:229], v[152:155]\n"
    "ds_read_b128 v[226:229], v220 offset:8192\n"
    "s_waitcnt lgkmcnt(2)\n"
    "v_mfma_f32_16x16x32_bf16 v[156:159], v[0:3], v[234:237], 0\n"
    "v_mfma_f32_16x16x32_bf16 v[160:163], v[4:7], v[234:237], 0\n"
    "s_mov_b64 exec, s[24:25]\n"
    "buffer_store_dwordx4 v[246:249], v220, s[28:31], s54 offen sc0 sc1 nt\n"
    "s_mov_b64 exec, -1\n"
    "v_mfma_f32_16x16x32_bf16 v[164:167], v[8:11], v[234:237], 0\n"
    "ds_read_b128 v[234:237], v220 offset:10240\n"
    "s_add_u32 s54, s54, 0x15000\n"
    "v_mfma_f32_16x16x32_bf16 v[144:147], v[60:63], v[230:233], v[144:147]\n"
    "v_mfma_f32_16x16x32_bf16 v[148:151], v[64:67], v[230:233], v[148:151]\n"
    "v_mfma_f32_16x16x32_bf16 v[152:155], v[68:71], v[230:233], v[152:155]\n"
    "ds_read_b128 v[230:233], v220 offset:9216\n"
    "s_mov_b64 exec, s[24:25]\n"
    "buffer_store_dwordx4 v[250:253], v220, s[28:31], s54 offen sc0 sc1 nt\n"
    "s_mov_b64 exec, -1\n"
    "s_waitcnt lgkmcnt(3)\n"
    "v_mfma_f32_16x16x32_bf16 v[156:159], v[12:15], v[238:241], v[156:159]\n"
    "s_add_u32 s54, s54, 0x15000\n"
    "v_mfma_f32_16x16x32_bf16 v[160:163], v[16:19], v[238:241], v[160:163]\n"
    "v_cvt_pk_bf16_f32 v144, v144, v145\n"
    "v_mfma_f32_16x16x32_bf16 v[164:167], v[20:23], v[238:241], v[164:167]\n"
    "ds_read_b128 v[238:241], v220 offset:11264\n"
    "v_cvt_pk_bf16_f32 v145, v146, v147\n"
    "v_cvt_pk_bf16_f32 v146, v148, v149\n"
    "s_waitcnt lgkmcnt(3)\n"
    "v_mfma_f32_16x16x32_bf16 v[156:159], v[24:27], v[226:229], v[156:159]\n"
    "v_cvt_pk_bf16_f32 v147, v150, v151\n"
    "ds_write_b128 v225, v[144:147] offset:36864\n"
    "v_mfma_f32_16x16x32_bf16 v[160:163], v[28:31], v[226:229], v[160:163]\n"
    "s_mov_b64 exec, s[26:27]\n"
    "buffer_store_dwordx4 v[168:171], v220, s[28:31], s54 offen sc0 sc1 nt\n"
    "s_mov_b64 exec, -1\n"
    "v_cvt_pk_bf16_f32 v148, v152, v153\n"
    "v_mfma_f32_16x16x32_bf16 v[164:167], v[32:35], v[226:229], v[164:167]\n"
    "ds_read_b128 v[226:229], v220 offset:12288\n"
    "v_cvt_pk_bf16_f32 v149, v154, v155\n"
    "ds_write_b64 v254, v[148:149] offset:36864\n"
    "s_waitcnt lgkmcnt(4)\n"
    "v_mfma_f32_16x16x32_bf16 v[156:159], v[36:39], v[230:233], v[156:159]\n"
    "s_add_u32 s54, s54, 0x15000\n"
    "v_mfma_f32_16x16x32_bf16 v[160:163], v[40:43], v[230:233], v[160:163]\n"
    "v_mfma_f32_16x16x32_bf16 v[164:167], v[44:47], v[230:233], v[164:167]\n"
    "ds_read_b128 v[230:233], v220 offset:13312\n"
    "v_mfma_f32_16x16x32_bf16 v[156:159], v[48:51], v[234:237], v[156:159]\n"
    "v_mfma_f32_16x16x32_bf16 v[160:163], v[52:55], v[234:237], v[160:163]\n"
    "v_mfma_f32_16x16x32_bf16 v[164:167], v[56:59], v[234:237], v[164:167]\n"
    "ds_read_b128 v[234:237], v220 offset:14336\n"
    "s_waitcnt lgkmcnt(3)\n"
    "v_mfma_f32_16x16x32_bf16 v[144:147], v[0:3], v[226:229], 0\n"
    "v_mfma_f32_16x16x32_bf16 v[156:159], v[60:63], v[238:241], v[156:159]\n"
    "v_mfma_f32_16x16x32_bf16 v[160:163], v[64:67], v[238:241], v[160:163]\n"
    "v_mfma_f32_16x16x32_bf16 v[164:167], v[68:71], v[238:241], v[164:167]\n"
    "ds_read_b128 v[238:241], v220 offset:15360\n"
    "v_mfma_f32_16x16x32_bf16 v[148:151], v[4:7], v[226:229], 0\n"
    "v_mfma_f32_16x16x32_bf16 v[152:155], v[8:11], v[226:229], 0\n"
    "ds_read_b128 v[226:229], v220 offset:16384\n"
    "s_waitcnt lgkmcnt(3)\n"
    "v_mfma_f32_16x16x32_bf16 v[144:147], v[12:15], v[230:233], v[144:147]\n"
    "v_cvt_pk_bf16_f32 v156, v156, v157\n"
    "v_mfma_f32_16x16x32_bf16 v[148:151], v[16:19], v[230:233], v[148:151]\n"
    "v_cvt_pk_bf16_f32 v157, v158, v159\n"
    "v_cvt_pk_bf16_f32 v158, v160, v161\n"
    "v_mfma_f32_16x16x32_bf16 v[152:155], v[20:23], v[230:233], v[152:155]\n"
    "ds_read_b128 v[230:233], v220 offset:17408\n"
    "v_cvt_pk_bf16_f32 v159, v162, v163\n"
    "ds_write_b128 v225, v[156:159] offset:43008\n"
    "s_waitcnt lgkmcnt(4)\n"
    "v_mfma_f32_16x16x32_bf16 v[144:147], v[24:27], v[234:237], v[144:147]\n"
    "v_cvt_pk_bf16_f32 v160, v164, v165\n"
    "v_cvt_pk_bf16_f32 v161, v166, v167\n"
    "v_mfma_f32_16x16x32_bf16 v[148:151], v[28:31], v[234:237], v[148:151]\n"
    "ds_write_b64 v254, v[160:161] offset:43008\n"
    "v_mfma_f32_16x16x32_bf16 v[152:155], v[32:35], v[234:237], v[152:155]\n"
    "s_waitcnt lgkmcnt(4)\n"
    "v_mfma_f32_16x16x32_bf16 v[144:147], v[36:39], v[238:241], v[144:147]\n"
    "v_mfma_f32_16x16x32_bf16 v[148:151], v[40:43], v[238:241], v[148:151]\n"
    "v_mfma_f32_16x16x32_bf16 v[152:155], v[44:47], v[238:241], v[152:155]\n"
    "s_waitcnt lgkmcnt(3)\n"
    "v_mfma_f32_16x16x32_bf16 v[144:147], v[48:51], v[226:229], v[144:147]\n"
    "v_mfma_f32_16x16x32_bf16 v[148:151], v[52:55], v[226:229], v[148:151]\n"
    "v_mfma_f32_16x16x32_bf16 v[152:155], v[56:59], v[226:229], v[152:155]\n"
    "s_waitcnt lgkmcnt(2)\n"
    "v_mfma_f32_16x16x32_bf16 v[144:147], v[60:63], v[230:233], v[144:147]\n"
    "v_mfma_f32_16x16x32_bf16 v[148:151], v[64:67], v[230:233], v[148:151]\n"
    "v_mfma_f32_16x16x32_bf16 v[152:155], v[68:71], v[230:233], v[152:155]\n"
    "s_nop 5\n"
    "v_cvt_pk_bf16_f32 v144, v144, v145\n"
    "v_cvt_pk_bf16_f32 v145, v146, v147\n"
    "v_cvt_pk_bf16_f32 v146, v148, v149\n"
    "v_cvt_pk_bf16_f32 v147, v150, v151\n"
    "ds_write_b128 v225, v[144:147] offset:49152\n"
    "v_cvt_pk_bf16_f32 v148, v152, v153\n"
    "v_cvt_pk_bf16_f32 v149, v154, v155\n"
    "ds_write_b64 v254, v[148:149] offset:49152\n"
    "s_waitcnt lgkmcnt(0)\n"
    "s_barrier\n"
    "s_setprio 0\n"
    "v_mbcnt_lo_u32_b32 v1, -1, 0\n"
    "v_mbcnt_hi_u32_b32 v1, -1, v1\n"
    "v_and_b32 v2, 15, v1\n"
    "v_lshrrev_b32 v3, 4, v1\n"
    "v_lshrrev_b32 v70, 2, v2\n"
    "v_mul_u32_u24 v70, 7, v70\n"
    "s_mul_i32 s48, s45, 28\n"
    "s_add_u32 s48, s48, 6\n"
    "v_add_u32 v70, s48, v70\n"
    "s_mov_b32 s49, 0x15000\n"
    "v_mul_lo_u32 v70, v70, s49\n"
    "s_mul_i32 s48, s46, 2688\n"
    "s_mul_i32 s47, s44, 96\n"
    "s_add_u32 s48, s48, s47\n"
    "v_lshlrev_b32 v4, 3, v3\n"
    "v_add3_u32 v70, v70, v4, s48\n"
    "v_and_b32 v4, 3, v2\n"
    "v_cmp_ne_u32_e64 s[60:61], 0, v4\n"
    "v_bfrev_b32 v4, 1\n"
    "s_nop 0\n"
    "v_cndmask_b32_e64 v70, v70, v4, s[60:61]\n"
    "buffer_load_dwordx2 v[64:65], v70, s[40:43], s51 offen offset:0 sc0 sc1\n"
    "buffer_load_dwordx2 v[66:67], v70, s[40:43], s52 offen offset:0 sc0 sc1\n"
    "buffer_load_dwordx2 v[68:69], v70, s[40:43], s53 offen offset:0 sc0 sc1\n"
    "ds_read_b128 v[226:229], v220 offset:18432\n"
    "ds_read_b128 v[230:233], v220 offset:19456\n"
    "ds_read_b128 v[234:237], v220 offset:20480\n"
    "ds_read_b128 v[238:241], v220 offset:21504\n"
    "ds_read_b128 v[242:245], v220 offset:22528\n"
    "ds_read_b128 v[246:249], v220 offset:23552\n"
    "ds_read_b128 v[250:253], v220 offset:24576\n"
    "v_lshlrev_b32 v144, 16, v180\n"
    "v_and_b32 v145, 0xffff0000, v180\n"
    "v_lshlrev_b32 v146, 16, v181\n"
    "v_and_b32 v147, 0xffff0000, v181\n"
    "v_lshlrev_b32 v148, 16, v182\n"
    "v_and_b32 v149, 0xffff0000, v182\n"
    "v_lshlrev_b32 v150, 16, v183\n"
    "s_waitcnt lgkmcnt(6)\n"
    "v_mfma_f32_16x16x32_bf16 v[144:147], v[72:75], v[226:229], v[144:147]\n"
    "v_and_b32 v151, 0xffff0000, v183\n"
    "v_lshlrev_b32 v152, 16, v184\n"
    "s_waitcnt lgkmcnt(5)\n"
    "v_mfma_f32_16x16x32_bf16 v[144:147], v[84:87], v[230:233], v[144:147]\n"
    "v_and_b32 v153, 0xffff0000, v184\n"
    "v_lshlrev_b32 v154, 16, v185\n"
    "v_mfma_f32_16x16x32_bf16 v[148:151], v[76:79], v[226:229], v[148:151]\n"
    "v_and_b32 v155, 0xffff0000, v185\n"
    "v_lshlrev_b32 v156, 16, v186\n"
    "v_mfma_f32_16x16x32_bf16 v[148:151], v[88:91], v[230:233], v[148:151]\n"
    "v_and_b32 v157, 0xffff0000, v186\n"
    "v_lshlrev_b32 v158, 16, v187\n"
    "v_mfma_f32_16x16x32_bf16 v[152:155], v[80:83], v[226:229], v[152:155]\n"
    "ds_read_b128 v[226:229], v220 offset:25600\n"
    "v_and_b32 v159, 0xffff0000, v187\n"
    "v_lshlrev_b32 v160, 16, v188\n"
    "v_mfma_f32_16x16x32_bf16 v[152:155], v[92:95], v[230:233], v[152:155]\n"
    "ds_read_b128 v[230:233], v220 offset:26624\n"
    "v_and_b32 v161, 0xffff0000, v188\n"
    "v_lshlrev_b32 v162, 16, v189\n"
    "s_waitcnt lgkmcnt(6)\n"
    "v_mfma_f32_16x16x32_bf16 v[144:147], v[96:99], v[234:237], v[144:147]\n"
    "v_and_b32 v163, 0xffff0000, v189\n"
    "v_lshlrev_b32 v164, 16, v190\n"
    "v_mfma_f32_16x16x32_bf16 v[148:151], v[100:103], v[234:237], v[148:151]\n"
    "v_and_b32 v165, 0xffff0000, v190\n"
    "v_lshlrev_b32 v166, 16, v191\n"
    "v_mfma_f32_16x16x32_bf16 v[152:155], v[104:107], v[234:237], v[152:155]\n"
    "ds_read_b128 v[234:237], v220 offset:27648\n"
    "v_and_b32 v167, 0xffff0000, v191\n"
    "v_lshlrev_b32 v168, 16, v192\n"
    "s_waitcnt lgkmcnt(6)\n"
    "v_mfma_f32_16x16x32_bf16 v[144:147], v[108:111], v[238:241], v[144:147]\n"
    "v_and_b32 v169, 0xffff0000, v192\n"
    "v_lshlrev_b32 v170, 16, v193\n"
    "v_mfma_f32_16x16x32_bf16 v[148:151], v[112:115], v[238:241], v[148:151]\n"
    "v_and_b32 v171, 0xffff0000, v193\n"
    "v_lshlrev_b32 v172, 16, v194\n"
    "v_mfma_f32_16x16x32_bf16 v[152:155], v[116:119], v[238:241], v[152:155]\n"
    "ds_read_b128 v[238:241], v220 offset:28672\n"
    "v_and_b32 v173, 0xffff0000, v194\n"
    "v_lshlrev_b32 v174, 16, v195\n"
    "s_waitcnt lgkmcnt(6)\n"
    "v_mfma_f32_16x16x32_bf16 v[144:147], v[120:123], v[242:245], v[144:147]\n"
    "v_and_b32 v175, 0xffff0000, v195\n"
    "v_lshlrev_b32 v176, 16, v196\n"
    "v_mfma_f32_16x16x32_bf16 v[148:151], v[124:127], v[242:245], v[148:151]\n"
    "v_and_b32 v177, 0xffff0000, v196\n"
    "v_lshlrev_b32 v178, 16, v197\n"
    "v_mfma_f32_16x16x32_bf16 v[152:155], v[128:131], v[242:245], v[152:155]\n"
    "ds_read_b128 v[242:245], v220 offset:29696\n"
    "v_and_b32 v179, 0xffff0000, v197\n"
    "v_add_u32 v254, v216, v224\n"
    "s_waitcnt lgkmcnt(6)\n"
    "v_mfma_f32_16x16x32_bf16 v[144:147], v[132:135], v[246:249], v[144:147]\n"
    "v_add_u32 v255, v217, v224\n"
    "s_waitcnt vmcnt(0)\n"
    "v_and_b32 v39, 0xffff0000, v69\n"
    "v_mfma_f32_16x16x32_bf16 v[148:151], v[136:139], v[246:249], v[148:151]\n"
    "v_lshlrev_b32 v28, 16, v64\n"
    "v_and_b32 v29, 0xffff0000, v64\n"
    "v_mfma_f32_16x16x32_bf16 v[152:155], v[140:143], v[246:249], v[152:155]\n"
    "ds_read_b128 v[246:249], v220 offset:30720\n"
    "v_lshlrev_b32 v30, 16, v65\n"
    "v_cvt_pk_bf16_f32 v144, v144, v145\n"
    "s_waitcnt lgkmcnt(6)\n"
    "v_mfma_f32_16x16x32_bf16 v[156:159], v[72:75], v[250:253], v[156:159]\n"
    "v_cvt_pk_bf16_f32 v145, v146, v147\n"
    "ds_write_b64 v254, v[144:145] offset:0\n"
    "v_mfma_f32_16x16x32_bf16 v[160:163], v[76:79], v[250:253], v[160:163]\n"
    "v_cvt_pk_bf16_f32 v146, v148, v149\n"
    "v_cvt_pk_bf16_f32 v147, v150, v151\n"
    "v_mfma_f32_16x16x32_bf16 v[164:167], v[80:83], v[250:253], v[164:167]\n"
    "ds_read_b128 v[250:253], v220 offset:31744\n"
    "ds_write_b64 v254, v[146:147] offset:32\n"
    "s_waitcnt lgkmcnt(8)\n"
    "v_mfma_f32_16x16x32_bf16 v[156:159], v[84:87], v[226:229], v[156:159]\n"
    "v_cvt_pk_bf16_f32 v148, v152, v153\n"
    "v_cvt_pk_bf16_f32 v149, v154, v155\n"
    "v_mfma_f32_16x16x32_bf16 v[160:163], v[88:91], v[226:229], v[160:163]\n"
    "ds_write_b64 v254, v[148:149] offset:64\n"
    "v_mfma_f32_16x16x32_bf16 v[164:167], v[92:95], v[226:229], v[164:167]\n"
    "ds_read_b128 v[226:229], v220 offset:32768\n"
    "v_add_u32 v254, v218, v224\n"
    "v_lshlrev_b32 v144, 16, v198\n"
    "s_waitcnt lgkmcnt(5)\n"
    "v_mfma_f32_16x16x32_bf16 v[168:171], v[72:75], v[246:249], v[168:171]\n"
    "v_and_b32 v145, 0xffff0000, v198\n"
    "v_lshlrev_b32 v146, 16, v199\n"
    "v_mfma_f32_16x16x32_bf16 v[156:159], v[96:99], v[230:233], v[156:159]\n"
    "v_and_b32 v147, 0xffff0000, v199\n"
    "v_lshlrev_b32 v148, 16, v200\n"
    "v_mfma_f32_16x16x32_bf16 v[160:163], v[100:103], v[230:233], v[160:163]\n"
    "v_and_b32 v149, 0xffff0000, v200\n"
    "v_lshlrev_b32 v150, 16, v201\n"
    "v_mfma_f32_16x16x32_bf16 v[164:167], v[104:107], v[230:233], v[164:167]\n"
    "ds_read_b128 v[230:233], v220 offset:33792\n"
    "v_and_b32 v151, 0xffff0000, v201\n"
    "v_lshlrev_b32 v152, 16, v202\n"
    "v_mfma_f32_16x16x32_bf16 v[172:175], v[76:79], v[246:249], v[172:175]\n"
    "v_and_b32 v153, 0xffff0000, v202\n"
    "v_lshlrev_b32 v154, 16, v203\n"
    "s_waitcnt lgkmcnt(4)\n"
    "v_mfma_f32_16x16x32_bf16 v[168:171], v[84:87], v[250:253], v[168:171]\n"
    "v_and_b32 v155, 0xffff0000, v203\n"
    "v_and_b32 v31, 0xffff0000, v65\n"
    "v_mfma_f32_16x16x32_bf16 v[156:159], v[108:111], v[234:237], v[156:159]\n"
    "v_lshlrev_b32 v32, 16, v66\n"
    "v_and_b32 v33, 0xffff0000, v66\n"
    "v_mfma_f32_16x16x32_bf16 v[160:163], v[112:115], v[234:237], v[160:163]\n"
    "v_lshlrev_b32 v34, 16, v67\n"
    "v_and_b32 v35, 0xffff0000, v67\n"
    "v_mfma_f32_16x16x32_bf16 v[164:167], v[116:119], v[234:237], v[164:167]\n"
    "ds_read_b128 v[234:237], v220 offset:34816\n"
    "v_lshlrev_b32 v36, 16, v68\n"
    "v_and_b32 v37, 0xffff0000, v68\n"
    "v_mfma_f32_16x16x32_bf16 v[176:179], v[80:83], v[246:249], v[176:179]\n"
    "v_lshlrev_b32 v38, 16, v69\n"
    "v_mfma_f32_16x16x32_bf16 v[172:175], v[88:91], v[250:253], v[172:175]\n"
    "s_waitcnt lgkmcnt(2)\n"
    "v_mfma_f32_16x16x32_bf16 v[168:171], v[96:99], v[226:229], v[168:171]\n"
    "v_mfma_f32_16x16x32_bf16 v[156:159], v[120:123], v[238:241], v[156:159]\n"
    "v_mfma_f32_16x16x32_bf16 v[160:163], v[124:127], v[238:241], v[160:163]\n"
    "v_mfma_f32_16x16x32_bf16 v[164:167], v[128:131], v[238:241], v[164:167]\n"
    "ds_read_b128 v[238:241], v220 offset:35840\n"
    "v_mfma_f32_16x16x32_bf16 v[176:179], v[92:95], v[250:253], v[176:179]\n"
    "v_mfma_f32_16x16x32_bf16 v[172:175], v[100:103], v[226:229], v[172:175]\n"
    "s_waitcnt lgkmcnt(2)\n"
    "v_mfma_f32_16x16x32_bf16 v[168:171], v[108:111], v[230:233], v[168:171]\n"
    "v_mfma_f32_16x16x32_bf16 v[176:179], v[104:107], v[226:229], v[176:179]\n"
    "v_mfma_f32_16x16x32_bf16 v[156:159], v[132:135], v[242:245], v[156:159]\n"
    "v_mfma_f32_16x16x32_bf16 v[172:175], v[112:115], v[230:233], v[172:175]\n"
    "s_waitcnt lgkmcnt(1)\n"
    "v_mfma_f32_16x16x32_bf16 v[168:171], v[120:123], v[234:237], v[168:171]\n"
    "v_mfma_f32_16x16x32_bf16 v[160:163], v[136:139], v[242:245], v[160:163]\n"
    "v_mfma_f32_16x16x32_bf16 v[176:179], v[116:119], v[230:233], v[176:179]\n"
    "v_mfma_f32_16x16x32_bf16 v[164:167], v[140:143], v[242:245], v[164:167]\n"
    "v_mfma_f32_16x16x32_bf16 v[172:175], v[124:127], v[234:237], v[172:175]\n"
    "s_nop 0\n"
    "v_cvt_pk_bf16_f32 v156, v156, v157\n"
    "s_waitcnt lgkmcnt(0)\n"
    "v_mfma_f32_16x16x32_bf16 v[168:171], v[132:135], v[238:241], v[168:171]\n"
    "v_cvt_pk_bf16_f32 v157, v158, v159\n"
    "ds_write_b64 v255, v[156:157] offset:0\n"
    "v_mfma_f32_16x16x32_bf16 v[176:179], v[128:131], v[234:237], v[176:179]\n"
    "v_cvt_pk_bf16_f32 v158, v160, v161\n"
    "v_cvt_pk_bf16_f32 v159, v162, v163\n"
    "v_mfma_f32_16x16x32_bf16 v[172:175], v[136:139], v[238:241], v[172:175]\n"
    "ds_write_b64 v255, v[158:159] offset:32\n"
    "v_mfma_f32_16x16x32_bf16 v[176:179], v[140:143], v[238:241], v[176:179]\n"
    "v_cvt_pk_bf16_f32 v160, v164, v165\n"
    "v_cvt_pk_bf16_f32 v161, v166, v167\n"
    "ds_write_b64 v255, v[160:161] offset:64\n"
    "v_cvt_pk_bf16_f32 v168, v168, v169\n"
    "v_cvt_pk_bf16_f32 v169, v170, v171\n"
    "ds_write_b64 v254, v[168:169] offset:0\n"
    "v_cvt_pk_bf16_f32 v170, v172, v173\n"
    "v_cvt_pk_bf16_f32 v171, v174, v175\n"
    "ds_write_b64 v254, v[170:171] offset:32\n"
    "v_cvt_pk_bf16_f32 v172, v176, v177\n"
    "v_cvt_pk_bf16_f32 v173, v178, v179\n"
    "ds_write_b64 v254, v[172:173] offset:64\n"
    "v_lshlrev_b32 v156, 16, v204\n"
    "v_and_b32 v157, 0xffff0000, v204\n"
    "v_lshlrev_b32 v158, 16, v205\n"
    "v_and_b32 v159, 0xffff0000, v205\n"
    "v_lshlrev_b32 v160, 16, v206\n"
    "v_and_b32 v161, 0xffff0000, v206\n"
    "v_lshlrev_b32 v162, 16, v207\n"
    "v_and_b32 v163, 0xffff0000, v207\n"
    "v_lshlrev_b32 v164, 16, v208\n"
    "v_and_b32 v165, 0xffff0000, v208\n"
    "s_waitcnt lgkmcnt(0)\n"
    "s_barrier\n"
    "ds_read_b128 v[226:229], v220 offset:36864\n"
    "ds_read_b128 v[0:3], v222 offset:0\n"
    "ds_read_b128 v[230:233], v220 offset:37888\n"
    "ds_read_b128 v[250:253], v220 offset:43008\n"
    "ds_read_b128 v[234:237], v220 offset:38912\n"
    "ds_read_b128 v[238:241], v220 offset:39936\n"
    "ds_read_b128 v[4:7], v222 offset:3744\n"
    "ds_read_b128 v[242:245], v220 offset:40960\n"
    "ds_read_b128 v[246:249], v220 offset:41984\n"
    "ds_read_b128 v[8:11], v222 offset:7488\n"
    "ds_read_b128 v[12:15], v222 offset:11232\n"
    "ds_read_b128 v[16:19], v222 offset:14976\n"
    "ds_read_b128 v[20:23], v222 offset:18720\n"
    "ds_read_b128 v[24:27], v222 offset:22464\n"
    "s_waitcnt lgkmcnt(13)\n"
    "v_mfma_f32_16x16x32_bf16 v[144:147], v[72:75], v[226:229], v[144:147]\n"
    "s_waitcnt lgkmcnt(12)\n"
    "s_mov_b64 exec, s[24:25]\n"
    "buffer_store_dwordx4 v[0:3], v220, s[28:31], s54 offen sc0 sc1 nt\n"
    "s_mov_b64 exec, -1\n"
    "v_lshlrev_b32 v166, 16, v209\n"
    "v_mfma_f32_16x16x32_bf16 v[148:151], v[76:79], v[226:229], v[148:151]\n"
    "s_add_u32 s54, s54, 0x15000\n"
    "v_and_b32 v167, 0xffff0000, v209\n"
    "v_add_u32 v254, v216, v224\n"
    "v_mfma_f32_16x16x32_bf16 v[152:155], v[80:83], v[226:229], v[152:155]\n"
    "ds_read_b128 v[226:229], v220 offset:44032\n"
    "v_add_u32 v255, v217, v224\n"
    "v_and_b32 v179, 0xffff0000, v215\n"
    "s_waitcnt lgkmcnt(12)\n"
    "v_mfma_f32_16x16x32_bf16 v[144:147], v[84:87], v[230:233], v[144:147]\n"
    "v_lshlrev_b32 v168, 16, v210\n"
    "v_and_b32 v169, 0xffff0000, v210\n"
    "v_mfma_f32_16x16x32_bf16 v[148:151], v[88:91], v[230:233], v[148:151]\n"
    "v_lshlrev_b32 v170, 16, v211\n"
    "s_waitcnt lgkmcnt(8)\n"
    "s_mov_b64 exec, s[24:25]\n"
    "buffer_store_dwordx4 v[4:7], v220, s[28:31], s54 offen sc0 sc1 nt\n"
    "s_mov_b64 exec, -1\n"
    "v_mfma_f32_16x16x32_bf16 v[152:155], v[92:95], v[230:233], v[152:155]\n"
    "ds_read_b128 v[230:233], v220 offset:45056\n"
    "s_add_u32 s54, s54, 0x15000\n"
    "v_and_b32 v171, 0xffff0000, v211\n"
    "v_mfma_f32_16x16x32_bf16 v[156:159], v[72:75], v[250:253], v[156:159]\n"
    "v_lshlrev_b32 v172, 16, v212\n"
    "v_and_b32 v173, 0xffff0000, v212\n"
    "v_mfma_f32_16x16x32_bf16 v[144:147], v[96:99], v[234:237], v[144:147]\n"
    "v_lshlrev_b32 v174, 16, v213\n"
    "v_and_b32 v175, 0xffff0000, v213\n"
    "v_mfma_f32_16x16x32_bf16 v[148:151], v[100:103], v[234:237], v[148:151]\n"
    "v_lshlrev_b32 v176, 16, v214\n"
    "v_and_b32 v177, 0xffff0000, v214\n"
    "v_mfma_f32_16x16x32_bf16 v[152:155], v[104:107], v[234:237], v[152:155]\n"
    "ds_read_b128 v[234:237], v220 offset:46080\n"
    "s_waitcnt lgkmcnt(7)\n"
    "s_mov_b64 exec, s[24:25]\n"
    "buffer_store_dwordx4 v[8:11], v220, s[28:31], s54 offen sc0 sc1 nt\n"
    "s_mov_b64 exec, -1\n"
    "v_mfma_f32_16x16x32_bf16 v[160:163], v[76:79], v[250:253], v[160:163]\n"
    "s_add_u32 s54, s54, 0x15000\n"
    "v_lshlrev_b32 v178, 16, v215\n"
    "s_waitcnt lgkmcnt(2)\n"
    "v_mfma_f32_16x16x32_bf16 v[156:159], v[84:87], v[226:229], v[156:159]\n"
    "v_mfma_f32_16x16x32_bf16 v[144:147], v[108:111], v[238:241], v[144:147]\n"
    "v_mfma_f32_16x16x32_bf16 v[148:151], v[112:115], v[238:241], v[148:151]\n"
    "s_mov_b64 exec, s[24:25]\n"
    "buffer_store_dwordx4 v[12:15], v220, s[28:31], s54 offen sc0 sc1 nt\n"
    "s_mov_b64 exec, -1\n"
    "v_mfma_f32_16x16x32_bf16 v[152:155], v[116:119], v[238:241], v[152:155]\n"
    "ds_read_b128 v[238:241], v220 offset:47104\n"
    "s_add_u32 s54, s54, 0x15000\n"
    "v_mfma_f32_16x16x32_bf16 v[164:167], v[80:83], v[250:253], v[164:167]\n"
    "v_mfma_f32_16x16x32_bf16 v[160:163], v[88:91], v[226:229], v[160:163]\n"
    "s_waitcnt lgkmcnt(2)\n"
    "v_mfma_f32_16x16x32_bf16 v[156:159], v[96:99], v[230:233], v[156:159]\n"
    "s_mov_b64 exec, s[24:25]\n"
    "buffer_store_dwordx4 v[16:19], v220, s[28:31], s54 offen sc0 sc1 nt\n"
    "s_mov_b64 exec, -1\n"
    "v_mfma_f32_16x16x32_bf16 v[144:147], v[120:123], v[242:245], v[144:147]\n"
    "s_add_u32 s54, s54, 0x15000\n"
    "v_mfma_f32_16x16x32_bf16 v[148:151], v[124:127], v[242:245], v[148:151]\n"
    "v_mfma_f32_16x16x32_bf16 v[152:155], v[128:131], v[242:245], v[152:155]\n"
    "ds_read_b128 v[242:245], v220 offset:48128\n"
    "v_mfma_f32_16x16x32_bf16 v[164:167], v[92:95], v[226:229], v[164:167]\n"
    "s_mov_b64 exec, s[24:25]\n"
    "buffer_store_dwordx4 v[20:23], v220, s[28:31], s54 offen sc0 sc1 nt\n"
    "s_mov_b64 exec, -1\n"
    "v_mfma_f32_16x16x32_bf16 v[160:163], v[100:103], v[230:233], v[160:163]\n"
    "s_add_u32 s54, s54, 0x15000\n"
    "s_waitcnt lgkmcnt(2)\n"
    "v_mfma_f32_16x16x32_bf16 v[156:159], v[108:111], v[234:237], v[156:159]\n"
    "v_mfma_f32_16x16x32_bf16 v[164:167], v[104:107], v[230:233], v[164:167]\n"
    "v_mfma_f32_16x16x32_bf16 v[144:147], v[132:135], v[246:249], v[144:147]\n"
    "s_mov_b64 exec, s[26:27]\n"
    "buffer_store_dwordx4 v[24:27], v220, s[28:31], s54 offen sc0 sc1 nt\n"
    "s_mov_b64 exec, -1\n"
    "v_mfma_f32_16x16x32_bf16 v[160:163], v[112:115], v[234:237], v[160:163]\n"
    "s_add_u32 s54, s54, 0x15000\n"
    "s_waitcnt lgkmcnt(1)\n"
    "v_mfma_f32_16x16x32_bf16 v[156:159], v[120:123], v[238:241], v[156:159]\n"
    "v_mfma_f32_16x16x32_bf16 v[148:151], v[136:139], v[246:249], v[148:151]\n"
    "v_mfma_f32_16x16x32_bf16 v[164:167], v[116:119], v[234:237], v[164:167]\n"
    "s_nop 0\n"
    "v_cvt_pk_bf16_f32 v144, v144, v145\n"
    "v_mfma_f32_16x16x32_bf16 v[152:155], v[140:143], v[246:249], v[152:155]\n"
    "v_cvt_pk_bf16_f32 v145, v146, v147\n"
    "ds_write_b64 v254, v[144:145] offset:33696\n"
    "v_mfma_f32_16x16x32_bf16 v[160:163], v[124:127], v[238:241], v[160:163]\n"
    "s_nop 0\n"
    "v_cvt_pk_bf16_f32 v146, v148, v149\n"
    "v_cvt_pk_bf16_f32 v147, v150, v151\n"
    "s_waitcnt lgkmcnt(1)\n"
    "v_mfma_f32_16x16x32_bf16 v[156:159], v[132:135], v[242:245], v[156:159]\n"
    "ds_write_b64 v254, v[146:147] offset:33728\n"
    "v_mfma_f32_16x16x32_bf16 v[164:167], v[128:131], v[238:241], v[164:167]\n"
    "v_cvt_pk_bf16_f32 v148, v152, v153\n"
    "v_cvt_pk_bf16_f32 v149, v154, v155\n"
    "v_mfma_f32_16x16x32_bf16 v[160:163], v[136:139], v[242:245], v[160:163]\n"
    "ds_write_b64 v254, v[148:149] offset:33760\n"
    "v_mfma_f32_16x16x32_bf16 v[164:167], v[140:143], v[242:245], v[164:167]\n"
    "v_add_u32 v254, v218, v224\n"
    "v_cvt_pk_bf16_f32 v156, v156, v157\n"
    "v_cvt_pk_bf16_f32 v157, v158, v159\n"
    "ds_write_b64 v255, v[156:157] offset:33696\n"
    "s_nop 1\n"
    "v_cvt_pk_bf16_f32 v158, v160, v161\n"
    "v_cvt_pk_bf16_f32 v159, v162, v163\n"
    "ds_write_b64 v255, v[158:159] offset:33728\n"
    "v_cvt_pk_bf16_f32 v160, v164, v165\n"
    "v_cvt_pk_bf16_f32 v161, v166, v167\n"
    "ds_write_b64 v255, v[160:161] offset:33760\n"
    "s_waitcnt lgkmcnt(0)\n"
    "s_barrier\n"
    "ds_read_b128 v[40:43], v220 offset:55296\n"
    "ds_read_b128 v[44:47], v220 offset:56320\n"
    "ds_read_b128 v[48:51], v220 offset:57344\n"
    "ds_read_b128 v[52:55], v220 offset:58368\n"
    "ds_read_b128 v[226:229], v220 offset:49152\n"
    "ds_read_b128 v[56:59], v220 offset:59392\n"
    "ds_read_b128 v[230:233], v220 offset:50176\n"
    "ds_read_b128 v[60:63], v220 offset:60416\n"
    "ds_read_b128 v[234:237], v220 offset:51200\n"
    "ds_read_b128 v[238:241], v220 offset:52224\n"
    "ds_read_b128 v[242:245], v220 offset:53248\n"
    "ds_read_b128 v[246:249], v220 offset:54272\n"
    "s_waitcnt lgkmcnt(11)\n"
    "v_mfma_f32_16x16x32_bf16 v[28:31], v[72:75], v[40:43], v[28:31]\n"
    "s_waitcnt lgkmcnt(10)\n"
    "v_mfma_f32_16x16x32_bf16 v[28:31], v[84:87], v[44:47], v[28:31]\n"
    "s_waitcnt lgkmcnt(9)\n"
    "v_mfma_f32_16x16x32_bf16 v[28:31], v[96:99], v[48:51], v[28:31]\n"
    "s_waitcnt lgkmcnt(8)\n"
    "v_mfma_f32_16x16x32_bf16 v[28:31], v[108:111], v[52:55], v[28:31]\n"
    "s_waitcnt lgkmcnt(7)\n"
    "v_mfma_f32_16x16x32_bf16 v[168:171], v[72:75], v[226:229], v[168:171]\n"
    "v_mfma_f32_16x16x32_bf16 v[32:35], v[76:79], v[40:43], v[32:35]\n"
    "v_mfma_f32_16x16x32_bf16 v[172:175], v[76:79], v[226:229], v[172:175]\n"
    "s_waitcnt lgkmcnt(6)\n"
    "v_mfma_f32_16x16x32_bf16 v[28:31], v[120:123], v[56:59], v[28:31]\n"
    "s_waitcnt lgkmcnt(5)\n"
    "v_mfma_f32_16x16x32_bf16 v[168:171], v[84:87], v[230:233], v[168:171]\n"
    "v_mfma_f32_16x16x32_bf16 v[176:179], v[80:83], v[226:229], v[176:179]\n"
    "v_mfma_f32_16x16x32_bf16 v[32:35], v[88:91], v[44:47], v[32:35]\n"
    "v_mfma_f32_16x16x32_bf16 v[172:175], v[88:91], v[230:233], v[172:175]\n"
    "s_waitcnt lgkmcnt(4)\n"
    "v_mfma_f32_16x16x32_bf16 v[28:31], v[132:135], v[60:63], v[28:31]\n"
    "s_waitcnt lgkmcnt(3)\n"
    "v_mfma_f32_16x16x32_bf16 v[168:171], v[96:99], v[234:237], v[168:171]\n"
    "v_mfma_f32_16x16x32_bf16 v[176:179], v[92:95], v[230:233], v[176:179]\n"
    "v_mfma_f32_16x16x32_bf16 v[32:35], v[100:103], v[48:51], v[32:35]\n"
    "v_mfma_f32_16x16x32_bf16 v[172:175], v[100:103], v[234:237], v[172:175]\n"
    "s_waitcnt lgkmcnt(2)\n"
    "v_mfma_f32_16x16x32_bf16 v[168:171], v[108:111], v[238:241], v[168:171]\n"
    "v_mfma_f32_16x16x32_bf16 v[176:179], v[104:107], v[234:237], v[176:179]\n"
    "v_cvt_pk_bf16_f32 v28, v28, v29\n"
    "v_mfma_f32_16x16x32_bf16 v[32:35], v[112:115], v[52:55], v[32:35]\n"
    "v_cvt_pk_bf16_f32 v29, v30, v31\n"
    "buffer_store_dwordx2 v[28:29], v70, s[40:43], s51 offen offset:0 sc0 sc1\n"
    "v_mfma_f32_16x16x32_bf16 v[36:39], v[80:83], v[40:43], v[36:39]\n"
    "v_mfma_f32_16x16x32_bf16 v[172:175], v[112:115], v[238:241], v[172:175]\n"
    "s_waitcnt lgkmcnt(1)\n"
    "v_mfma_f32_16x16x32_bf16 v[168:171], v[120:123], v[242:245], v[168:171]\n"
    "v_mfma_f32_16x16x32_bf16 v[176:179], v[116:119], v[238:241], v[176:179]\n"
    "v_mfma_f32_16x16x32_bf16 v[32:35], v[124:127], v[56:59], v[32:35]\n"
    "v_mfma_f32_16x16x32_bf16 v[36:39], v[92:95], v[44:47], v[36:39]\n"
    "v_mfma_f32_16x16x32_bf16 v[172:175], v[124:127], v[242:245], v[172:175]\n"
    "s_waitcnt lgkmcnt(0)\n"
    "v_mfma_f32_16x16x32_bf16 v[168:171], v[132:135], v[246:249], v[168:171]\n"
    "v_mfma_f32_16x16x32_bf16 v[176:179], v[128:131], v[242:245], v[176:179]\n"
    "v_mfma_f32_16x16x32_bf16 v[32:35], v[136:139], v[60:63], v[32:35]\n"
    "v_mfma_f32_16x16x32_bf16 v[36:39], v[104:107], v[48:51], v[36:39]\n"
    "v_mfma_f32_16x16x32_bf16 v[172:175], v[136:139], v[246:249], v[172:175]\n"
    "v_mfma_f32_16x16x32_bf16 v[176:179], v[140:143], v[246:249], v[176:179]\n"
    "v_mfma_f32_16x16x32_bf16 v[36:39], v[116:119], v[52:55], v[36:39]\n"
    "s_nop 1\n"
    "v_cvt_pk_bf16_f32 v168, v168, v169\n"
    "v_mfma_f32_16x16x32_bf16 v[36:39], v[128:131], v[56:59], v[36:39]\n"
    "v_cvt_pk_bf16_f32 v169, v170, v171\n"
    "v_cvt_pk_bf16_f32 v30, v32, v33\n"
    "v_mfma_f32_16x16x32_bf16 v[36:39], v[140:143], v[60:63], v[36:39]\n"
    "ds_write_b64 v254, v[168:169] offset:33696\n"
    "v_cvt_pk_bf16_f32 v31, v34, v35\n"
    "v_cvt_pk_bf16_f32 v170, v172, v173\n"
    "v_cvt_pk_bf16_f32 v171, v174, v175\n"
    "buffer_store_dwordx2 v[30:31], v70, s[40:43], s52 offen offset:0 sc0 sc1\n"
    "ds_write_b64 v254, v[170:171] offset:33728\n"
    "v_cvt_pk_bf16_f32 v172, v176, v177\n"
    "v_cvt_pk_bf16_f32 v173, v178, v179\n"
    "ds_write_b64 v254, v[172:173] offset:33760\n"
    "v_cvt_pk_bf16_f32 v32, v36, v37\n"
    "v_cvt_pk_bf16_f32 v33, v38, v39\n"
    "buffer_store_dwordx2 v[32:33], v70, s[40:43], s53 offen offset:0 sc0 sc1\n"
    "s_waitcnt lgkmcnt(0)\n"
    "s_barrier\n"
    "ds_read_b128 v[0:3], v222 offset:33696\n"
    "ds_read_b128 v[4:7], v222 offset:37440\n"
    "ds_read_b128 v[8:11], v222 offset:41184\n"
    "ds_read_b128 v[12:15], v222 offset:44928\n"
    "ds_read_b128 v[16:19], v222 offset:48672\n"
    "ds_read_b128 v[20:23], v222 offset:52416\n"
    "ds_read_b128 v[24:27], v222 offset:56160\n"
    "s_waitcnt lgkmcnt(6)\n"
    "s_mov_b64 exec, s[24:25]\n"
    "buffer_store_dwordx4 v[0:3], v220, s[28:31], s54 offen sc0 sc1 nt\n"
    "s_mov_b64 exec, -1\n"
    "s_add_u32 s54, s54, 0x15000\n"
    "s_waitcnt lgkmcnt(5)\n"
    "s_mov_b64 exec, s[24:25]\n"
    "buffer_store_dwordx4 v[4:7], v220, s[28:31], s54 offen sc0 sc1 nt\n"
    "s_mov_b64 exec, -1\n"
    "s_add_u32 s54, s54, 0x15000\n"
    "s_waitcnt lgkmcnt(4)\n"
    "s_mov_b64 exec, s[24:25]\n"
    "buffer_store_dwordx4 v[8:11], v220, s[28:31], s54 offen sc0 sc1 nt\n"
    "s_mov_b64 exec, -1\n"
    "s_add_u32 s54, s54, 0x15000\n"
    "s_waitcnt lgkmcnt(3)\n"
    "s_mov_b64 exec, s[24:25]\n"
    "buffer_store_dwordx4 v[12:15], v220, s[28:31], s54 offen sc0 sc1 nt\n"
    "s_mov_b64 exec, -1\n"
    "s_add_u32 s54, s54, 0x15000\n"
    "s_waitcnt lgkmcnt(2)\n"
    "s_mov_b64 exec, s[24:25]\n"
    "buffer_store_dwordx4 v[16:19], v220, s[28:31], s54 offen sc0 sc1 nt\n"
    "s_mov_b64 exec, -1\n"
    "s_add_u32 s54, s54, 0x15000\n"
    "s_waitcnt lgkmcnt(1)\n"
    "s_mov_b64 exec, s[24:25]\n"
    "buffer_store_dwordx4 v[20:23], v220, s[28:31], s54 offen sc0 sc1 nt\n"
    "s_mov_b64 exec, -1\n"
    "s_add_u32 s54, s54, 0x15000\n"
    "s_waitcnt lgkmcnt(0)\n"
    "s_mov_b64 exec, s[26:27]\n"
    "buffer_store_dwordx4 v[24:27], v220, s[28:31], s54 offen sc0 sc1 nt\n"
    "s_mov_b64 exec, -1\n"
    "s_add_u32 s54, s54, 0x15000\n"
    "s_nop 3\n"
    "s_branch LEND%=\n"
    "LAMAIN%=:\n"
    "v_mov_b32 v222, 0\n"
    "v_mov_b32 v223, 0\n"
    "v_mov_b32 v224, 0\n"
    "v_mov_b32 v225, 0\n"
    "s_mul_i32 s47, s44, 8160\n"
    "s_add_u32 s47, s47, 0x10740\n"
    "v_lshlrev_b32 v216, 4, v210\n"
    "v_add_u32 v216, s47, v216\n"
    "ds_write_b128 v216, v[222:225] offset:0\n"
    "ds_write_b128 v216, v[222:225] offset:1024\n"
    "ds_write_b128 v216, v[222:225] offset:2048\n"
    "ds_write_b128 v216, v[222:225] offset:3072\n"
    "ds_write_b128 v216, v[222:225] offset:4096\n"
    "ds_write_b128 v216, v[222:225] offset:5120\n"
    "ds_write_b128 v216, v[222:225] offset:6144\n"
    "ds_write_b128 v216, v[222:225] offset:7136\n"
    "v_lshrrev_b32 v144, 2, v210\n"
    "v_mul_u32_u24 v144, 0x480, v144\n"
    "v_lshrrev_b32 v213, 3, v210\n"
    "v_and_b32 v213, 2, v213\n"
    "v_xor_b32 v213, v213, v210\n"
    "v_and_b32 v213, 3, v213\n"
    "v_lshl_add_u32 v144, v213, 4, v144\n"
    "s_mul_i32 s47, s44, 96\n"
    "s_add_u32 s47, s47, 384\n"
    "s_add_u32 s48, s47, s51\n"
    "v_add_u32 v144, s48, v144\n"
    "v_add_u32 v145, 0x4800, v144\n"
    "v_lshrrev_b32 v146, 1, v210\n"
    "v_mul_u32_u24 v146, 0x480, v146\n"
    "v_and_b32 v213, 1, v210\n"
    "v_lshl_add_u32 v146, v213, 4, v146\n"
    "s_add_u32 s48, s47, s53\n"
    "v_add_u32 v146, s48, v146\n"
    "v_mov_b32 v147, 0x27ec0\n"
    "v_mov_b32 v148, 1\n"
    "ds_write_b64 v147, v[222:223] offset:0\n"
    "s_mul_i32 s54, s44, 6144\n"
    "s_add_u32 s54, s54, 0x83a0\n"
    "s_mul_i32 s56, s44, 3072\n"
    "s_add_u32 s56, s56, 0x216c0\n"
    "v_and_b32 v213, 15, v210\n"
    "v_lshrrev_b32 v214, 4, v210\n"
    "v_lshrrev_b32 v215, 1, v214\n"
    "v_and_b32 v216, 1, v214\n"
    "v_lshrrev_b32 v220, 3, v213\n"
    "v_cmp_eq_u32_e64 s[58:59], v220, v216\n"
    "v_and_b32 v220, 7, v213\n"
    "v_lshlrev_b32 v220, 1, v220\n"
    "v_lshl_add_u32 v217, v213, 4, v220\n"
    "v_mul_u32_u24 v220, 0xff0, v215\n"
    "s_mul_i32 s47, s44, 8160\n"
    "s_add_u32 s47, s47, 0x10740\n"
    "v_add3_u32 v217, v217, v220, s47\n"
    "s_lshl_b32 s47, s44, 10\n"
    "s_add_u32 s47, s47, 0x25ec0\n"
    "v_lshlrev_b32 v218, 1, v213\n"
    "v_add_u32 v218, s47, v218\n"
    "v_mul_u32_u24 v220, 0x240, v215\n"
    "v_add_u32 v219, v218, v220\n"
    "v_mul_u32_u24 v220, 0x60, v215\n"
    "v_add_u32 v218, v218, v220\n"
    "v_mov_b32 v220, 0x3f80\n"
    "s_mov_b32 s60, -1\n"
    "s_mov_b32 s61, 0\n"
    "s_waitcnt vmcnt(1)\n"
    "ds_read_u16 v226, v218 offset:0\n"
    "ds_read_u16 v227, v218 offset:32\n"
    "ds_read_u16 v228, v218 offset:64\n"
    "ds_read_u16 v229, v218 offset:288\n"
    "ds_read_u16 v230, v218 offset:320\n"
    "ds_read_u16 v231, v218 offset:352\n"
    "ds_read_u16 v232, v218 offset:576\n"
    "ds_read_u16 v233, v218 offset:608\n"
    "ds_read_u16 v234, v218 offset:640\n"
    "ds_read_u16 v235, v218 offset:384\n"
    "ds_read_u16 v236, v218 offset:416\n"
    "ds_read_u16 v237, v218 offset:448\n"
    "ds_read_u16 v238, v219 offset:192\n"
    "ds_read_u16 v239, v219 offset:224\n"
    "ds_read_u16 v240, v219 offset:256\n"
    "s_waitcnt lgkmcnt(5)\n"
    "v_cndmask_b32_e64 v235, v235, v220, s[60:61]\n"
    "s_waitcnt lgkmcnt(4)\n"
    "v_cndmask_b32_e64 v236, v236, v220, s[60:61]\n"
    "s_waitcnt lgkmcnt(3)\n"
    "v_cndmask_b32_e64 v237, v237, v220, s[60:61]\n"
    "s_mov_b64 exec, s[58:59]\n"
    "ds_write_b16 v217, v226 offset:0\n"
    "ds_write_b16 v217, v227 offset:272\n"
    "ds_write_b16 v217, v228 offset:544\n"
    "ds_write_b16 v217, v229 offset:816\n"
    "ds_write_b16 v217, v230 offset:1088\n"
    "ds_write_b16 v217, v231 offset:1360\n"
    "ds_write_b16 v217, v232 offset:1632\n"
    "ds_write_b16 v217, v233 offset:1904\n"
    "ds_write_b16 v217, v234 offset:2176\n"
    "ds_write_b16 v217, v235 offset:2448\n"
    "ds_write_b16 v217, v236 offset:2720\n"
    "ds_write_b16 v217, v237 offset:2992\n"
    "s_waitcnt lgkmcnt(14)\n"
    "ds_write_b16 v217, v238 offset:3264\n"
    "s_waitcnt lgkmcnt(14)\n"
    "ds_write_b16 v217, v239 offset:3536\n"
    "s_waitcnt lgkmcnt(14)\n"
    "ds_write_b16 v217, v240 offset:3808\n"
    "s_mov_b64 exec, -1\n"
    "v_lshlrev_b32 v206, 4, v210\n"
    "v_add_u32 v206, 0x186c0, v206\n"
    "v_add_u32 v207, s62, v206\n"
    "v_add_u32 v208, s63, v206\n"
    "v_mov_b32 v194, 0x3f803f80\n"
    "v_mov_b32 v195, 0x3f803f80\n"
    "v_mov_b32 v196, 0x3f803f80\n"
    "v_mov_b32 v197, 0x3f803f80\n"
    "v_mov_b32 v205, 0\n"
    "v_mov_b32 v209, 0xf149f2ca\n"
    "v_and_b32 v213, 16, v210\n"
    "v_lshlrev_b32 v213, 1, v213\n"
    "v_lshrrev_b32 v214, 2, v210\n"
    "v_lshlrev_b32 v150, 6, v214\n"
    "v_lshlrev_b32 v152, 5, v214\n"
    "v_and_b32 v214, 3, v210\n"
    "v_lshlrev_b32 v214, 3, v214\n"
    "v_add_u32 v152, v152, v214\n"
    "v_add_u32 v152, s54, v152\n"
    "v_xor_b32 v214, v214, v213\n"
    "v_add_u32 v150, v150, v214\n"
    "v_xor_b32 v151, 32, v150\n"
    "v_add_u32 v150, s54, v150\n"
    "v_add_u32 v151, s54, v151\n"
    "v_and_b32 v213, 16, v210\n"
    "v_lshlrev_b32 v213, 1, v213\n"
    "v_lshrrev_b32 v214, 2, v210\n"
    "v_lshlrev_b32 v153, 6, v214\n"
    "v_lshlrev_b32 v155, 5, v214\n"
    "v_and_b32 v214, 3, v210\n"
    "v_lshlrev_b32 v214, 3, v214\n"
    "v_add_u32 v155, v155, v214\n"
    "v_add_u32 v155, s56, v155\n"
    "v_xor_b32 v214, v214, v213\n"
    "v_add_u32 v153, v153, v214\n"
    "v_xor_b32 v154, 32, v153\n"
    "v_add_u32 v153, s56, v153\n"
    "v_add_u32 v154, s56, v154\n"
    "s_waitcnt lgkmcnt(0)\n"
    "s_barrier\n"
    "s_mov_b32 s49, 0x0\n"
    "s_add_u32 m0, s54, 0x0\n"
    "s_nop 0\n"
    "buffer_load_dwordx4 v144, s[24:27], s49 offen lds\n"
    "s_add_u32 m0, s54, 0x400\n"
    "s_nop 0\n"
    "buffer_load_dwordx4 v145, s[24:27], s49 offen lds\n"
    "s_add_u32 m0, s54, 0x800\n"
    "s_nop 0\n"
    "buffer_load_dwordx4 v146, s[24:27], s49 offen lds\n"
    "s_mov_b32 s49, 0x9000\n"
    "s_add_u32 m0, s54, 0xc00\n"
    "s_nop 0\n"
    "buffer_load_dwordx4 v144, s[24:27], s49 offen lds\n"
    "s_add_u32 m0, s54, 0x1000\n"
    "s_nop 0\n"
    "buffer_load_dwordx4 v145, s[24:27], s49 offen lds\n"
    "s_add_u32 m0, s54, 0x1400\n"
    "s_nop 0\n"
    "buffer_load_dwordx4 v146, s[24:27], s49 offen lds\n"
    "s_mov_b32 s49, 0x12000\n"
    "s_add_u32 m0, s56, 0x0\n"
    "s_nop 0\n"
    "buffer_load_dwordx4 v144, s[24:27], s49 offen lds\n"
    "s_add_u32 m0, s56, 0x400\n"
    "s_nop 0\n"
    "buffer_load_dwordx4 v145, s[24:27], s49 offen lds\n"
    "s_add_u32 m0, s56, 0x800\n"
    "s_nop 0\n"
    "buffer_load_dwordx4 v146, s[24:27], s49 offen lds\n"
    "s_setprio 3\n"
    "s_waitcnt vmcnt(6)\n"
    "ds_read_b64_tr_b16 v[0:1], v150 offset:0\n"
    "ds_read_b64_tr_b16 v[2:3], v150 offset:1024\n"
    "ds_read_b64_tr_b16 v[4:5], v151 offset:0\n"
    "ds_read_b64_tr_b16 v[6:7], v151 offset:1024\n"
    "ds_read_b64_tr_b16 v[8:9], v152 offset:2048\n"
    "ds_read_b64_tr_b16 v[10:11], v152 offset:2560\n"
    "s_waitcnt lgkmcnt(0)\n"
    "s_mov_b32 s49, 0x1b000\n"
    "s_add_u32 m0, s54, 0x0\n"
    "s_nop 0\n"
    "buffer_load_dwordx4 v144, s[24:27], s49 offen lds\n"
    "s_add_u32 m0, s54, 0x400\n"
    "s_nop 0\n"
    "buffer_load_dwordx4 v145, s[24:27], s49 offen lds\n"
    "s_add_u32 m0, s54, 0x800\n"
    "s_nop 0\n"
    "buffer_load_dwordx4 v146, s[24:27], s49 offen lds\n"
    "s_waitcnt vmcnt(6)\n"
    "ds_read_b64_tr_b16 v[12:13], v150 offset:3072\n"
    "ds_read_b64_tr_b16 v[14:15], v150 offset:4096\n"
    "ds_read_b64_tr_b16 v[16:17], v151 offset:3072\n"
    "ds_read_b64_tr_b16 v[18:19], v151 offset:4096\n"
    "ds_read_b64_tr_b16 v[20:21], v152 offset:5120\n"
    "ds_read_b64_tr_b16 v[22:23], v152 offset:5632\n"
    "s_waitcnt lgkmcnt(0)\n"
    "s_mov_b32 s49, 0x24000\n"
    "s_add_u32 m0, s54, 0xc00\n"
    "s_nop 0\n"
    "buffer_load_dwordx4 v144, s[24:27], s49 offen lds\n"
    "s_add_u32 m0, s54, 0x1000\n"
    "s_nop 0\n"
    "buffer_load_dwordx4 v145, s[24:27], s49 offen lds\n"
    "s_add_u32 m0, s54, 0x1400\n"
    "s_nop 0\n"
    "buffer_load_dwordx4 v146, s[24:27], s49 offen lds\n"
    "s_waitcnt vmcnt(6)\n"
    "ds_read_b64_tr_b16 v[24:25], v153 offset:0\n"
    "ds_read_b64_tr_b16 v[26:27], v153 offset:1024\n"
    "ds_read_b64_tr_b16 v[28:29], v154 offset:0\n"
    "ds_read_b64_tr_b16 v[30:31], v154 offset:1024\n"
    "ds_read_b64_tr_b16 v[32:33], v155 offset:2048\n"
    "ds_read_b64_tr_b16 v[34:35], v155 offset:2560\n"
    "s_waitcnt lgkmcnt(0)\n"
    "s_mov_b32 s49, 0x2d000\n"
    "s_add_u32 m0, s56, 0x0\n"
    "s_nop 0\n"
    "buffer_load_dwordx4 v144, s[24:27], s49 offen lds\n"
    "s_add_u32 m0, s56, 0x400\n"
    "s_nop 0\n"
    "buffer_load_dwordx4 v145, s[24:27], s49 offen lds\n"
    "s_add_u32 m0, s56, 0x800\n"
    "s_nop 0\n"
    "buffer_load_dwordx4 v146, s[24:27], s49 offen lds\n"
    "s_waitcnt vmcnt(6)\n"
    "ds_read_b64_tr_b16 v[36:37], v150 offset:0\n"
    "ds_read_b64_tr_b16 v[38:39], v150 offset:1024\n"
    "ds_read_b64_tr_b16 v[40:41], v151 offset:0\n"
    "ds_read_b64_tr_b16 v[42:43], v151 offset:1024\n"
    "ds_read_b64_tr_b16 v[44:45], v152 offset:2048\n"
    "ds_read_b64_tr_b16 v[46:47], v152 offset:2560\n"
    "s_waitcnt lgkmcnt(0)\n"
    "s_mov_b32 s49, 0x180\n"
    "s_add_u32 m0, s54, 0x0\n"
    "s_nop 0\n"
    "buffer_load_dwordx4 v144, s[24:27], s49 offen lds\n"
    "s_add_u32 m0, s54, 0x400\n"
    "s_nop 0\n"
    "buffer_load_dwordx4 v145, s[24:27], s49 offen lds\n"
    "s_add_u32 m0, s54, 0x800\n"
    "s_nop 0\n"
    "buffer_load_dwordx4 v146, s[24:27], s49 offen lds\n"
    "s_waitcnt vmcnt(6)\n"
    "ds_read_b64_tr_b16 v[48:49], v150 offset:3072\n"
    "ds_read_b64_tr_b16 v[50:51], v150 offset:4096\n"
    "ds_read_b64_tr_b16 v[52:53], v151 offset:3072\n"
    "ds_read_b64_tr_b16 v[54:55], v151 offset:4096\n"
    "ds_read_b64_tr_b16 v[56:57], v152 offset:5120\n"
    "ds_read_b64_tr_b16 v[58:59], v152 offset:5632\n"
    "s_waitcnt lgkmcnt(0)\n"
    "s_mov_b32 s49, 0x9180\n"
    "s_add_u32 m0, s54, 0xc00\n"
    "s_nop 0\n"
    "buffer_load_dwordx4 v144, s[24:27], s49 offen lds\n"
    "s_add_u32 m0, s54, 0x1000\n"
    "s_nop 0\n"
    "buffer_load_dwordx4 v145, s[24:27], s49 offen lds\n"
    "s_add_u32 m0, s54, 0x1400\n"
    "s_nop 0\n"
    "buffer_load_dwordx4 v146, s[24:27], s49 offen lds\n"
    "s_waitcnt vmcnt(6)\n"
    "ds_read_b64_tr_b16 v[60:61], v153 offset:0\n"
    "ds_read_b64_tr_b16 v[62:63], v153 offset:1024\n"
    "ds_read_b64_tr_b16 v[64:65], v154 offset:0\n"
    "ds_read_b64_tr_b16 v[66:67], v154 offset:1024\n"
    "ds_read_b64_tr_b16 v[68:69], v155 offset:2048\n"
    "ds_read_b64_tr_b16 v[70:71], v155 offset:2560\n"
    "s_waitcnt lgkmcnt(0)\n"
    "s_mov_b32 s49, 0x12180\n"
    "s_add_u32 m0, s56, 0x0\n"
    "s_nop 0\n"
    "buffer_load_dwordx4 v144, s[24:27], s49 offen lds\n"
    "s_add_u32 m0, s56, 0x400\n"
    "s_nop 0\n"
    "buffer_load_dwordx4 v145, s[24:27], s49 offen lds\n"
    "s_add_u32 m0, s56, 0x800\n"
    "s_nop 0\n"
    "buffer_load_dwordx4 v146, s[24:27], s49 offen lds\n"
    "s_waitcnt lgkmcnt(0)\n"
    "s_barrier\n"
    "s_waitcnt vmcnt(6)\n"
    "ds_read_b64_tr_b16 v[72:73], v150 offset:0\n"
    "ds_read_b64_tr_b16 v[74:75], v150 offset:1024\n"
    "ds_read_b64_tr_b16 v[76:77], v151 offset:0\n"
    "ds_read_b64_tr_b16 v[78:79], v151 offset:1024\n"
    "ds_read_b64_tr_b16 v[80:81], v152 offset:2048\n"
    "ds_read_b64_tr_b16 v[82:83], v152 offset:2560\n"
    "s_waitcnt lgkmcnt(0)\n"
    "s_mov_b32 s49, 0x1b180\n"
    "s_add_u32 m0, s54, 0x0\n"
    "s_nop 0\n"
    "buffer_load_dwordx4 v144, s[24:27], s49 offen lds\n"
    "s_add_u32 m0, s54, 0x400\n"
    "s_nop 0\n"
    "buffer_load_dwordx4 v145, s[24:27], s49 offen lds\n"
    "s_add_u32 m0, s54, 0x800\n"
    "s_nop 0\n"
    "buffer_load_dwordx4 v146, s[24:27], s49 offen lds\n"
    "s_waitcnt vmcnt(6)\n"
    "ds_read_b64_tr_b16 v[84:85], v150 offset:3072\n"
    "ds_read_b64_tr_b16 v[86:87], v150 offset:4096\n"
    "ds_read_b64_tr_b16 v[88:89], v151 offset:3072\n"
    "ds_read_b64_tr_b16 v[90:91], v151 offset:4096\n"
    "ds_read_b64_tr_b16 v[92:93], v152 offset:5120\n"
    "ds_read_b64_tr_b16 v[94:95], v152 offset:5632\n"
    "s_waitcnt lgkmcnt(0)\n"
    "s_mov_b32 s49, 0x24180\n"
    "s_add_u32 m0, s54, 0xc00\n"
    "s_nop 0\n"
    "buffer_load_dwordx4 v144, s[24:27], s49 offen lds\n"
    "s_add_u32 m0, s54, 0x1000\n"
    "s_nop 0\n"
    "buffer_load_dwordx4 v145, s[24:27], s49 offen lds\n"
    "s_add_u32 m0, s54, 0x1400\n"
    "s_nop 0\n"
    "buffer_load_dwordx4 v146, s[24:27], s49 offen lds\n"
    "s_waitcnt vmcnt(6)\n"
    "ds_read_b64_tr_b16 v[96:97], v153 offset:0\n"
    "ds_read_b64_tr_b16 v[98:99], v153 offset:1024\n"
    "ds_read_b64_tr_b16 v[100:101], v154 offset:0\n"
    "ds_read_b64_tr_b16 v[102:103], v154 offset:1024\n"
    "ds_read_b64_tr_b16 v[104:105], v155 offset:2048\n"
    "ds_read_b64_tr_b16 v[106:107], v155 offset:2560\n"
    "s_waitcnt lgkmcnt(0)\n"
    "s_mov_b32 s49, 0x2d180\n"
    "s_add_u32 m0, s56, 0x0\n"
    "s_nop 0\n"
    "buffer_load_dwordx4 v144, s[24:27], s49 offen lds\n"
    "s_add_u32 m0, s56, 0x400\n"
    "s_nop 0\n"
    "buffer_load_dwordx4 v145, s[24:27], s49 offen lds\n"
    "s_add_u32 m0, s56, 0x800\n"
    "s_nop 0\n"
    "buffer_load_dwordx4 v146, s[24:27], s49 offen lds\n"
    "s_mov_b64 exec, 1\n"
    "ds_add_u32 v147, v148 offset:0\n"
    "s_mov_b64 exec, -1\n"
    "s_waitcnt vmcnt(6)\n"
    "ds_read_b64_tr_b16 v[108:109], v150 offset:0\n"
    "ds_read_b64_tr_b16 v[110:111], v150 offset:1024\n"
    "ds_read_b64_tr_b16 v[112:113], v151 offset:0\n"
    "ds_read_b64_tr_b16 v[114:115], v151 offset:1024\n"
    "ds_read_b64_tr_b16 v[116:117], v152 offset:2048\n"
    "ds_read_b64_tr_b16 v[118:119], v152 offset:2560\n"
    "s_waitcnt vmcnt(3)\n"
    "ds_read_b64_tr_b16 v[120:121], v150 offset:3072\n"
    "ds_read_b64_tr_b16 v[122:123], v150 offset:4096\n"
    "ds_read_b64_tr_b16 v[124:125], v151 offset:3072\n"
    "ds_read_b64_tr_b16 v[126:127], v151 offset:4096\n"
    "ds_read_b64_tr_b16 v[128:129], v152 offset:5120\n"
    "ds_read_b64_tr_b16 v[130:131], v152 offset:5632\n"
    "s_mov_b64 exec, 1\n"
    "ds_add_u32 v147, v148 offset:0\n"
    "s_mov_b64 exec, -1\n"
    "s_waitcnt vmcnt(0)\n"
    "ds_read_b64_tr_b16 v[132:133], v153 offset:0\n"
    "ds_read_b64_tr_b16 v[134:135], v153 offset:1024\n"
    "ds_read_b64_tr_b16 v[136:137], v154 offset:0\n"
    "ds_read_b64_tr_b16 v[138:139], v154 offset:1024\n"
    "ds_read_b64_tr_b16 v[140:141], v155 offset:2048\n"
    "ds_read_b64_tr_b16 v[142:143], v155 offset:2560\n"
    "s_waitcnt lgkmcnt(0)\n"
    "s_barrier\n"
    "s_setprio 3\n"
    "ds_read_b128 v[234:237], v206 offset:49152\n"
    "ds_read_b128 v[238:241], v206 offset:50176\n"
    "ds_read_b128 v[242:245], v206 offset:51200\n"
    "ds_read_b128 v[246:249], v206 offset:52224\n"
    "ds_read_b128 v[250:253], v206 offset:53248\n"
    "s_mov_b32 s60, 0xffff\n"
    "s_mov_b32 s61, 0x0\n"
    "s_setprio 0\n"
    "v_cndmask_b32_e64 v192, v209, 0, s[60:61]\n"
    "s_mov_b32 s60, 0xffff0000\n"
    "s_mov_b32 s61, 0x0\n"
    "s_setprio 1\n"
    "s_waitcnt lgkmcnt(4)\n"
    "v_mfma_f32_16x16x32_bf16 v[230:233], v[234:237], v[80:83], 0\n"
    "v_mfma_f32_16x16x32_bf16 v[210:213], v[0:3], v[234:237], 0\n"
    "v_mfma_f32_16x16x32_bf16 v[222:225], v[234:237], v[72:75], 0\n"
    "v_mfma_f32_16x16x32_bf16 v[214:217], v[4:7], v[234:237], 0\n"
    "s_waitcnt lgkmcnt(3)\n"
    "v_mfma_f32_16x16x32_bf16 v[230:233], v[238:241], v[92:95], v[230:233]\n"
    "v_mfma_f32_16x16x32_bf16 v[226:229], v[234:237], v[76:79], 0\n"
    "v_mfma_f32_16x16x32_bf16 v[218:221], v[8:11], v[234:237], 0\n"
    "s_setprio 3\n"
    "ds_read_b128 v[234:237], v206 offset:54272\n"
    "s_setprio 1\n"
    "v_mfma_f32_16x16x32_bf16 v[210:213], v[12:15], v[238:241], v[210:213]\n"
    "v_mfma_f32_16x16x32_bf16 v[222:225], v[238:241], v[84:87], v[222:225]\n"
    "v_mfma_f32_16x16x32_bf16 v[214:217], v[16:19], v[238:241], v[214:217]\n"
    "s_waitcnt lgkmcnt(3)\n"
    "v_mfma_f32_16x16x32_bf16 v[230:233], v[242:245], v[104:107], v[230:233]\n"
    "v_mfma_f32_16x16x32_bf16 v[226:229], v[238:241], v[88:91], v[226:229]\n"
    "v_mfma_f32_16x16x32_bf16 v[218:221], v[20:23], v[238:241], v[218:221]\n"
    "s_setprio 3\n"
    "ds_read_b128 v[238:241], v206 offset:0\n"
    "s_setprio 1\n"
    "v_mfma_f32_16x16x32_bf16 v[210:213], v[24:27], v[242:245], v[210:213]\n"
    "v_mfma_f32_16x16x32_bf16 v[222:225], v[242:245], v[96:99], v[222:225]\n"
    "v_mfma_f32_16x16x32_bf16 v[214:217], v[28:31], v[242:245], v[214:217]\n"
    "s_waitcnt lgkmcnt(3)\n"
    "v_mfma_f32_16x16x32_bf16 v[230:233], v[246:249], v[116:119], v[230:233]\n"
    "v_mfma_f32_16x16x32_bf16 v[226:229], v[242:245], v[100:103], v[226:229]\n"
    "v_mfma_f32_16x16x32_bf16 v[218:221], v[32:35], v[242:245], v[218:221]\n"
    "s_setprio 3\n"
    "ds_read_b128 v[242:245], v206 offset:1024\n"
    "s_setprio 1\n"
    "v_mfma_f32_16x16x32_bf16 v[210:213], v[36:39], v[246:249], v[210:213]\n"
    "v_mfma_f32_16x16x32_bf16 v[222:225], v[246:249], v[108:111], v[222:225]\n"
    "v_mfma_f32_16x16x32_bf16 v[214:217], v[40:43], v[246:249], v[214:217]\n"
    "s_waitcnt lgkmcnt(3)\n"
    "v_mfma_f32_16x16x32_bf16 v[230:233], v[250:253], v[128:131], v[230:233]\n"
    "v_mfma_f32_16x16x32_bf16 v[226:229], v[246:249], v[112:115], v[226:229]\n"
    "v_mfma_f32_16x16x32_bf16 v[218:221], v[44:47], v[246:249], v[218:221]\n"
    "s_setprio 3\n"
    "ds_read_b128 v[246:249], v206 offset:2048\n"
    "s_setprio 1\n"
    "v_mfma_f32_16x16x32_bf16 v[210:213], v[48:51], v[250:253], v[210:213]\n"
    "v_mfma_f32_16x16x32_bf16 v[222:225], v[250:253], v[120:123], v[222:225]\n"
    "v_mfma_f32_16x16x32_bf16 v[214:217], v[52:55], v[250:253], v[214:217]\n"
    "s_waitcnt lgkmcnt(3)\n"
    "v_mfma_f32_16x16x32_bf16 v[230:233], v[234:237], v[140:143], v[230:233]\n"
    "v_mfma_f32_16x16x32_bf16 v[226:229], v[250:253], v[124:127], v[226:229]\n"
    "v_mfma_f32_16x16x32_bf16 v[218:221], v[56:59], v[250:253], v[218:221]\n"
    "s_setprio 3\n"
    "ds_read_b128 v[250:253], v206 offset:3072\n"
    "s_setprio 1\n"
    "v_mfma_f32_16x16x32_bf16 v[210:213], v[60:63], v[234:237], v[210:213]\n"
    "v_mfma_f32_16x16x32_bf16 v[222:225], v[234:237], v[132:135], v[222:225]\n"
    "v_mfma_f32_16x16x32_bf16 v[214:217], v[64:67], v[234:237], v[214:217]\n"
    "v_cvt_pk_bf16_f32 v190, v230, v231\n"
    "v_mfma_f32_16x16x32_bf16 v[226:229], v[234:237], v[136:139], v[226:229]\n"
    "v_cvt_pk_bf16_f32 v191, v232, v233\n"
    "v_mfma_f32_16x16x32_bf16 v[218:221], v[68:71], v[234:237], v[218:221]\n"
    "s_setprio 3\n"
    "ds_read_b128 v[234:237], v206 offset:4096\n"
    "s_setprio 1\n"
    "v_cvt_pk_bf16_f32 v162, v210, v211\n"
    "v_cvt_pk_bf16_f32 v163, v212, v213\n"
    "s_waitcnt lgkmcnt(4)\n"
    "v_mfma_f32_16x16x32_bf16 v[230:233], v[238:241], v[80:83], 0\n"
    "v_cvt_pk_bf16_f32 v174, v222, v223\n"
    "v_cvt_pk_bf16_f32 v175, v224, v225\n"
    "v_mfma_f32_16x16x32_bf16 v[210:213], v[0:3], v[238:241], 0\n"
    "v_cvt_pk_bf16_f32 v164, v214, v215\n"
    "v_cvt_pk_bf16_f32 v165, v216, v217\n"
    "v_mfma_f32_16x16x32_bf16 v[222:225], v[238:241], v[72:75], 0\n"
    "v_cvt_pk_bf16_f32 v182, v226, v227\n"
    "v_cvt_pk_bf16_f32 v183, v228, v229\n"
    "v_mfma_f32_16x16x32_bf16 v[214:217], v[4:7], v[238:241], 0\n"
    "v_cvt_pk_bf16_f32 v166, v218, v219\n"
    "v_cvt_pk_bf16_f32 v167, v220, v221\n"
    "s_waitcnt lgkmcnt(3)\n"
    "v_mfma_f32_16x16x32_bf16 v[230:233], v[242:245], v[92:95], v[230:233]\n"
    "v_mfma_f32_16x16x32_bf16 v[226:229], v[238:241], v[76:79], 0\n"
    "v_mfma_f32_16x16x32_bf16 v[218:221], v[8:11], v[238:241], 0\n"
    "s_setprio 3\n"
    "ds_read_b128 v[238:241], v206 offset:5120\n"
    "s_setprio 1\n"
    "v_mfma_f32_16x16x32_bf16 v[210:213], v[12:15], v[242:245], v[210:213]\n"
    "v_mfma_f32_16x16x32_bf16 v[222:225], v[242:245], v[84:87], v[222:225]\n"
    "v_mfma_f32_16x16x32_bf16 v[214:217], v[16:19], v[242:245], v[214:217]\n"
    "s_waitcnt lgkmcnt(3)\n"
    "v_mfma_f32_16x16x32_bf16 v[230:233], v[246:249], v[104:107], v[230:233]\n"
    "v_mfma_f32_16x16x32_bf16 v[226:229], v[242:245], v[88:91], v[226:229]\n"
    "v_mfma_f32_16x16x32_bf16 v[218:221], v[20:23], v[242:245], v[218:221]\n"
    "s_setprio 3\n"
    "ds_read_b128 v[242:245], v206 offset:6144\n"
    "s_setprio 1\n"
    "v_mfma_f32_16x16x32_bf16 v[210:213], v[24:27], v[246:249], v[210:213]\n"
    "v_mfma_f32_16x16x32_bf16 v[222:225], v[246:249], v[96:99], v[222:225]\n"
    "v_mfma_f32_16x16x32_bf16 v[214:217], v[28:31], v[246:249], v[214:217]\n"
    "s_waitcnt lgkmcnt(3)\n"
    "v_mfma_f32_16x16x32_bf16 v[230:233], v[250:253], v[116:119], v[230:233]\n"
    "v_mfma_f32_16x16x32_bf16 v[226:229], v[246:249], v[100:103], v[226:229]\n"
    "v_mfma_f32_16x16x32_bf16 v[218:221], v[32:35], v[246:249], v[218:221]\n"
    "s_setprio 3\n"
    "ds_read_b128 v[246:249], v206 offset:7168\n"
    "s_setprio 1\n"
    "v_mfma_f32_16x16x32_bf16 v[210:213], v[36:39], v[250:253], v[210:213]\n"
    "v_mfma_f32_16x16x32_bf16 v[222:225], v[250:253], v[108:111], v[222:225]\n"
    "v_mfma_f32_16x16x32_bf16 v[214:217], v[40:43], v[250:253], v[214:217]\n"
    "s_waitcnt lgkmcnt(3)\n"
    "v_mfma_f32_16x16x32_bf16 v[230:233], v[234:237], v[128:131], v[230:233]\n"
    "v_mfma_f32_16x16x32_bf16 v[226:229], v[250:253], v[112:115], v[226:229]\n"
    "v_mfma_f32_16x16x32_bf16 v[218:221], v[44:47], v[250:253], v[218:221]\n"
    "s_setprio 3\n"
    "ds_read_b128 v[250:253], v206 offset:8192\n"
    "s_setprio 1\n"
    "v_mfma_f32_16x16x32_bf16 v[210:213], v[48:51], v[234:237], v[210:213]\n"
    "v_mfma_f32_16x16x32_bf16 v[222:225], v[234:237], v[120:123], v[222:225]\n"
    "v_mfma_f32_16x16x32_bf16 v[214:217], v[52:55], v[234:237], v[214:217]\n"
    "s_waitcnt lgkmcnt(3)\n"
    "v_mfma_f32_16x16x32_bf16 v[230:233], v[238:241], v[140:143], v[230:233]\n"
    "v_mfma_f32_16x16x32_bf16 v[226:229], v[234:237], v[124:127], v[226:229]\n"
    "v_mfma_f32_16x16x32_bf16 v[218:221], v[56:59], v[234:237], v[218:221]\n"
    "s_setprio 3\n"
    "ds_read_b128 v[234:237], v206 offset:9216\n"
    "s_setprio 1\n"
    "v_mfma_f32_16x16x32_bf16 v[210:213], v[60:63], v[238:241], v[210:213]\n"
    "v_mfma_f32_16x16x32_bf16 v[222:225], v[238:241], v[132:135], v[222:225]\n"
    "v_mfma_f32_16x16x32_bf16 v[214:217], v[64:67], v[238:241], v[214:217]\n"
    "v_cvt_pk_bf16_f32 v184, v230, v231\n"
    "v_mfma_f32_16x16x32_bf16 v[226:229], v[238:241], v[136:139], v[226:229]\n"
    "v_cvt_pk_bf16_f32 v185, v232, v233\n"
    "v_mfma_f32_16x16x32_bf16 v[218:221], v[68:71], v[238:241], v[218:221]\n"
    "s_setprio 3\n"
    "ds_read_b128 v[238:241], v206 offset:10240\n"
    "s_setprio 1\n"
    "v_cvt_pk_bf16_f32 v144, v210, v211\n"
    "v_cvt_pk_bf16_f32 v145, v212, v213\n"
    "s_waitcnt lgkmcnt(4)\n"
    "v_mfma_f32_16x16x32_bf16 v[230:233], v[242:245], v[80:83], 0\n"
    "v_cvt_pk_bf16_f32 v168, v222, v223\n"
    "v_cvt_pk_bf16_f32 v169, v224, v225\n"
    "v_mfma_f32_16x16x32_bf16 v[210:213], v[0:3], v[242:245], 0\n"
    "v_cvt_pk_bf16_f32 v146, v214, v215\n"
    "v_cvt_pk_bf16_f32 v147, v216, v217\n"
    "v_mfma_f32_16x16x32_bf16 v[222:225], v[242:245], v[72:75], 0\n"
    "v_cvt_pk_bf16_f32 v176, v226, v227\n"
    "v_cvt_pk_bf16_f32 v177, v228, v229\n"
    "v_mfma_f32_16x16x32_bf16 v[214:217], v[4:7], v[242:245], 0\n"
    "v_cvt_pk_bf16_f32 v148, v218, v219\n"
    "v_cvt_pk_bf16_f32 v149, v220, v221\n"
    "s_waitcnt lgkmcnt(3)\n"
    "v_mfma_f32_16x16x32_bf16 v[230:233], v[246:249], v[92:95], v[230:233]\n"
    "v_mfma_f32_16x16x32_bf16 v[226:229], v[242:245], v[76:79], 0\n"
    "v_mfma_f32_16x16x32_bf16 v[218:221], v[8:11], v[242:245], 0\n"
    "s_setprio 3\n"
    "ds_read_b128 v[242:245], v206 offset:11264\n"
    "s_setprio 1\n"
    "v_mfma_f32_16x16x32_bf16 v[210:213], v[12:15], v[246:249], v[210:213]\n"
    "v_mfma_f32_16x16x32_bf16 v[222:225], v[246:249], v[84:87], v[222:225]\n"
    "v_mfma_f32_16x16x32_bf16 v[214:217], v[16:19], v[246:249], v[214:217]\n"
    "s_waitcnt lgkmcnt(3)\n"
    "v_mfma_f32_16x16x32_bf16 v[230:233], v[250:253], v[104:107], v[230:233]\n"
    "v_mfma_f32_16x16x32_bf16 v[226:229], v[246:249], v[88:91], v[226:229]\n"
    "v_mfma_f32_16x16x32_bf16 v[218:221], v[20:23], v[246:249], v[218:221]\n"
    "s_setprio 3\n"
    "ds_read_b128 v[246:249], v206 offset:12288\n"
    "s_setprio 1\n"
    "v_mfma_f32_16x16x32_bf16 v[210:213], v[24:27], v[250:253], v[210:213]\n"
    "v_mfma_f32_16x16x32_bf16 v[222:225], v[250:253], v[96:99], v[222:225]\n"
    "v_mfma_f32_16x16x32_bf16 v[214:217], v[28:31], v[250:253], v[214:217]\n"
    "s_waitcnt lgkmcnt(3)\n"
    "v_mfma_f32_16x16x32_bf16 v[230:233], v[234:237], v[116:119], v[230:233]\n"
    "v_mfma_f32_16x16x32_bf16 v[226:229], v[250:253], v[100:103], v[226:229]\n"
    "v_mfma_f32_16x16x32_bf16 v[218:221], v[32:35], v[250:253], v[218:221]\n"
    "s_setprio 3\n"
    "ds_read_b128 v[250:253], v206 offset:13312\n"
    "s_setprio 1\n"
    "v_mfma_f32_16x16x32_bf16 v[210:213], v[36:39], v[234:237], v[210:213]\n"
    "v_mfma_f32_16x16x32_bf16 v[222:225], v[234:237], v[108:111], v[222:225]\n"
    "v_mfma_f32_16x16x32_bf16 v[214:217], v[40:43], v[234:237], v[214:217]\n"
    "s_waitcnt lgkmcnt(3)\n"
    "v_mfma_f32_16x16x32_bf16 v[230:233], v[238:241], v[128:131], v[230:233]\n"
    "v_mfma_f32_16x16x32_bf16 v[226:229], v[234:237], v[112:115], v[226:229]\n"
    "v_mfma_f32_16x16x32_bf16 v[218:221], v[44:47], v[234:237], v[218:221]\n"
    "s_setprio 3\n"
    "ds_read_b128 v[234:237], v206 offset:14336\n"
    "s_setprio 1\n"
    "v_mfma_f32_16x16x32_bf16 v[210:213], v[48:51], v[238:241], v[210:213]\n"
    "v_mfma_f32_16x16x32_bf16 v[222:225], v[238:241], v[120:123], v[222:225]\n"
    "v_mfma_f32_16x16x32_bf16 v[214:217], v[52:55], v[238:241], v[214:217]\n"
    "s_waitcnt lgkmcnt(3)\n"
    "v_mfma_f32_16x16x32_bf16 v[230:233], v[242:245], v[140:143], v[230:233]\n"
    "v_mfma_f32_16x16x32_bf16 v[226:229], v[238:241], v[124:127], v[226:229]\n"
    "v_mfma_f32_16x16x32_bf16 v[218:221], v[56:59], v[238:241], v[218:221]\n"
    "s_setprio 3\n"
    "ds_read_b128 v[238:241], v206 offset:15360\n"
    "s_setprio 1\n"
    "v_mfma_f32_16x16x32_bf16 v[210:213], v[60:63], v[242:245], v[210:213]\n"
    "v_mfma_f32_16x16x32_bf16 v[222:225], v[242:245], v[132:135], v[222:225]\n"
    "v_mfma_f32_16x16x32_bf16 v[214:217], v[64:67], v[242:245], v[214:217]\n"
    "v_cvt_pk_bf16_f32 v186, v230, v231\n"
    "v_mfma_f32_16x16x32_bf16 v[226:229], v[242:245], v[136:139], v[226:229]\n"
    "v_cvt_pk_bf16_f32 v187, v232, v233\n"
    "v_mfma_f32_16x16x32_bf16 v[218:221], v[68:71], v[242:245], v[218:221]\n"
    "s_setprio 3\n"
    "ds_read_b128 v[242:245], v206 offset:16384\n"
    "s_setprio 1\n"
    "v_cvt_pk_bf16_f32 v150, v210, v211\n"
    "v_cvt_pk_bf16_f32 v151, v212, v213\n"
    "s_waitcnt lgkmcnt(4)\n"
    "v_mfma_f32_16x16x32_bf16 v[230:233], v[246:249], v[80:83], 0\n"
    "v_cvt_pk_bf16_f32 v170, v222, v223\n"
    "v_cvt_pk_bf16_f32 v171, v224, v225\n"
    "v_mfma_f32_16x16x32_bf16 v[210:213], v[0:3], v[246:249], 0\n"
    "v_cvt_pk_bf16_f32 v152, v214, v215\n"
    "v_cvt_pk_bf16_f32 v153, v216, v217\n"
    "v_mfma_f32_16x16x32_bf16 v[222:225], v[246:249], v[72:75], 0\n"
    "v_cvt_pk_bf16_f32 v178, v226, v227\n"
    "v_cvt_pk_bf16_f32 v179, v228, v229\n"
    "v_mfma_f32_16x16x32_bf16 v[214:217], v[4:7], v[246:249], 0\n"
    "v_cvt_pk_bf16_f32 v154, v218, v219\n"
    "v_cvt_pk_bf16_f32 v155, v220, v221\n"
    "s_waitcnt lgkmcnt(3)\n"
    "v_mfma_f32_16x16x32_bf16 v[230:233], v[250:253], v[92:95], v[230:233]\n"
    "v_mfma_f32_16x16x32_bf16 v[226:229], v[246:249], v[76:79], 0\n"
    "v_mfma_f32_16x16x32_bf16 v[218:221], v[8:11], v[246:249], 0\n"
    "s_setprio 3\n"
    "ds_read_b128 v[246:249], v206 offset:17408\n"
    "s_setprio 1\n"
    "v_mfma_f32_16x16x32_bf16 v[210:213], v[12:15], v[250:253], v[210:213]\n"
    "v_mfma_f32_16x16x32_bf16 v[222:225], v[250:253], v[84:87], v[222:225]\n"
    "v_mfma_f32_16x16x32_bf16 v[214:217], v[16:19], v[250:253], v[214:217]\n"
    "s_waitcnt lgkmcnt(3)\n"
    "v_mfma_f32_16x16x32_bf16 v[230:233], v[234:237], v[104:107], v[230:233]\n"
    "v_mfma_f32_16x16x32_bf16 v[226:229], v[250:253], v[88:91], v[226:229]\n"
    "v_mfma_f32_16x16x32_bf16 v[218:221], v[20:23], v[250:253], v[218:221]\n"
    "v_mfma_f32_16x16x32_bf16 v[210:213], v[24:27], v[234:237], v[210:213]\n"
    "v_mfma_f32_16x16x32_bf16 v[222:225], v[234:237], v[96:99], v[222:225]\n"
    "v_mfma_f32_16x16x32_bf16 v[214:217], v[28:31], v[234:237], v[214:217]\n"
    "s_waitcnt lgkmcnt(2)\n"
    "v_mfma_f32_16x16x32_bf16 v[230:233], v[238:241], v[116:119], v[230:233]\n"
    "v_mfma_f32_16x16x32_bf16 v[226:229], v[234:237], v[100:103], v[226:229]\n"
    "v_mfma_f32_16x16x32_bf16 v[218:221], v[32:35], v[234:237], v[218:221]\n"
    "v_mfma_f32_16x16x32_bf16 v[210:213], v[36:39], v[238:241], v[210:213]\n"
    "v_mfma_f32_16x16x32_bf16 v[222:225], v[238:241], v[108:111], v[222:225]\n"
    "v_mfma_f32_16x16x32_bf16 v[214:217], v[40:43], v[238:241], v[214:217]\n"
    "s_waitcnt lgkmcnt(1)\n"
    "v_mfma_f32_16x16x32_bf16 v[230:233], v[242:245], v[128:131], v[230:233]\n"
    "v_mfma_f32_16x16x32_bf16 v[226:229], v[238:241], v[112:115], v[226:229]\n"
    "v_mfma_f32_16x16x32_bf16 v[218:221], v[44:47], v[238:241], v[218:221]\n"
    "v_mfma_f32_16x16x32_bf16 v[210:213], v[48:51], v[242:245], v[210:213]\n"
    "v_mfma_f32_16x16x32_bf16 v[222:225], v[242:245], v[120:123], v[222:225]\n"
    "v_mfma_f32_16x16x32_bf16 v[214:217], v[52:55], v[242:245], v[214:217]\n"
    "s_waitcnt lgkmcnt(0)\n"
    "v_mfma_f32_16x16x32_bf16 v[230:233], v[246:249], v[140:143], v[230:233]\n"
    "s_barrier\n"
    "s_setprio 2\n"
    "ds_read_b64 v[252:253], v208 offset:24576\n"
    "s_setprio 1\n"
    "v_mfma_f32_16x16x32_bf16 v[226:229], v[242:245], v[124:127], v[226:229]\n"
    "v_mfma_f32_16x16x32_bf16 v[218:221], v[56:59], v[242:245], v[218:221]\n"
    "s_setprio 2\n"
    "ds_read_b128 v[242:245], v207 offset:18432\n"
    "s_setprio 1\n"
    "v_mfma_f32_16x16x32_bf16 v[210:213], v[60:63], v[246:249], v[210:213]\n"
    "v_cvt_pk_bf16_f32 v188, v230, v231\n"
    "v_mfma_f32_16x16x32_bf16 v[222:225], v[246:249], v[132:135], v[222:225]\n"
    "v_cvt_pk_bf16_f32 v189, v232, v233\n"
    "v_mfma_f32_16x16x32_bf16 v[214:217], v[64:67], v[246:249], v[214:217]\n"
    "s_nop 3\n"
    "v_cvt_pk_bf16_f32 v156, v210, v211\n"
    "v_mfma_f32_16x16x32_bf16 v[218:221], v[68:71], v[246:249], v[218:221]\n"
    "v_cvt_pk_bf16_f32 v157, v212, v213\n"
    "v_mfma_f32_16x16x32_bf16 v[226:229], v[246:249], v[136:139], v[226:229]\n"
    "s_setprio 2\n"
    "ds_read_b128 v[248:251], v207 offset:24576\n"
    "ds_read_b64 v[246:247], v208 offset:18432\n"
    "s_setprio 1\n"
    "v_cvt_pk_bf16_f32 v172, v222, v223\n"
    "s_setprio 2\n"
    "s_waitcnt lgkmcnt(2)\n"
    "v_mfma_f32_16x16x32_bf16 v[210:213], v[144:147], v[242:245], 0\n"
    "s_setprio 1\n"
    "v_cvt_pk_bf16_f32 v173, v224, v225\n"
    "v_cvt_pk_bf16_f32 v158, v214, v215\n"
    "s_setprio 2\n"
    "v_mfma_f32_16x16x32_bf16 v[222:225], v[162:165], v[242:245], v[192:195]\n"
    "s_setprio 1\n"
    "v_cvt_pk_bf16_f32 v159, v216, v217\n"
    "v_cvt_pk_bf16_f32 v160, v218, v219\n"
    "s_setprio 2\n"
    "v_mfma_f32_16x16x32_bf16 v[214:217], v[150:153], v[242:245], 0\n"
    "s_setprio 1\n"
    "v_cvt_pk_bf16_f32 v161, v220, v221\n"
    "v_cvt_pk_bf16_f32 v180, v226, v227\n"
    "s_setprio 2\n"
    "v_mfma_f32_16x16x32_bf16 v[218:221], v[156:159], v[242:245], 0\n"
    "ds_read_b128 v[242:245], v207 offset:30720\n"
    "s_setprio 1\n"
    "v_cvt_pk_bf16_f32 v181, v228, v229\n"
    "s_setprio 2\n"
    "s_waitcnt lgkmcnt(2)\n"
    "v_mfma_f32_16x16x32_bf16 v[226:229], v[144:147], v[248:251], 0\n"
    "v_mfma_f32_16x16x32_bf16 v[238:241], v[162:165], v[248:251], v[192:195]\n"
    "s_waitcnt lgkmcnt(1)\n"
    "v_mfma_f32_16x16x16_bf16 v[210:213], v[148:149], v[246:247], v[210:213]\n"
    "v_mfma_f32_16x16x16_bf16 v[222:225], v[166:167], v[246:247], v[222:225]\n"
    "v_mfma_f32_16x16x32_bf16 v[230:233], v[150:153], v[248:251], 0\n"
    "v_mfma_f32_16x16x16_bf16 v[214:217], v[154:155], v[246:247], v[214:217]\n"
    "v_mfma_f32_16x16x32_bf16 v[234:237], v[156:159], v[248:251], 0\n"
    "ds_read_b128 v[248:251], v207 offset:55296\n"
    "v_mfma_f32_16x16x16_bf16 v[218:221], v[160:161], v[246:247], v[218:221]\n"
    "ds_read_b64 v[246:247], v208 offset:30720\n"
    "v_mfma_f32_16x16x16_bf16 v[226:229], v[148:149], v[252:253], v[226:229]\n"
    "s_nop 0\n"
    "v_max3_f32 v223, v222, v210, v211\n"
    "v_mfma_f32_16x16x16_bf16 v[238:241], v[166:167], v[252:253], v[238:241]\n"
    "v_max3_f32 v224, v212, v213, v214\n"
    "v_max3_f32 v223, v223, v215, v216\n"
    "v_mfma_f32_16x16x16_bf16 v[230:233], v[154:155], v[252:253], v[230:233]\n"
    "v_max3_f32 v224, v224, v217, v218\n"
    "v_max3_f32 v223, v223, v219, v220\n"
    "v_mfma_f32_16x16x16_bf16 v[234:237], v[160:161], v[252:253], v[234:237]\n"
    "ds_read_b64 v[252:253], v208 offset:55296\n"
    "s_setprio 0\n"
    "v_max3_f32 v223, v223, v224, v221\n"
    "v_mov_b32 v224, v223\n"
    "v_max3_f32 v239, v238, v226, v227\n"
    "v_max3_f32 v240, v228, v229, v230\n"
    "v_max3_f32 v239, v239, v231, v232\n"
    "v_permlane32_swap_b32 v223, v224\n"
    "v_max3_f32 v240, v240, v233, v234\n"
    "v_max3_f32 v239, v239, v235, v236\n"
    "v_max3_f32 v239, v239, v240, v237\n"
    "v_max_f32 v223, v223, v224\n"
    "v_mov_b32 v240, v239\n"
    "v_mov_b32 v224, v223\n"
    "s_nop 0\n"
    "v_permlane32_swap_b32 v239, v240\n"
    "v_permlane16_swap_b32 v223, v224\n"
    "v_max_f32 v239, v239, v240\n"
    "v_max_f32 v223, v223, v224\n"
    "v_mov_b32 v240, v239\n"
    "v_mul_f32 v223, 0xbe553b95, v223\n"
    "v_fmamk_f32 v210, v210, 0x3e553b95, v223\n"
    "v_fmamk_f32 v211, v211, 0x3e553b95, v223\n"
    "v_fmamk_f32 v212, v212, 0x3e553b95, v223\n"
    "v_fmamk_f32 v213, v213, 0x3e553b95, v223\n"
    "v_fmamk_f32 v214, v214, 0x3e553b95, v223\n"
    "v_fmamk_f32 v215, v215, 0x3e553b95, v223\n"
    "v_fmamk_f32 v216, v216, 0x3e553b95, v223\n"
    "v_fmamk_f32 v217, v217, 0x3e553b95, v223\n"
    "v_permlane16_swap_b32 v239, v240\n"
    "v_exp_f32 v210, v210\n"
    "v_exp_f32 v211, v211\n"
    "v_exp_f32 v212, v212\n"
    "v_exp_f32 v213, v213\n"
    "v_exp_f32 v214, v214\n"
    "v_exp_f32 v215, v215\n"
    "v_exp_f32 v216, v216\n"
    "v_exp_f32 v217, v217\n"
    "v_fmamk_f32 v218, v218, 0x3e553b95, v223\n"
    "v_fmamk_f32 v219, v219, 0x3e553b95, v223\n"
    "v_fmamk_f32 v220, v220, 0x3e553b95, v223\n"
    "v_fmamk_f32 v221, v221, 0x3e553b95, v223\n"
    "v_fmamk_f32 v222, v222, 0x3e553b95, v223\n"
    "v_exp_f32 v218, v218\n"
    "v_exp_f32 v219, v219\n"
    "v_exp_f32 v220, v220\n"
    "v_exp_f32 v221, v221\n"
    "v_exp_f32 v222, v222\n"
    "v_max_f32 v239, v239, v240\n"
    "v_cvt_pk_bf16_f32 v198, v210, v211\n"
    "v_cvt_pk_bf16_f32 v199, v212, v213\n"
    "v_cvt_pk_bf16_f32 v200, v214, v215\n"
    "v_cvt_pk_bf16_f32 v201, v216, v217\n"
    "v_mul_f32 v239, 0xbe553b95, v239\n"
    "v_fmamk_f32 v226, v226, 0x3e553b95, v239\n"
    "s_setprio 2\n"
    "v_mfma_f32_16x16x32_bf16 v[210:213], v[168:171], v[198:201], 0\n"
    "v_fmamk_f32 v227, v227, 0x3e553b95, v239\n"
    "v_fmamk_f32 v228, v228, 0x3e553b95, v239\n"
    "v_mfma_f32_16x16x32_bf16 v[214:217], v[176:179], v[198:201], 0\n"
    "s_setprio 0\n"
    "v_fmamk_f32 v229, v229, 0x3e553b95, v239\n"
    "v_fmamk_f32 v230, v230, 0x3e553b95, v239\n"
    "v_fmamk_f32 v231, v231, 0x3e553b95, v239\n"
    "v_fmamk_f32 v232, v232, 0x3e553b95, v239\n"
    "v_fmamk_f32 v233, v233, 0x3e553b95, v239\n"
    "v_cvt_pk_bf16_f32 v202, v218, v219\n"
    "v_cvt_pk_bf16_f32 v203, v220, v221\n"
    "s_setprio 2\n"
    "v_mfma_f32_16x16x32_bf16 v[218:221], v[184:187], v[198:201], 0\n"
    "v_cvt_pk_bf16_f32 v204, v222, 0\n"
    "v_exp_f32 v226, v226\n"
    "v_mfma_f32_16x16x32_bf16 v[222:225], v[194:197], v[198:201], 0\n"
    "v_exp_f32 v227, v227\n"
    "v_mfma_f32_16x16x32_bf16 v[210:213], v[172:175], v[202:205], v[210:213]\n"
    "v_exp_f32 v228, v228\n"
    "v_mfma_f32_16x16x32_bf16 v[214:217], v[180:183], v[202:205], v[214:217]\n"
    "v_exp_f32 v229, v229\n"
    "v_mfma_f32_16x16x32_bf16 v[218:221], v[188:191], v[202:205], v[218:221]\n"
    "v_exp_f32 v230, v230\n"
    "v_mfma_f32_16x16x32_bf16 v[222:225], v[194:197], v[202:205], v[222:225]\n"
    "s_setprio 0\n"
    "v_exp_f32 v231, v231\n"
    "v_exp_f32 v232, v232\n"
    "v_exp_f32 v233, v233\n"
    "v_fmamk_f32 v238, v238, 0x3e553b95, v239\n"
    "v_exp_f32 v238, v238\n"
    "v_fmamk_f32 v234, v234, 0x3e553b95, v239\n"
    "v_fmamk_f32 v235, v235, 0x3e553b95, v239\n"
    "v_fmamk_f32 v236, v236, 0x3e553b95, v239\n"
    "v_fmamk_f32 v237, v237, 0x3e553b95, v239\n"
    "v_cvt_pk_bf16_f32 v198, v226, v227\n"
    "v_cvt_pk_bf16_f32 v199, v228, v229\n"
    "v_cvt_pk_bf16_f32 v200, v230, v231\n"
    "v_cvt_pk_bf16_f32 v201, v232, v233\n"
    "v_exp_f32 v234, v234\n"
    "v_exp_f32 v235, v235\n"
    "s_setprio 2\n"
    "v_mfma_f32_16x16x32_bf16 v[226:229], v[168:171], v[198:201], 0\n"
    "v_exp_f32 v236, v236\n"
    "v_mfma_f32_16x16x32_bf16 v[230:233], v[176:179], v[198:201], 0\n"
    "v_exp_f32 v237, v237\n"
    "v_cvt_pk_bf16_f32 v204, v238, 0\n"
    "v_mfma_f32_16x16x32_bf16 v[238:241], v[194:197], v[198:201], 0\n"
    "v_cvt_pk_bf16_f32 v202, v234, v235\n"
    "v_cvt_pk_bf16_f32 v203, v236, v237\n"
    "v_mfma_f32_16x16x32_bf16 v[234:237], v[184:187], v[198:201], 0\n"
    "v_rcp_f32 v223, v222\n"
    "v_mfma_f32_16x16x32_bf16 v[238:241], v[194:197], v[202:205], v[238:241]\n"
    "v_mul_f32 v210, v210, v223\n"
    "v_mul_f32 v211, v211, v223\n"
    "v_mfma_f32_16x16x32_bf16 v[226:229], v[172:175], v[202:205], v[226:229]\n"
    "v_mul_f32 v212, v212, v223\n"
    "v_mul_f32 v213, v213, v223\n"
    "v_mfma_f32_16x16x32_bf16 v[230:233], v[180:183], v[202:205], v[230:233]\n"
    "v_mul_f32 v214, v214, v223\n"
    "v_mul_f32 v215, v215, v223\n"
    "v_mfma_f32_16x16x32_bf16 v[234:237], v[188:191], v[202:205], v[234:237]\n"
    "s_setprio 0\n"
    "v_mul_f32 v216, v216, v223\n"
    "v_mul_f32 v217, v217, v223\n"
    "v_cvt_pk_bf16_f32 v210, v210, v211\n"
    "v_cvt_pk_bf16_f32 v211, v212, v213\n"
    "v_cvt_pk_bf16_f32 v212, v214, v215\n"
    "v_cvt_pk_bf16_f32 v213, v216, v217\n"
    "v_rcp_f32 v239, v238\n"
    "v_mul_f32 v218, v218, v223\n"
    "v_mul_f32 v219, v219, v223\n"
    "v_mul_f32 v220, v220, v223\n"
    "v_mul_f32 v221, v221, v223\n"
    "s_setprio 2\n"
    "s_waitcnt lgkmcnt(3)\n"
    "v_mfma_f32_16x16x32_bf16 v[222:225], v[162:165], v[242:245], v[192:195]\n"
    "ds_write_b128 v207, v[210:213] offset:18432\n"
    "v_mfma_f32_16x16x32_bf16 v[210:213], v[144:147], v[242:245], 0\n"
    "v_cvt_pk_bf16_f32 v214, v218, v219\n"
    "v_cvt_pk_bf16_f32 v215, v220, v221\n"
    "v_mfma_f32_16x16x32_bf16 v[218:221], v[156:159], v[242:245], 0\n"
    "ds_write_b64 v208, v[214:215] offset:18432\n"
    "v_mfma_f32_16x16x32_bf16 v[214:217], v[150:153], v[242:245], 0\n"
    "v_mul_f32 v226, v226, v239\n"
    "v_mul_f32 v227, v227, v239\n"
    "s_waitcnt lgkmcnt(3)\n"
    "v_mfma_f32_16x16x16_bf16 v[210:213], v[148:149], v[246:247], v[210:213]\n"
    "v_mul_f32 v228, v228, v239\n"
    "v_mul_f32 v229, v229, v239\n"
    "v_mfma_f32_16x16x16_bf16 v[222:225], v[166:167], v[246:247], v[222:225]\n"
    "v_mul_f32 v230, v230, v239\n"
    "v_mul_f32 v231, v231, v239\n"
    "v_mfma_f32_16x16x16_bf16 v[214:217], v[154:155], v[246:247], v[214:217]\n"
    "v_mul_f32 v232, v232, v239\n"
    "v_mul_f32 v233, v233, v239\n"
    "v_mfma_f32_16x16x16_bf16 v[218:221], v[160:161], v[246:247], v[218:221]\n"
    "s_setprio 0\n"
    "v_cvt_pk_bf16_f32 v226, v226, v227\n"
    "v_cvt_pk_bf16_f32 v227, v228, v229\n"
    "v_cvt_pk_bf16_f32 v228, v230, v231\n"
    "v_cvt_pk_bf16_f32 v229, v232, v233\n"
    "v_mul_f32 v234, v234, v239\n"
    "v_mul_f32 v235, v235, v239\n"
    "v_mul_f32 v236, v236, v239\n"
    "v_mul_f32 v237, v237, v239\n"
    "s_setprio 2\n"
    "v_mfma_f32_16x16x32_bf16 v[238:241], v[162:165], v[248:251], v[192:195]\n"
    "ds_write_b128 v207, v[226:229] offset:24576\n"
    "v_mfma_f32_16x16x32_bf16 v[226:229], v[144:147], v[248:251], 0\n"
    "v_cvt_pk_bf16_f32 v230, v234, v235\n"
    "v_cvt_pk_bf16_f32 v231, v236, v237\n"
    "v_mfma_f32_16x16x32_bf16 v[234:237], v[156:159], v[248:251], 0\n"
    "ds_write_b64 v208, v[230:231] offset:24576\n"
    "v_mfma_f32_16x16x32_bf16 v[230:233], v[150:153], v[248:251], 0\n"
    "v_max3_f32 v223, v222, v210, v211\n"
    "v_max3_f32 v224, v212, v213, v214\n"
    "s_waitcnt lgkmcnt(4)\n"
    "v_mfma_f32_16x16x16_bf16 v[226:229], v[148:149], v[252:253], v[226:229]\n"
    "v_max3_f32 v223, v223, v215, v216\n"
    "v_max3_f32 v224, v224, v217, v218\n"
    "v_mfma_f32_16x16x16_bf16 v[238:241], v[166:167], v[252:253], v[238:241]\n"
    "v_max3_f32 v223, v223, v219, v220\n"
    "v_max3_f32 v223, v223, v224, v221\n"
    "v_mfma_f32_16x16x16_bf16 v[230:233], v[154:155], v[252:253], v[230:233]\n"
    "v_mov_b32 v224, v223\n"
    "v_cndmask_b32_e64 v192, v209, 0, s[60:61]\n"
    "v_mfma_f32_16x16x16_bf16 v[234:237], v[160:161], v[252:253], v[234:237]\n"
    "s_mov_b32 s60, 0x0\n"
    "s_setprio 0\n"
    "v_permlane32_swap_b32 v223, v224\n"
    "v_max3_f32 v239, v238, v226, v227\n"
    "v_max_f32 v223, v223, v224\n"
    "v_mov_b32 v224, v223\n"
    "v_max3_f32 v240, v228, v229, v230\n"
    "v_max3_f32 v239, v239, v231, v232\n"
    "v_permlane16_swap_b32 v223, v224\n"
    "v_max3_f32 v240, v240, v233, v234\n"
    "v_max3_f32 v239, v239, v235, v236\n"
    "v_max3_f32 v239, v239, v240, v237\n"
    "v_mov_b32 v240, v239\n"
    "v_max_f32 v223, v223, v224\n"
    "v_mul_f32 v223, 0xbe553b95, v223\n"
    "v_permlane32_swap_b32 v239, v240\n"
    "v_fmamk_f32 v210, v210, 0x3e553b95, v223\n"
    "v_max_f32 v239, v239, v240\n"
    "v_mov_b32 v240, v239\n"
    "v_fmamk_f32 v211, v211, 0x3e553b95, v223\n"
    "v_fmamk_f32 v212, v212, 0x3e553b95, v223\n"
    "v_fmamk_f32 v213, v213, 0x3e553b95, v223\n"
    "v_fmamk_f32 v214, v214, 0x3e553b95, v223\n"
    "v_fmamk_f32 v215, v215, 0x3e553b95, v223\n"
    "v_fmamk_f32 v216, v216, 0x3e553b95, v223\n"
    "v_fmamk_f32 v217, v217, 0x3e553b95, v223\n"
    "v_permlane16_swap_b32 v239, v240\n"
    "v_exp_f32 v210, v210\n"
    "v_exp_f32 v211, v211\n"
    "v_exp_f32 v212, v212\n"
    "v_exp_f32 v213, v213\n"
    "v_exp_f32 v214, v214\n"
    "v_exp_f32 v215, v215\n"
    "v_exp_f32 v216, v216\n"
    "v_exp_f32 v217, v217\n"
    "v_fmamk_f32 v218, v218, 0x3e553b95, v223\n"
    "v_fmamk_f32 v219, v219, 0x3e553b95, v223\n"
    "v_fmamk_f32 v220, v220, 0x3e553b95, v223\n"
    "v_fmamk_f32 v221, v221, 0x3e553b95, v223\n"
    "v_fmamk_f32 v222, v222, 0x3e553b95, v223\n"
    "v_exp_f32 v218, v218\n"
    "v_exp_f32 v219, v219\n"
    "v_exp_f32 v220, v220\n"
    "v_exp_f32 v221, v221\n"
    "v_exp_f32 v222, v222\n"
    "v_max_f32 v239, v239, v240\n"
    "v_cvt_pk_bf16_f32 v198, v210, v211\n"
    "v_cvt_pk_bf16_f32 v199, v212, v213\n"
    "v_cvt_pk_bf16_f32 v200, v214, v215\n"
    "v_cvt_pk_bf16_f32 v201, v216, v217\n"
    "v_mul_f32 v239, 0xbe553b95, v239\n"
    "s_barrier\n"
    "s_setprio 2\n"
    "v_mfma_f32_16x16x32_bf16 v[210:213], v[168:171], v[198:201], 0\n"
    "s_setprio 3\n"
    "ds_read_b128 v[250:253], v206 offset:0\n"
    "ds_read_b128 v[242:245], v206 offset:3072\n"
    "ds_read_b128 v[246:249], v206 offset:4096\n"
    "s_setprio 2\n"
    "v_mfma_f32_16x16x32_bf16 v[214:217], v[176:179], v[198:201], 0\n"
    "s_mov_b32 s61, 0xffff\n"
    "s_setprio 0\n"
    "v_fmamk_f32 v226, v226, 0x3e553b95, v239\n"
    "v_fmamk_f32 v227, v227, 0x3e553b95, v239\n"
    "v_fmamk_f32 v228, v228, 0x3e553b95, v239\n"
    "v_fmamk_f32 v229, v229, 0x3e553b95, v239\n"
    "v_fmamk_f32 v230, v230, 0x3e553b95, v239\n"
    "v_fmamk_f32 v231, v231, 0x3e553b95, v239\n"
    "v_fmamk_f32 v232, v232, 0x3e553b95, v239\n"
    "v_fmamk_f32 v233, v233, 0x3e553b95, v239\n"
    "v_cvt_pk_bf16_f32 v202, v218, v219\n"
    "v_cvt_pk_bf16_f32 v203, v220, v221\n"
    "s_setprio 2\n"
    "v_mfma_f32_16x16x32_bf16 v[218:221], v[184:187], v[198:201], 0\n"
    "v_cvt_pk_bf16_f32 v204, v222, 0\n"
    "v_exp_f32 v226, v226\n"
    "v_mfma_f32_16x16x32_bf16 v[222:225], v[194:197], v[198:201], 0\n"
    "v_exp_f32 v227, v227\n"
    "v_mfma_f32_16x16x32_bf16 v[210:213], v[172:175], v[202:205], v[210:213]\n"
    "v_exp_f32 v228, v228\n"
    "v_mfma_f32_16x16x32_bf16 v[214:217], v[180:183], v[202:205], v[214:217]\n"
    "v_exp_f32 v229, v229\n"
    "v_mfma_f32_16x16x32_bf16 v[218:221], v[188:191], v[202:205], v[218:221]\n"
    "v_exp_f32 v230, v230\n"
    "v_mfma_f32_16x16x32_bf16 v[222:225], v[194:197], v[202:205], v[222:225]\n"
    "s_setprio 0\n"
    "v_exp_f32 v231, v231\n"
    "v_exp_f32 v232, v232\n"
    "v_exp_f32 v233, v233\n"
    "v_fmamk_f32 v238, v238, 0x3e553b95, v239\n"
    "v_exp_f32 v238, v238\n"
    "v_fmamk_f32 v234, v234, 0x3e553b95, v239\n"
    "v_fmamk_f32 v235, v235, 0x3e553b95, v239\n"
    "v_fmamk_f32 v236, v236, 0x3e553b95, v239\n"
    "v_fmamk_f32 v237, v237, 0x3e553b95, v239\n"
    "v_cvt_pk_bf16_f32 v198, v226, v227\n"
    "v_cvt_pk_bf16_f32 v199, v228, v229\n"
    "v_cvt_pk_bf16_f32 v200, v230, v231\n"
    "v_cvt_pk_bf16_f32 v201, v232, v233\n"
    "v_exp_f32 v234, v234\n"
    "v_exp_f32 v235, v235\n"
    "s_setprio 2\n"
    "v_mfma_f32_16x16x32_bf16 v[226:229], v[168:171], v[198:201], 0\n"
    "v_exp_f32 v236, v236\n"
    "v_mfma_f32_16x16x32_bf16 v[230:233], v[176:179], v[198:201], 0\n"
    "v_exp_f32 v237, v237\n"
    "v_cvt_pk_bf16_f32 v204, v238, 0\n"
    "v_mfma_f32_16x16x32_bf16 v[238:241], v[194:197], v[198:201], 0\n"
    "v_cvt_pk_bf16_f32 v202, v234, v235\n"
    "v_cvt_pk_bf16_f32 v203, v236, v237\n"
    "v_mfma_f32_16x16x32_bf16 v[234:237], v[184:187], v[198:201], 0\n"
    "v_rcp_f32 v223, v222\n"
    "v_mfma_f32_16x16x32_bf16 v[238:241], v[194:197], v[202:205], v[238:241]\n"
    "v_mul_f32 v210, v210, v223\n"
    "v_mul_f32 v211, v211, v223\n"
    "v_mfma_f32_16x16x32_bf16 v[226:229], v[172:175], v[202:205], v[226:229]\n"
    "v_mul_f32 v212, v212, v223\n"
    "v_mul_f32 v213, v213, v223\n"
    "v_mfma_f32_16x16x32_bf16 v[230:233], v[180:183], v[202:205], v[230:233]\n"
    "v_mul_f32 v214, v214, v223\n"
    "v_mul_f32 v215, v215, v223\n"
    "v_mfma_f32_16x16x32_bf16 v[234:237], v[188:191], v[202:205], v[234:237]\n"
    "s_setprio 0\n"
    "v_rcp_f32 v239, v238\n"
    "v_mul_f32 v216, v216, v223\n"
    "v_mul_f32 v217, v217, v223\n"
    "v_cvt_pk_bf16_f32 v210, v210, v211\n"
    "v_cvt_pk_bf16_f32 v211, v212, v213\n"
    "v_mul_f32 v226, v226, v239\n"
    "v_mul_f32 v227, v227, v239\n"
    "v_cvt_pk_bf16_f32 v212, v214, v215\n"
    "v_cvt_pk_bf16_f32 v213, v216, v217\n"
    "v_mul_f32 v228, v228, v239\n"
    "v_mul_f32 v229, v229, v239\n"
    "v_mul_f32 v218, v218, v223\n"
    "v_mul_f32 v219, v219, v223\n"
    "v_mul_f32 v220, v220, v223\n"
    "v_mul_f32 v221, v221, v223\n"
    "s_setprio 1\n"
    "s_waitcnt lgkmcnt(2)\n"
    "v_mfma_f32_16x16x32_bf16 v[222:225], v[250:253], v[72:75], 0\n"
    "s_setprio 2\n"
    "ds_write_b128 v207, v[210:213] offset:30720\n"
    "s_setprio 1\n"
    "v_mfma_f32_16x16x32_bf16 v[210:213], v[0:3], v[250:253], 0\n"
    "s_setprio 0\n"
    "v_mul_f32 v230, v230, v239\n"
    "v_mul_f32 v231, v231, v239\n"
    "v_cvt_pk_bf16_f32 v226, v226, v227\n"
    "v_mul_f32 v234, v234, v239\n"
    "v_mul_f32 v235, v235, v239\n"
    "v_mul_f32 v236, v236, v239\n"
    "v_mul_f32 v237, v237, v239\n"
    "v_cvt_pk_bf16_f32 v227, v228, v229\n"
    "v_cvt_pk_bf16_f32 v214, v218, v219\n"
    "v_cvt_pk_bf16_f32 v215, v220, v221\n"
    "s_setprio 1\n"
    "v_mfma_f32_16x16x32_bf16 v[218:221], v[8:11], v[250:253], 0\n"
    "s_setprio 2\n"
    "ds_write_b64 v208, v[214:215] offset:30720\n"
    "s_setprio 1\n"
    "v_mfma_f32_16x16x32_bf16 v[214:217], v[4:7], v[250:253], 0\n"
    "s_setprio 0\n"
    "v_cvt_pk_bf16_f32 v228, v230, v231\n"
    "v_mul_f32 v232, v232, v239\n"
    "v_mul_f32 v233, v233, v239\n"
    "s_setprio 3\n"
    "ds_read_b128 v[238:241], v206 offset:2048\n"
    "s_setprio 0\n"
    "v_cvt_pk_bf16_f32 v230, v234, v235\n"
    "v_cvt_pk_bf16_f32 v231, v236, v237\n"
    "s_setprio 3\n"
    "ds_read_b128 v[234:237], v206 offset:1024\n"
    "s_setprio 0\n"
    "v_cvt_pk_bf16_f32 v229, v232, v233\n"
    "s_setprio 2\n"
    "s_mov_b32 exec_lo, 0x10001\n"
    "s_mov_b32 exec_hi, 0x10001\n"
    "ds_write_b128 v207, v[226:229] offset:55296\n"
    "s_mov_b64 exec, -1\n"
    "s_setprio 1\n"
    "v_mfma_f32_16x16x32_bf16 v[226:229], v[250:253], v[76:79], 0\n"
    "s_setprio 2\n"
    "s_mov_b32 exec_lo, 0x10001\n"
    "s_mov_b32 exec_hi, 0x10001\n"
    "ds_write_b64 v208, v[230:231] offset:55296\n"
    "s_mov_b64 exec, -1\n"
    "s_setprio 1\n"
    "v_mfma_f32_16x16x32_bf16 v[230:233], v[250:253], v[80:83], 0\n"
    "s_setprio 3\n"
    "ds_read_b128 v[250:253], v206 offset:5120\n"
    "s_setprio 1\n"
    "s_waitcnt lgkmcnt(3)\n"
    "v_mfma_f32_16x16x32_bf16 v[230:233], v[234:237], v[92:95], v[230:233]\n"
    "v_mfma_f32_16x16x32_bf16 v[210:213], v[12:15], v[234:237], v[210:213]\n"
    "v_mfma_f32_16x16x32_bf16 v[222:225], v[234:237], v[84:87], v[222:225]\n"
    "v_mfma_f32_16x16x32_bf16 v[214:217], v[16:19], v[234:237], v[214:217]\n"
    "v_mfma_f32_16x16x32_bf16 v[230:233], v[238:241], v[104:107], v[230:233]\n"
    "v_mfma_f32_16x16x32_bf16 v[218:221], v[20:23], v[234:237], v[218:221]\n"
    "v_mfma_f32_16x16x32_bf16 v[210:213], v[24:27], v[238:241], v[210:213]\n"
    "v_mfma_f32_16x16x32_bf16 v[222:225], v[238:241], v[96:99], v[222:225]\n"
    "v_mfma_f32_16x16x32_bf16 v[214:217], v[28:31], v[238:241], v[214:217]\n"
    "v_mfma_f32_16x16x32_bf16 v[230:233], v[242:245], v[116:119], v[230:233]\n"
    "v_mfma_f32_16x16x32_bf16 v[226:229], v[234:237], v[88:91], v[226:229]\n"
    "s_setprio 3\n"
    "ds_read_b128 v[234:237], v206 offset:6144\n"
    "s_setprio 1\n"
    "v_mfma_f32_16x16x32_bf16 v[218:221], v[32:35], v[238:241], v[218:221]\n"
    "v_mfma_f32_16x16x32_bf16 v[210:213], v[36:39], v[242:245], v[210:213]\n"
    "v_mfma_f32_16x16x32_bf16 v[222:225], v[242:245], v[108:111], v[222:225]\n"
    "v_mfma_f32_16x16x32_bf16 v[214:217], v[40:43], v[242:245], v[214:217]\n"
    "v_mfma_f32_16x16x32_bf16 v[230:233], v[246:249], v[128:131], v[230:233]\n"
    "v_mfma_f32_16x16x32_bf16 v[226:229], v[238:241], v[100:103], v[226:229]\n"
    "s_setprio 3\n"
    "ds_read_b128 v[238:241], v206 offset:7168\n"
    "s_setprio 1\n"
    "v_mfma_f32_16x16x32_bf16 v[218:221], v[44:47], v[242:245], v[218:221]\n"
    "v_mfma_f32_16x16x32_bf16 v[210:213], v[48:51], v[246:249], v[210:213]\n"
    "v_mfma_f32_16x16x32_bf16 v[222:225], v[246:249], v[120:123], v[222:225]\n"
    "v_mfma_f32_16x16x32_bf16 v[214:217], v[52:55], v[246:249], v[214:217]\n"
    "s_waitcnt lgkmcnt(2)\n"
    "v_mfma_f32_16x16x32_bf16 v[230:233], v[250:253], v[140:143], v[230:233]\n"
    "v_mfma_f32_16x16x32_bf16 v[226:229], v[242:245], v[112:115], v[226:229]\n"
    "s_setprio 3\n"
    "ds_read_b128 v[242:245], v206 offset:8192\n"
    "s_setprio 1\n"
    "v_mfma_f32_16x16x32_bf16 v[218:221], v[56:59], v[246:249], v[218:221]\n"
    "v_mfma_f32_16x16x32_bf16 v[210:213], v[60:63], v[250:253], v[210:213]\n"
    "v_mfma_f32_16x16x32_bf16 v[222:225], v[250:253], v[132:135], v[222:225]\n"
    "v_mfma_f32_16x16x32_bf16 v[214:217], v[64:67], v[250:253], v[214:217]\n"
    "v_cvt_pk_bf16_f32 v184, v230, v231\n"
    "v_mfma_f32_16x16x32_bf16 v[226:229], v[246:249], v[124:127], v[226:229]\n"
    "s_setprio 3\n"
    "ds_read_b128 v[246:249], v206 offset:9216\n"
    "s_setprio 1\n"
    "v_cvt_pk_bf16_f32 v185, v232, v233\n"
    "v_cvt_pk_bf16_f32 v144, v210, v211\n"
    "v_mfma_f32_16x16x32_bf16 v[218:221], v[68:71], v[250:253], v[218:221]\n"
    "v_cvt_pk_bf16_f32 v145, v212, v213\n"
    "v_cvt_pk_bf16_f32 v168, v222, v223\n"
    "v_mfma_f32_16x16x32_bf16 v[226:229], v[250:253], v[136:139], v[226:229]\n"
    "s_setprio 3\n"
    "ds_read_b128 v[250:253], v206 offset:10240\n"
    "s_setprio 1\n"
    "v_cvt_pk_bf16_f32 v169, v224, v225\n"
    "v_cvt_pk_bf16_f32 v146, v214, v215\n"
    "s_waitcnt lgkmcnt(4)\n"
    "v_mfma_f32_16x16x32_bf16 v[230:233], v[234:237], v[80:83], 0\n"
    "v_cvt_pk_bf16_f32 v147, v216, v217\n"
    "v_cvt_pk_bf16_f32 v148, v218, v219\n"
    "v_mfma_f32_16x16x32_bf16 v[210:213], v[0:3], v[234:237], 0\n"
    "v_cvt_pk_bf16_f32 v149, v220, v221\n"
    "v_cvt_pk_bf16_f32 v176, v226, v227\n"
    "v_mfma_f32_16x16x32_bf16 v[222:225], v[234:237], v[72:75], 0\n"
    "v_cvt_pk_bf16_f32 v177, v228, v229\n"
    "v_mfma_f32_16x16x32_bf16 v[214:217], v[4:7], v[234:237], 0\n"
    "s_waitcnt lgkmcnt(3)\n"
    "v_mfma_f32_16x16x32_bf16 v[230:233], v[238:241], v[92:95], v[230:233]\n"
    "v_mfma_f32_16x16x32_bf16 v[218:221], v[8:11], v[234:237], 0\n"
    "v_mfma_f32_16x16x32_bf16 v[210:213], v[12:15], v[238:241], v[210:213]\n"
    "v_mfma_f32_16x16x32_bf16 v[222:225], v[238:241], v[84:87], v[222:225]\n"
    "v_mfma_f32_16x16x32_bf16 v[214:217], v[16:19], v[238:241], v[214:217]\n"
    "s_waitcnt lgkmcnt(2)\n"
    "v_mfma_f32_16x16x32_bf16 v[230:233], v[242:245], v[104:107], v[230:233]\n"
    "v_mfma_f32_16x16x32_bf16 v[226:229], v[234:237], v[76:79], 0\n"
    "s_setprio 3\n"
    "ds_read_b128 v[234:237], v206 offset:11264\n"
    "s_setprio 1\n"
    "v_mfma_f32_16x16x32_bf16 v[218:221], v[20:23], v[238:241], v[218:221]\n"
    "v_mfma_f32_16x16x32_bf16 v[210:213], v[24:27], v[242:245], v[210:213]\n"
    "v_mfma_f32_16x16x32_bf16 v[222:225], v[242:245], v[96:99], v[222:225]\n"
    "v_mfma_f32_16x16x32_bf16 v[214:217], v[28:31], v[242:245], v[214:217]\n"
    "s_waitcnt lgkmcnt(2)\n"
    "v_mfma_f32_16x16x32_bf16 v[230:233], v[246:249], v[116:119], v[230:233]\n"
    "v_mfma_f32_16x16x32_bf16 v[226:229], v[238:241], v[88:91], v[226:229]\n"
    "s_setprio 3\n"
    "ds_read_b128 v[238:241], v206 offset:12288\n"
    "s_setprio 1\n"
    "v_mfma_f32_16x16x32_bf16 v[218:221], v[32:35], v[242:245], v[218:221]\n"
    "v_mfma_f32_16x16x32_bf16 v[210:213], v[36:39], v[246:249], v[210:213]\n"
    "v_mfma_f32_16x16x32_bf16 v[222:225], v[246:249], v[108:111], v[222:225]\n"
    "v_mfma_f32_16x16x32_bf16 v[214:217], v[40:43], v[246:249], v[214:217]\n"
    "s_waitcnt lgkmcnt(2)\n"
    "v_mfma_f32_16x16x32_bf16 v[230:233], v[250:253], v[128:131], v[230:233]\n"
    "v_mfma_f32_16x16x32_bf16 v[226:229], v[242:245], v[100:103], v[226:229]\n"
    "s_setprio 3\n"
    "ds_read_b128 v[242:245], v206 offset:13312\n"
    "s_setprio 1\n"
    "v_mfma_f32_16x16x32_bf16 v[218:221], v[44:47], v[246:249], v[218:221]\n"
    "v_mfma_f32_16x16x32_bf16 v[210:213], v[48:51], v[250:253], v[210:213]\n"
    "v_mfma_f32_16x16x32_bf16 v[222:225], v[250:253], v[120:123], v[222:225]\n"
    "v_mfma_f32_16x16x32_bf16 v[214:217], v[52:55], v[250:253], v[214:217]\n"
    "s_waitcnt lgkmcnt(2)\n"
    "v_mfma_f32_16x16x32_bf16 v[230:233], v[234:237], v[140:143], v[230:233]\n"
    "v_mfma_f32_16x16x32_bf16 v[226:229], v[246:249], v[112:115], v[226:229]\n"
    "s_setprio 3\n"
    "ds_read_b128 v[246:249], v206 offset:14336\n"
    "s_setprio 1\n"
    "v_mfma_f32_16x16x32_bf16 v[218:221], v[56:59], v[250:253], v[218:221]\n"
    "v_mfma_f32_16x16x32_bf16 v[210:213], v[60:63], v[234:237], v[210:213]\n"
    "v_mfma_f32_16x16x32_bf16 v[222:225], v[234:237], v[132:135], v[222:225]\n"
    "v_mfma_f32_16x16x32_bf16 v[214:217], v[64:67], v[234:237], v[214:217]\n"
    "v_cvt_pk_bf16_f32 v186, v230, v231\n"
    "v_mfma_f32_16x16x32_bf16 v[226:229], v[250:253], v[124:127], v[226:229]\n"
    "s_setprio 3\n"
    "ds_read_b128 v[250:253], v206 offset:15360\n"
    "s_setprio 1\n"
    "v_cvt_pk_bf16_f32 v187, v232, v233\n"
    "v_cvt_pk_bf16_f32 v150, v210, v211\n"
    "v_mfma_f32_16x16x32_bf16 v[218:221], v[68:71], v[234:237], v[218:221]\n"
    "v_cvt_pk_bf16_f32 v151, v212, v213\n"
    "v_cvt_pk_bf16_f32 v170, v222, v223\n"
    "v_mfma_f32_16x16x32_bf16 v[226:229], v[234:237], v[136:139], v[226:229]\n"
    "s_setprio 3\n"
    "ds_read_b128 v[234:237], v206 offset:16384\n"
    "s_setprio 1\n"
    "v_cvt_pk_bf16_f32 v171, v224, v225\n"
    "v_cvt_pk_bf16_f32 v152, v214, v215\n"
    "s_waitcnt lgkmcnt(4)\n"
    "v_mfma_f32_16x16x32_bf16 v[230:233], v[238:241], v[80:83], 0\n"
    "v_cvt_pk_bf16_f32 v153, v216, v217\n"
    "v_cvt_pk_bf16_f32 v154, v218, v219\n"
    "v_mfma_f32_16x16x32_bf16 v[210:213], v[0:3], v[238:241], 0\n"
    "v_cvt_pk_bf16_f32 v155, v220, v221\n"
    "v_cvt_pk_bf16_f32 v178, v226, v227\n"
    "v_mfma_f32_16x16x32_bf16 v[222:225], v[238:241], v[72:75], 0\n"
    "v_cvt_pk_bf16_f32 v179, v228, v229\n"
    "v_mfma_f32_16x16x32_bf16 v[214:217], v[4:7], v[238:241], 0\n"
    "s_waitcnt lgkmcnt(3)\n"
    "v_mfma_f32_16x16x32_bf16 v[230:233], v[242:245], v[92:95], v[230:233]\n"
    "v_mfma_f32_16x16x32_bf16 v[218:221], v[8:11], v[238:241], 0\n"
    "v_mfma_f32_16x16x32_bf16 v[210:213], v[12:15], v[242:245], v[210:213]\n"
    "v_mfma_f32_16x16x32_bf16 v[222:225], v[242:245], v[84:87], v[222:225]\n"
    "v_mfma_f32_16x16x32_bf16 v[214:217], v[16:19], v[242:245], v[214:217]\n"
    "s_waitcnt lgkmcnt(2)\n"
    "v_mfma_f32_16x16x32_bf16 v[230:233], v[246:249], v[104:107], v[230:233]\n"
    "v_mfma_f32_16x16x32_bf16 v[226:229], v[238:241], v[76:79], 0\n"
    "s_setprio 3\n"
    "ds_read_b128 v[238:241], v206 offset:17408\n"
    "s_setprio 1\n"
    "v_mfma_f32_16x16x32_bf16 v[218:221], v[20:23], v[242:245], v[218:221]\n"
    "v_mfma_f32_16x16x32_bf16 v[210:213], v[24:27], v[246:249], v[210:213]\n"
    "v_mfma_f32_16x16x32_bf16 v[222:225], v[246:249], v[96:99], v[222:225]\n"
    "v_mfma_f32_16x16x32_bf16 v[214:217], v[28:31], v[246:249], v[214:217]\n"
    "s_waitcnt lgkmcnt(2)\n"
    "v_mfma_f32_16x16x32_bf16 v[230:233], v[250:253], v[116:119], v[230:233]\n"
    "v_mfma_f32_16x16x32_bf16 v[226:229], v[242:245], v[88:91], v[226:229]\n"
    "v_mfma_f32_16x16x32_bf16 v[218:221], v[32:35], v[246:249], v[218:221]\n"
    "v_mfma_f32_16x16x32_bf16 v[210:213], v[36:39], v[250:253], v[210:213]\n"
    "v_mfma_f32_16x16x32_bf16 v[222:225], v[250:253], v[108:111], v[222:225]\n"
    "v_mfma_f32_16x16x32_bf16 v[214:217], v[40:43], v[250:253], v[214:217]\n"
    "s_waitcnt lgkmcnt(1)\n"
    "v_mfma_f32_16x16x32_bf16 v[230:233], v[234:237], v[128:131], v[230:233]\n"
    "v_mfma_f32_16x16x32_bf16 v[226:229], v[246:249], v[100:103], v[226:229]\n"
    "v_mfma_f32_16x16x32_bf16 v[218:221], v[44:47], v[250:253], v[218:221]\n"
    "v_mfma_f32_16x16x32_bf16 v[210:213], v[48:51], v[234:237], v[210:213]\n"
    "v_mfma_f32_16x16x32_bf16 v[222:225], v[234:237], v[120:123], v[222:225]\n"
    "v_mfma_f32_16x16x32_bf16 v[214:217], v[52:55], v[234:237], v[214:217]\n"
    "s_waitcnt lgkmcnt(0)\n"
    "v_mfma_f32_16x16x32_bf16 v[230:233], v[238:241], v[140:143], v[230:233]\n"
    "s_barrier\n"
    "s_setprio 2\n"
    "ds_read_b128 v[242:245], v207 offset:36864\n"
    "ds_read_b64 v[246:247], v208 offset:36864\n"
    "s_setprio 1\n"
    "v_mfma_f32_16x16x32_bf16 v[226:229], v[250:253], v[112:115], v[226:229]\n"
    "s_setprio 2\n"
    "ds_read_b128 v[248:251], v207 offset:43008\n"
    "ds_read_b64 v[252:253], v208 offset:43008\n"
    "s_setprio 1\n"
    "v_mfma_f32_16x16x32_bf16 v[218:221], v[56:59], v[234:237], v[218:221]\n"
    "v_cvt_pk_bf16_f32 v188, v230, v231\n"
    "v_mfma_f32_16x16x32_bf16 v[210:213], v[60:63], v[238:241], v[210:213]\n"
    "v_cvt_pk_bf16_f32 v189, v232, v233\n"
    "v_mfma_f32_16x16x32_bf16 v[222:225], v[238:241], v[132:135], v[222:225]\n"
    "v_mfma_f32_16x16x32_bf16 v[214:217], v[64:67], v[238:241], v[214:217]\n"
    "v_mfma_f32_16x16x32_bf16 v[226:229], v[234:237], v[124:127], v[226:229]\n"
    "v_mfma_f32_16x16x32_bf16 v[218:221], v[68:71], v[238:241], v[218:221]\n"
    "v_mfma_f32_16x16x32_bf16 v[226:229], v[238:241], v[136:139], v[226:229]\n"
    "s_nop 1\n"
    "v_cvt_pk_bf16_f32 v156, v210, v211\n"
    "s_setprio 2\n"
    "s_waitcnt lgkmcnt(1)\n"
    "v_mfma_f32_16x16x32_bf16 v[238:241], v[162:165], v[248:251], v[192:195]\n"
    "s_setprio 1\n"
    "v_cvt_pk_bf16_f32 v157, v212, v213\n"
    "v_cvt_pk_bf16_f32 v172, v222, v223\n"
    "s_setprio 2\n"
    "v_mfma_f32_16x16x32_bf16 v[210:213], v[144:147], v[242:245], 0\n"
    "s_setprio 1\n"
    "v_cvt_pk_bf16_f32 v173, v224, v225\n"
    "v_cvt_pk_bf16_f32 v158, v214, v215\n"
    "s_setprio 2\n"
    "v_mfma_f32_16x16x32_bf16 v[222:225], v[162:165], v[242:245], v[192:195]\n"
    "s_setprio 1\n"
    "v_cvt_pk_bf16_f32 v159, v216, v217\n"
    "v_cvt_pk_bf16_f32 v160, v218, v219\n"
    "s_setprio 2\n"
    "v_mfma_f32_16x16x32_bf16 v[214:217], v[150:153], v[242:245], 0\n"
    "s_setprio 1\n"
    "v_cvt_pk_bf16_f32 v161, v220, v221\n"
    "v_cvt_pk_bf16_f32 v180, v226, v227\n"
    "s_setprio 2\n"
    "v_mfma_f32_16x16x32_bf16 v[218:221], v[156:159], v[242:245], 0\n"
    "ds_read_b128 v[242:245], v207 offset:49152\n"
    "s_setprio 1\n"
    "v_cvt_pk_bf16_f32 v181, v228, v229\n"
    "s_setprio 2\n"
    "v_mfma_f32_16x16x32_bf16 v[226:229], v[144:147], v[248:251], 0\n"
    "v_mfma_f32_16x16x16_bf16 v[210:213], v[148:149], v[246:247], v[210:213]\n"
    "v_mfma_f32_16x16x16_bf16 v[222:225], v[166:167], v[246:247], v[222:225]\n"
    "v_mfma_f32_16x16x32_bf16 v[230:233], v[150:153], v[248:251], 0\n"
    "v_mfma_f32_16x16x16_bf16 v[214:217], v[154:155], v[246:247], v[214:217]\n"
    "v_mfma_f32_16x16x32_bf16 v[234:237], v[156:159], v[248:251], 0\n"
    "ds_read_b128 v[248:251], v207 offset:55296\n"
    "v_mfma_f32_16x16x16_bf16 v[218:221], v[160:161], v[246:247], v[218:221]\n"
    "ds_read_b64 v[246:247], v208 offset:49152\n"
    "s_waitcnt lgkmcnt(3)\n"
    "v_mfma_f32_16x16x16_bf16 v[226:229], v[148:149], v[252:253], v[226:229]\n"
    "v_max3_f32 v223, v222, v210, v211\n"
    "v_mfma_f32_16x16x16_bf16 v[238:241], v[166:167], v[252:253], v[238:241]\n"
    "v_max3_f32 v224, v212, v213, v214\n"
    "v_max3_f32 v223, v223, v215, v216\n"
    "v_mfma_f32_16x16x16_bf16 v[230:233], v[154:155], v[252:253], v[230:233]\n"
    "v_max3_f32 v224, v224, v217, v218\n"
    "v_max3_f32 v223, v223, v219, v220\n"
    "v_mfma_f32_16x16x16_bf16 v[234:237], v[160:161], v[252:253], v[234:237]\n"
    "ds_read_b64 v[252:253], v208 offset:55296\n"
    "s_setprio 0\n"
    "v_max3_f32 v223, v223, v224, v221\n"
    "v_mov_b32 v224, v223\n"
    "v_max3_f32 v239, v238, v226, v227\n"
    "v_max3_f32 v240, v228, v229, v230\n"
    "v_max3_f32 v239, v239, v231, v232\n"
    "v_permlane32_swap_b32 v223, v224\n"
    "v_max3_f32 v240, v240, v233, v234\n"
    "v_max3_f32 v239, v239, v235, v236\n"
    "v_max3_f32 v239, v239, v240, v237\n"
    "v_max_f32 v223, v223, v224\n"
    "v_mov_b32 v240, v239\n"
    "v_mov_b32 v224, v223\n"
    "s_nop 0\n"
    "v_permlane32_swap_b32 v239, v240\n"
    "v_permlane16_swap_b32 v223, v224\n"
    "v_max_f32 v239, v239, v240\n"
    "v_max_f32 v223, v223, v224\n"
    "v_mov_b32 v240, v239\n"
    "v_mul_f32 v223, 0xbe553b95, v223\n"
    "v_fmamk_f32 v210, v210, 0x3e553b95, v223\n"
    "v_fmamk_f32 v211, v211, 0x3e553b95, v223\n"
    "v_fmamk_f32 v212, v212, 0x3e553b95, v223\n"
    "v_fmamk_f32 v213, v213, 0x3e553b95, v223\n"
    "v_fmamk_f32 v214, v214, 0x3e553b95, v223\n"
    "v_fmamk_f32 v215, v215, 0x3e553b95, v223\n"
    "v_fmamk_f32 v216, v216, 0x3e553b95, v223\n"
    "v_fmamk_f32 v217, v217, 0x3e553b95, v223\n"
    "v_permlane16_swap_b32 v239, v240\n"
    "v_exp_f32 v210, v210\n"
    "v_exp_f32 v211, v211\n"
    "v_exp_f32 v212, v212\n"
    "v_exp_f32 v213, v213\n"
    "v_exp_f32 v214, v214\n"
    "v_exp_f32 v215, v215\n"
    "v_exp_f32 v216, v216\n"
    "v_exp_f32 v217, v217\n"
    "v_fmamk_f32 v218, v218, 0x3e553b95, v223\n"
    "v_fmamk_f32 v219, v219, 0x3e553b95, v223\n"
    "v_fmamk_f32 v220, v220, 0x3e553b95, v223\n"
    "v_fmamk_f32 v221, v221, 0x3e553b95, v223\n"
    "v_fmamk_f32 v222, v222, 0x3e553b95, v223\n"
    "v_exp_f32 v218, v218\n"
    "v_exp_f32 v219, v219\n"
    "v_exp_f32 v220, v220\n"
    "v_exp_f32 v221, v221\n"
    "v_exp_f32 v222, v222\n"
    "v_max_f32 v239, v239, v240\n"
    "v_cvt_pk_bf16_f32 v198, v210, v211\n"
    "v_cvt_pk_bf16_f32 v199, v212, v213\n"
    "v_cvt_pk_bf16_f32 v200, v214, v215\n"
    "v_cvt_pk_bf16_f32 v201, v216, v217\n"
    "v_mul_f32 v239, 0xbe553b95, v239\n"
    "v_fmamk_f32 v226, v226, 0x3e553b95, v239\n"
    "s_setprio 2\n"
    "v_mfma_f32_16x16x32_bf16 v[210:213], v[168:171], v[198:201], 0\n"
    "v_fmamk_f32 v227, v227, 0x3e553b95, v239\n"
    "v_fmamk_f32 v228, v228, 0x3e553b95, v239\n"
    "v_mfma_f32_16x16x32_bf16 v[214:217], v[176:179], v[198:201], 0\n"
    "s_setprio 0\n"
    "v_fmamk_f32 v229, v229, 0x3e553b95, v239\n"
    "v_fmamk_f32 v230, v230, 0x3e553b95, v239\n"
    "v_fmamk_f32 v231, v231, 0x3e553b95, v239\n"
    "v_fmamk_f32 v232, v232, 0x3e553b95, v239\n"
    "v_fmamk_f32 v233, v233, 0x3e553b95, v239\n"
    "v_cvt_pk_bf16_f32 v202, v218, v219\n"
    "v_cvt_pk_bf16_f32 v203, v220, v221\n"
    "s_setprio 2\n"
    "v_mfma_f32_16x16x32_bf16 v[218:221], v[184:187], v[198:201], 0\n"
    "v_cvt_pk_bf16_f32 v204, v222, 0\n"
    "v_exp_f32 v226, v226\n"
    "v_mfma_f32_16x16x32_bf16 v[222:225], v[194:197], v[198:201], 0\n"
    "v_exp_f32 v227, v227\n"
    "v_mfma_f32_16x16x32_bf16 v[210:213], v[172:175], v[202:205], v[210:213]\n"
    "v_exp_f32 v228, v228\n"
    "v_mfma_f32_16x16x32_bf16 v[214:217], v[180:183], v[202:205], v[214:217]\n"
    "v_exp_f32 v229, v229\n"
    "v_mfma_f32_16x16x32_bf16 v[218:221], v[188:191], v[202:205], v[218:221]\n"
    "v_exp_f32 v230, v230\n"
    "v_mfma_f32_16x16x32_bf16 v[222:225], v[194:197], v[202:205], v[222:225]\n"
    "s_setprio 0\n"
    "v_exp_f32 v231, v231\n"
    "v_exp_f32 v232, v232\n"
    "v_exp_f32 v233, v233\n"
    "v_fmamk_f32 v238, v238, 0x3e553b95, v239\n"
    "v_exp_f32 v238, v238\n"
    "v_fmamk_f32 v234, v234, 0x3e553b95, v239\n"
    "v_fmamk_f32 v235, v235, 0x3e553b95, v239\n"
    "v_fmamk_f32 v236, v236, 0x3e553b95, v239\n"
    "v_fmamk_f32 v237, v237, 0x3e553b95, v239\n"
    "v_cvt_pk_bf16_f32 v198, v226, v227\n"
    "v_cvt_pk_bf16_f32 v199, v228, v229\n"
    "v_cvt_pk_bf16_f32 v200, v230, v231\n"
    "v_cvt_pk_bf16_f32 v201, v232, v233\n"
    "v_exp_f32 v234, v234\n"
    "v_exp_f32 v235, v235\n"
    "s_setprio 2\n"
    "v_mfma_f32_16x16x32_bf16 v[226:229], v[168:171], v[198:201], 0\n"
    "v_exp_f32 v236, v236\n"
    "v_mfma_f32_16x16x32_bf16 v[230:233], v[176:179], v[198:201], 0\n"
    "v_exp_f32 v237, v237\n"
    "v_cvt_pk_bf16_f32 v204, v238, 0\n"
    "v_mfma_f32_16x16x32_bf16 v[238:241], v[194:197], v[198:201], 0\n"
    "v_cvt_pk_bf16_f32 v202, v234, v235\n"
    "v_cvt_pk_bf16_f32 v203, v236, v237\n"
    "v_mfma_f32_16x16x32_bf16 v[234:237], v[184:187], v[198:201], 0\n"
    "v_rcp_f32 v223, v222\n"
    "v_mfma_f32_16x16x32_bf16 v[238:241], v[194:197], v[202:205], v[238:241]\n"
    "v_mul_f32 v210, v210, v223\n"
    "v_mul_f32 v211, v211, v223\n"
    "v_mfma_f32_16x16x32_bf16 v[226:229], v[172:175], v[202:205], v[226:229]\n"
    "v_mul_f32 v212, v212, v223\n"
    "v_mul_f32 v213, v213, v223\n"
    "v_mfma_f32_16x16x32_bf16 v[230:233], v[180:183], v[202:205], v[230:233]\n"
    "v_mul_f32 v214, v214, v223\n"
    "v_mul_f32 v215, v215, v223\n"
    "v_mfma_f32_16x16x32_bf16 v[234:237], v[188:191], v[202:205], v[234:237]\n"
    "s_setprio 0\n"
    "v_mul_f32 v216, v216, v223\n"
    "v_mul_f32 v217, v217, v223\n"
    "v_cvt_pk_bf16_f32 v210, v210, v211\n"
    "v_cvt_pk_bf16_f32 v211, v212, v213\n"
    "v_cvt_pk_bf16_f32 v212, v214, v215\n"
    "v_cvt_pk_bf16_f32 v213, v216, v217\n"
    "v_rcp_f32 v239, v238\n"
    "v_mul_f32 v218, v218, v223\n"
    "v_mul_f32 v219, v219, v223\n"
    "v_mul_f32 v220, v220, v223\n"
    "v_mul_f32 v221, v221, v223\n"
    "s_setprio 2\n"
    "s_waitcnt lgkmcnt(3)\n"
    "v_mfma_f32_16x16x32_bf16 v[222:225], v[162:165], v[242:245], v[192:195]\n"
    "ds_write_b128 v207, v[210:213] offset:36864\n"
    "v_mfma_f32_16x16x32_bf16 v[210:213], v[144:147], v[242:245], 0\n"
    "v_cvt_pk_bf16_f32 v214, v218, v219\n"
    "v_cvt_pk_bf16_f32 v215, v220, v221\n"
    "v_mfma_f32_16x16x32_bf16 v[218:221], v[156:159], v[242:245], 0\n"
    "ds_write_b64 v208, v[214:215] offset:36864\n"
    "v_mfma_f32_16x16x32_bf16 v[214:217], v[150:153], v[242:245], 0\n"
    "v_mul_f32 v226, v226, v239\n"
    "v_mul_f32 v227, v227, v239\n"
    "s_waitcnt lgkmcnt(3)\n"
    "v_mfma_f32_16x16x16_bf16 v[210:213], v[148:149], v[246:247], v[210:213]\n"
    "v_mul_f32 v228, v228, v239\n"
    "v_mul_f32 v229, v229, v239\n"
    "v_mfma_f32_16x16x16_bf16 v[222:225], v[166:167], v[246:247], v[222:225]\n"
    "v_mul_f32 v230, v230, v239\n"
    "v_mul_f32 v231, v231, v239\n"
    "v_mfma_f32_16x16x16_bf16 v[214:217], v[154:155], v[246:247], v[214:217]\n"
    "v_mul_f32 v232, v232, v239\n"
    "v_mul_f32 v233, v233, v239\n"
    "v_mfma_f32_16x16x16_bf16 v[218:221], v[160:161], v[246:247], v[218:221]\n"
    "s_setprio 0\n"
    "v_cvt_pk_bf16_f32 v226, v226, v227\n"
    "v_cvt_pk_bf16_f32 v227, v228, v229\n"
    "v_cvt_pk_bf16_f32 v228, v230, v231\n"
    "v_cvt_pk_bf16_f32 v229, v232, v233\n"
    "v_mul_f32 v234, v234, v239\n"
    "v_mul_f32 v235, v235, v239\n"
    "v_mul_f32 v236, v236, v239\n"
    "v_mul_f32 v237, v237, v239\n"
    "s_setprio 2\n"
    "v_mfma_f32_16x16x32_bf16 v[238:241], v[162:165], v[248:251], v[192:195]\n"
    "ds_write_b128 v207, v[226:229] offset:43008\n"
    "v_mfma_f32_16x16x32_bf16 v[226:229], v[144:147], v[248:251], 0\n"
    "v_cvt_pk_bf16_f32 v230, v234, v235\n"
    "v_cvt_pk_bf16_f32 v231, v236, v237\n"
    "v_mfma_f32_16x16x32_bf16 v[234:237], v[156:159], v[248:251], 0\n"
    "ds_write_b64 v208, v[230:231] offset:43008\n"
    "v_mfma_f32_16x16x32_bf16 v[230:233], v[150:153], v[248:251], 0\n"
    "v_max3_f32 v223, v222, v210, v211\n"
    "v_max3_f32 v224, v212, v213, v214\n"
    "s_waitcnt lgkmcnt(4)\n"
    "v_mfma_f32_16x16x16_bf16 v[226:229], v[148:149], v[252:253], v[226:229]\n"
    "v_max3_f32 v223, v223, v215, v216\n"
    "v_max3_f32 v224, v224, v217, v218\n"
    "v_mfma_f32_16x16x16_bf16 v[238:241], v[166:167], v[252:253], v[238:241]\n"
    "v_max3_f32 v223, v223, v219, v220\n"
    "v_max3_f32 v223, v223, v224, v221\n"
    "v_mfma_f32_16x16x16_bf16 v[230:233], v[154:155], v[252:253], v[230:233]\n"
    "v_mov_b32 v224, v223\n"
    "v_cndmask_b32_e64 v192, v209, 0, s[60:61]\n"
    "v_mfma_f32_16x16x16_bf16 v[234:237], v[160:161], v[252:253], v[234:237]\n"
    "s_mov_b32 s60, 0x0\n"
    "s_setprio 0\n"
    "v_permlane32_swap_b32 v223, v224\n"
    "v_max3_f32 v239, v238, v226, v227\n"
    "v_max_f32 v223, v223, v224\n"
    "v_mov_b32 v224, v223\n"
    "v_max3_f32 v240, v228, v229, v230\n"
    "v_max3_f32 v239, v239, v231, v232\n"
    "v_permlane16_swap_b32 v223, v224\n"
    "v_max3_f32 v240, v240, v233, v234\n"
    "v_max3_f32 v239, v239, v235, v236\n"
    "v_max3_f32 v239, v239, v240, v237\n"
    "v_mov_b32 v240, v239\n"
    "v_max_f32 v223, v223, v224\n"
    "v_mul_f32 v223, 0xbe553b95, v223\n"
    "s_barrier\n"
    "s_setprio 3\n"
    "ds_read_b128 v[242:245], v206 offset:0\n"
    "ds_read_b128 v[246:249], v206 offset:1024\n"
    "ds_read_b128 v[250:253], v206 offset:2048\n"
    "s_setprio 0\n"
    "v_permlane32_swap_b32 v239, v240\n"
    "v_fmamk_f32 v210, v210, 0x3e553b95, v223\n"
    "v_max_f32 v239, v239, v240\n"
    "v_mov_b32 v240, v239\n"
    "v_fmamk_f32 v211, v211, 0x3e553b95, v223\n"
    "v_fmamk_f32 v212, v212, 0x3e553b95, v223\n"
    "v_fmamk_f32 v213, v213, 0x3e553b95, v223\n"
    "v_fmamk_f32 v214, v214, 0x3e553b95, v223\n"
    "v_fmamk_f32 v215, v215, 0x3e553b95, v223\n"
    "v_fmamk_f32 v216, v216, 0x3e553b95, v223\n"
    "v_fmamk_f32 v217, v217, 0x3e553b95, v223\n"
    "v_permlane16_swap_b32 v239, v240\n"
    "v_exp_f32 v210, v210\n"
    "v_exp_f32 v211, v211\n"
    "v_exp_f32 v212, v212\n"
    "v_exp_f32 v213, v213\n"
    "v_exp_f32 v214, v214\n"
    "v_exp_f32 v215, v215\n"
    "v_exp_f32 v216, v216\n"
    "v_exp_f32 v217, v217\n"
    "v_fmamk_f32 v218, v218, 0x3e553b95, v223\n"
    "v_fmamk_f32 v219, v219, 0x3e553b95, v223\n"
    "v_fmamk_f32 v220, v220, 0x3e553b95, v223\n"
    "v_fmamk_f32 v221, v221, 0x3e553b95, v223\n"
    "v_fmamk_f32 v222, v222, 0x3e553b95, v223\n"
    "v_exp_f32 v218, v218\n"
    "v_exp_f32 v219, v219\n"
    "v_exp_f32 v220, v220\n"
    "v_exp_f32 v221, v221\n"
    "v_exp_f32 v222, v222\n"
    "v_max_f32 v239, v239, v240\n"
    "v_cvt_pk_bf16_f32 v198, v210, v211\n"
    "v_cvt_pk_bf16_f32 v199, v212, v213\n"
    "v_cvt_pk_bf16_f32 v200, v214, v215\n"
    "v_cvt_pk_bf16_f32 v201, v216, v217\n"
    "v_mul_f32 v239, 0xbe553b95, v239\n"
    "v_fmamk_f32 v226, v226, 0x3e553b95, v239\n"
    "s_setprio 2\n"
    "v_mfma_f32_16x16x32_bf16 v[210:213], v[168:171], v[198:201], 0\n"
    "s_mov_b32 s61, 0xffff0000\n"
    "v_fmamk_f32 v227, v227, 0x3e553b95, v239\n"
    "v_fmamk_f32 v228, v228, 0x3e553b95, v239\n"
    "v_mfma_f32_16x16x32_bf16 v[214:217], v[176:179], v[198:201], 0\n"
    "s_setprio 0\n"
    "v_fmamk_f32 v229, v229, 0x3e553b95, v239\n"
    "v_fmamk_f32 v230, v230, 0x3e553b95, v239\n"
    "v_fmamk_f32 v231, v231, 0x3e553b95, v239\n"
    "v_fmamk_f32 v232, v232, 0x3e553b95, v239\n"
    "v_fmamk_f32 v233, v233, 0x3e553b95, v239\n"
    "v_cvt_pk_bf16_f32 v202, v218, v219\n"
    "v_cvt_pk_bf16_f32 v203, v220, v221\n"
    "s_setprio 2\n"
    "v_mfma_f32_16x16x32_bf16 v[218:221], v[184:187], v[198:201], 0\n"
    "v_cvt_pk_bf16_f32 v204, v222, 0\n"
    "v_exp_f32 v226, v226\n"
    "v_mfma_f32_16x16x32_bf16 v[222:225], v[194:197], v[198:201], 0\n"
    "v_exp_f32 v227, v227\n"
    "v_mfma_f32_16x16x32_bf16 v[210:213], v[172:175], v[202:205], v[210:213]\n"
    "v_exp_f32 v228, v228\n"
    "v_mfma_f32_16x16x32_bf16 v[214:217], v[180:183], v[202:205], v[214:217]\n"
    "v_exp_f32 v229, v229\n"
    "v_mfma_f32_16x16x32_bf16 v[218:221], v[188:191], v[202:205], v[218:221]\n"
    "v_exp_f32 v230, v230\n"
    "v_mfma_f32_16x16x32_bf16 v[222:225], v[194:197], v[202:205], v[222:225]\n"
    "s_setprio 0\n"
    "v_exp_f32 v231, v231\n"
    "v_exp_f32 v232, v232\n"
    "v_exp_f32 v233, v233\n"
    "v_fmamk_f32 v238, v238, 0x3e553b95, v239\n"
    "v_exp_f32 v238, v238\n"
    "v_fmamk_f32 v234, v234, 0x3e553b95, v239\n"
    "v_fmamk_f32 v235, v235, 0x3e553b95, v239\n"
    "v_fmamk_f32 v236, v236, 0x3e553b95, v239\n"
    "v_fmamk_f32 v237, v237, 0x3e553b95, v239\n"
    "v_cvt_pk_bf16_f32 v198, v226, v227\n"
    "v_cvt_pk_bf16_f32 v199, v228, v229\n"
    "v_cvt_pk_bf16_f32 v200, v230, v231\n"
    "v_cvt_pk_bf16_f32 v201, v232, v233\n"
    "v_exp_f32 v234, v234\n"
    "v_exp_f32 v235, v235\n"
    "s_setprio 2\n"
    "v_mfma_f32_16x16x32_bf16 v[226:229], v[168:171], v[198:201], 0\n"
    "v_exp_f32 v236, v236\n"
    "v_mfma_f32_16x16x32_bf16 v[230:233], v[176:179], v[198:201], 0\n"
    "v_exp_f32 v237, v237\n"
    "v_cvt_pk_bf16_f32 v204, v238, 0\n"
    "v_mfma_f32_16x16x32_bf16 v[238:241], v[194:197], v[198:201], 0\n"
    "v_cvt_pk_bf16_f32 v202, v234, v235\n"
    "v_cvt_pk_bf16_f32 v203, v236, v237\n"
    "v_mfma_f32_16x16x32_bf16 v[234:237], v[184:187], v[198:201], 0\n"
    "v_rcp_f32 v223, v222\n"
    "v_mfma_f32_16x16x32_bf16 v[238:241], v[194:197], v[202:205], v[238:241]\n"
    "v_mul_f32 v210, v210, v223\n"
    "v_mul_f32 v211, v211, v223\n"
    "v_mfma_f32_16x16x32_bf16 v[226:229], v[172:175], v[202:205], v[226:229]\n"
    "v_mul_f32 v212, v212, v223\n"
    "v_mul_f32 v213, v213, v223\n"
    "v_mfma_f32_16x16x32_bf16 v[230:233], v[180:183], v[202:205], v[230:233]\n"
    "v_mul_f32 v214, v214, v223\n"
    "v_mul_f32 v215, v215, v223\n"
    "v_mfma_f32_16x16x32_bf16 v[234:237], v[188:191], v[202:205], v[234:237]\n"
    "s_setprio 0\n"
    "v_rcp_f32 v239, v238\n"
    "v_mul_f32 v216, v216, v223\n"
    "v_mul_f32 v217, v217, v223\n"
    "v_cvt_pk_bf16_f32 v210, v210, v211\n"
    "v_cvt_pk_bf16_f32 v211, v212, v213\n"
    "v_cvt_pk_bf16_f32 v212, v214, v215\n"
    "v_cvt_pk_bf16_f32 v213, v216, v217\n"
    "v_mul_f32 v218, v218, v223\n"
    "v_mul_f32 v219, v219, v223\n"
    "v_mul_f32 v220, v220, v223\n"
    "v_mul_f32 v221, v221, v223\n"
    "s_setprio 1\n"
    "s_waitcnt lgkmcnt(2)\n"
    "v_mfma_f32_16x16x32_bf16 v[222:225], v[242:245], v[72:75], 0\n"
    "s_setprio 2\n"
    "ds_write_b128 v207, v[210:213] offset:49152\n"
    "s_setprio 1\n"
    "v_mfma_f32_16x16x32_bf16 v[210:213], v[0:3], v[242:245], 0\n"
    "v_mul_f32 v226, v226, v239\n"
    "v_mul_f32 v227, v227, v239\n"
    "s_waitcnt lgkmcnt(2)\n"
    "v_mfma_f32_16x16x32_bf16 v[210:213], v[12:15], v[246:249], v[210:213]\n"
    "v_cvt_pk_bf16_f32 v214, v218, v219\n"
    "v_cvt_pk_bf16_f32 v215, v220, v221\n"
    "v_mfma_f32_16x16x32_bf16 v[218:221], v[8:11], v[242:245], 0\n"
    "s_setprio 2\n"
    "ds_write_b64 v208, v[214:215] offset:49152\n"
    "s_setprio 1\n"
    "v_mfma_f32_16x16x32_bf16 v[214:217], v[4:7], v[242:245], 0\n"
    "v_mul_f32 v228, v228, v239\n"
    "v_mul_f32 v229, v229, v239\n"
    "v_mfma_f32_16x16x32_bf16 v[222:225], v[246:249], v[84:87], v[222:225]\n"
    "v_mul_f32 v230, v230, v239\n"
    "v_mul_f32 v231, v231, v239\n"
    "v_mfma_f32_16x16x32_bf16 v[214:217], v[16:19], v[246:249], v[214:217]\n"
    "v_mul_f32 v232, v232, v239\n"
    "v_mul_f32 v233, v233, v239\n"
    "v_mfma_f32_16x16x32_bf16 v[218:221], v[20:23], v[246:249], v[218:221]\n"
    "v_cvt_pk_bf16_f32 v226, v226, v227\n"
    "v_cvt_pk_bf16_f32 v227, v228, v229\n"
    "s_waitcnt lgkmcnt(2)\n"
    "v_mfma_f32_16x16x32_bf16 v[210:213], v[24:27], v[250:253], v[210:213]\n"
    "v_cvt_pk_bf16_f32 v228, v230, v231\n"
    "v_cvt_pk_bf16_f32 v229, v232, v233\n"
    "v_mfma_f32_16x16x32_bf16 v[222:225], v[250:253], v[96:99], v[222:225]\n"
    "s_setprio 2\n"
    "s_mov_b32 exec_lo, 0x100010\n"
    "s_mov_b32 exec_hi, 0x100010\n"
    "ds_write_b128 v207, v[226:229] offset:55296\n"
    "s_mov_b64 exec, -1\n"
    "s_setprio 1\n"
    "v_mfma_f32_16x16x32_bf16 v[226:229], v[242:245], v[76:79], 0\n"
    "v_mul_f32 v234, v234, v239\n"
    "v_mul_f32 v235, v235, v239\n"
    "v_mfma_f32_16x16x32_bf16 v[226:229], v[246:249], v[88:91], v[226:229]\n"
    "v_mul_f32 v236, v236, v239\n"
    "v_mul_f32 v237, v237, v239\n"
    "v_mfma_f32_16x16x32_bf16 v[214:217], v[28:31], v[250:253], v[214:217]\n"
    "s_setprio 3\n"
    "ds_read_b128 v[238:241], v206 offset:4096\n"
    "s_setprio 0\n"
    "v_cvt_pk_bf16_f32 v230, v234, v235\n"
    "v_cvt_pk_bf16_f32 v231, v236, v237\n"
    "s_setprio 1\n"
    "v_mfma_f32_16x16x32_bf16 v[218:221], v[32:35], v[250:253], v[218:221]\n"
    "s_setprio 2\n"
    "s_mov_b32 exec_lo, 0x100010\n"
    "s_mov_b32 exec_hi, 0x100010\n"
    "ds_write_b64 v208, v[230:231] offset:55296\n"
    "s_mov_b64 exec, -1\n"
    "s_setprio 1\n"
    "v_mfma_f32_16x16x32_bf16 v[230:233], v[242:245], v[80:83], 0\n"
    "s_setprio 3\n"
    "ds_read_b128 v[234:237], v206 offset:3072\n"
    "ds_read_b128 v[242:245], v206 offset:5120\n"
    "s_setprio 1\n"
    "v_mfma_f32_16x16x32_bf16 v[230:233], v[246:249], v[92:95], v[230:233]\n"
    "s_setprio 3\n"
    "ds_read_b128 v[246:249], v206 offset:6144\n"
    "s_setprio 1\n"
    "v_mfma_f32_16x16x32_bf16 v[230:233], v[250:253], v[104:107], v[230:233]\n"
    "v_mfma_f32_16x16x32_bf16 v[226:229], v[250:253], v[100:103], v[226:229]\n"
    "s_setprio 3\n"
    "ds_read_b128 v[250:253], v206 offset:7168\n"
    "s_setprio 1\n"
    "s_waitcnt lgkmcnt(3)\n"
    "v_mfma_f32_16x16x32_bf16 v[230:233], v[234:237], v[116:119], v[230:233]\n"
    "v_mfma_f32_16x16x32_bf16 v[210:213], v[36:39], v[234:237], v[210:213]\n"
    "v_mfma_f32_16x16x32_bf16 v[222:225], v[234:237], v[108:111], v[222:225]\n"
    "v_mfma_f32_16x16x32_bf16 v[214:217], v[40:43], v[234:237], v[214:217]\n"
    "v_mfma_f32_16x16x32_bf16 v[230:233], v[238:241], v[128:131], v[230:233]\n"
    "v_mfma_f32_16x16x32_bf16 v[218:221], v[44:47], v[234:237], v[218:221]\n"
    "v_mfma_f32_16x16x32_bf16 v[226:229], v[234:237], v[112:115], v[226:229]\n"
    "s_setprio 3\n"
    "ds_read_b128 v[234:237], v206 offset:8192\n"
    "s_setprio 1\n"
    "v_mfma_f32_16x16x32_bf16 v[210:213], v[48:51], v[238:241], v[210:213]\n"
    "v_mfma_f32_16x16x32_bf16 v[222:225], v[238:241], v[120:123], v[222:225]\n"
    "v_mfma_f32_16x16x32_bf16 v[214:217], v[52:55], v[238:241], v[214:217]\n"
    "s_waitcnt lgkmcnt(3)\n"
    "v_mfma_f32_16x16x32_bf16 v[230:233], v[242:245], v[140:143], v[230:233]\n"
    "v_mfma_f32_16x16x32_bf16 v[218:221], v[56:59], v[238:241], v[218:221]\n"
    "v_mfma_f32_16x16x32_bf16 v[226:229], v[238:241], v[124:127], v[226:229]\n"
    "s_setprio 3\n"
    "ds_read_b128 v[238:241], v206 offset:9216\n"
    "s_setprio 1\n"
    "v_mfma_f32_16x16x32_bf16 v[210:213], v[60:63], v[242:245], v[210:213]\n"
    "v_mfma_f32_16x16x32_bf16 v[222:225], v[242:245], v[132:135], v[222:225]\n"
    "v_mfma_f32_16x16x32_bf16 v[214:217], v[64:67], v[242:245], v[214:217]\n"
    "v_cvt_pk_bf16_f32 v184, v230, v231\n"
    "v_mfma_f32_16x16x32_bf16 v[218:221], v[68:71], v[242:245], v[218:221]\n"
    "v_cvt_pk_bf16_f32 v185, v232, v233\n"
    "v_mfma_f32_16x16x32_bf16 v[226:229], v[242:245], v[136:139], v[226:229]\n"
    "s_setprio 3\n"
    "ds_read_b128 v[242:245], v206 offset:10240\n"
    "s_setprio 1\n"
    "v_cvt_pk_bf16_f32 v144, v210, v211\n"
    "v_cvt_pk_bf16_f32 v145, v212, v213\n"
    "s_waitcnt lgkmcnt(4)\n"
    "v_mfma_f32_16x16x32_bf16 v[230:233], v[246:249], v[80:83], 0\n"
    "v_cvt_pk_bf16_f32 v168, v222, v223\n"
    "v_cvt_pk_bf16_f32 v169, v224, v225\n"
    "v_mfma_f32_16x16x32_bf16 v[210:213], v[0:3], v[246:249], 0\n"
    "v_cvt_pk_bf16_f32 v146, v214, v215\n"
    "v_cvt_pk_bf16_f32 v147, v216, v217\n"
    "v_mfma_f32_16x16x32_bf16 v[222:225], v[246:249], v[72:75], 0\n"
    "v_cvt_pk_bf16_f32 v148, v218, v219\n"
    "v_cvt_pk_bf16_f32 v149, v220, v221\n"
    "v_mfma_f32_16x16x32_bf16 v[214:217], v[4:7], v[246:249], 0\n"
    "v_cvt_pk_bf16_f32 v176, v226, v227\n"
    "v_cvt_pk_bf16_f32 v177, v228, v229\n"
    "s_waitcnt lgkmcnt(3)\n"
    "v_mfma_f32_16x16x32_bf16 v[230:233], v[250:253], v[92:95], v[230:233]\n"
    "v_mfma_f32_16x16x32_bf16 v[218:221], v[8:11], v[246:249], 0\n"
    "v_mfma_f32_16x16x32_bf16 v[226:229], v[246:249], v[76:79], 0\n"
    "s_setprio 3\n"
    "ds_read_b128 v[246:249], v206 offset:11264\n"
    "s_setprio 1\n"
    "v_mfma_f32_16x16x32_bf16 v[210:213], v[12:15], v[250:253], v[210:213]\n"
    "v_mfma_f32_16x16x32_bf16 v[222:225], v[250:253], v[84:87], v[222:225]\n"
    "v_mfma_f32_16x16x32_bf16 v[214:217], v[16:19], v[250:253], v[214:217]\n"
    "s_waitcnt lgkmcnt(3)\n"
    "v_mfma_f32_16x16x32_bf16 v[230:233], v[234:237], v[104:107], v[230:233]\n"
    "v_mfma_f32_16x16x32_bf16 v[218:221], v[20:23], v[250:253], v[218:221]\n"
    "v_mfma_f32_16x16x32_bf16 v[226:229], v[250:253], v[88:91], v[226:229]\n"
    "s_setprio 3\n"
    "ds_read_b128 v[250:253], v206 offset:12288\n"
    "s_setprio 1\n"
    "v_mfma_f32_16x16x32_bf16 v[210:213], v[24:27], v[234:237], v[210:213]\n"
    "v_mfma_f32_16x16x32_bf16 v[222:225], v[234:237], v[96:99], v[222:225]\n"
    "v_mfma_f32_16x16x32_bf16 v[214:217], v[28:31], v[234:237], v[214:217]\n"
    "s_waitcnt lgkmcnt(3)\n"
    "v_mfma_f32_16x16x32_bf16 v[230:233], v[238:241], v[116:119], v[230:233]\n"
    "v_mfma_f32_16x16x32_bf16 v[218:221], v[32:35], v[234:237], v[218:221]\n"
    "v_mfma_f32_16x16x32_bf16 v[226:229], v[234:237], v[100:103], v[226:229]\n"
    "s_setprio 3\n"
    "ds_read_b128 v[234:237], v206 offset:13312\n"
    "s_setprio 1\n"
    "v_mfma_f32_16x16x32_bf16 v[210:213], v[36:39], v[238:241], v[210:213]\n"
    "v_mfma_f32_16x16x32_bf16 v[222:225], v[238:241], v[108:111], v[222:225]\n"
    "v_mfma_f32_16x16x32_bf16 v[214:217], v[40:43], v[238:241], v[214:217]\n"
    "s_waitcnt lgkmcnt(3)\n"
    "v_mfma_f32_16x16x32_bf16 v[230:233], v[242:245], v[128:131], v[230:233]\n"
    "v_mfma_f32_16x16x32_bf16 v[218:221], v[44:47], v[238:241], v[218:221]\n"
    "v_mfma_f32_16x16x32_bf16 v[226:229], v[238:241], v[112:115], v[226:229]\n"
    "s_setprio 3\n"
    "ds_read_b128 v[238:241], v206 offset:14336\n"
    "s_setprio 1\n"
    "v_mfma_f32_16x16x32_bf16 v[210:213], v[48:51], v[242:245], v[210:213]\n"
    "v_mfma_f32_16x16x32_bf16 v[222:225], v[242:245], v[120:123], v[222:225]\n"
    "v_mfma_f32_16x16x32_bf16 v[214:217], v[52:55], v[242:245], v[214:217]\n"
    "s_waitcnt lgkmcnt(3)\n"
    "v_mfma_f32_16x16x32_bf16 v[230:233], v[246:249], v[140:143], v[230:233]\n"
    "v_mfma_f32_16x16x32_bf16 v[218:221], v[56:59], v[242:245], v[218:221]\n"
    "v_mfma_f32_16x16x32_bf16 v[226:229], v[242:245], v[124:127], v[226:229]\n"
    "s_setprio 3\n"
    "ds_read_b128 v[242:245], v206 offset:15360\n"
    "s_setprio 1\n"
    "v_mfma_f32_16x16x32_bf16 v[210:213], v[60:63], v[246:249], v[210:213]\n"
    "v_mfma_f32_16x16x32_bf16 v[222:225], v[246:249], v[132:135], v[222:225]\n"
    "v_mfma_f32_16x16x32_bf16 v[214:217], v[64:67], v[246:249], v[214:217]\n"
    "v_cvt_pk_bf16_f32 v186, v230, v231\n"
    "v_mfma_f32_16x16x32_bf16 v[218:221], v[68:71], v[246:249], v[218:221]\n"
    "v_cvt_pk_bf16_f32 v187, v232, v233\n"
    "v_mfma_f32_16x16x32_bf16 v[226:229], v[246:249], v[136:139], v[226:229]\n"
    "s_setprio 3\n"
    "ds_read_b128 v[246:249], v206 offset:16384\n"
    "s_setprio 1\n"
    "v_cvt_pk_bf16_f32 v150, v210, v211\n"
    "v_cvt_pk_bf16_f32 v151, v212, v213\n"
    "s_waitcnt lgkmcnt(4)\n"
    "v_mfma_f32_16x16x32_bf16 v[230:233], v[250:253], v[80:83], 0\n"
    "v_cvt_pk_bf16_f32 v170, v222, v223\n"
    "v_cvt_pk_bf16_f32 v171, v224, v225\n"
    "v_mfma_f32_16x16x32_bf16 v[210:213], v[0:3], v[250:253], 0\n"
    "v_cvt_pk_bf16_f32 v152, v214, v215\n"
    "v_cvt_pk_bf16_f32 v153, v216, v217\n"
    "v_mfma_f32_16x16x32_bf16 v[222:225], v[250:253], v[72:75], 0\n"
    "v_cvt_pk_bf16_f32 v154, v218, v219\n"
    "v_cvt_pk_bf16_f32 v155, v220, v221\n"
    "v_mfma_f32_16x16x32_bf16 v[214:217], v[4:7], v[250:253], 0\n"
    "v_cvt_pk_bf16_f32 v178, v226, v227\n"
    "v_cvt_pk_bf16_f32 v179, v228, v229\n"
    "s_waitcnt lgkmcnt(3)\n"
    "v_mfma_f32_16x16x32_bf16 v[230:233], v[234:237], v[92:95], v[230:233]\n"
    "v_mfma_f32_16x16x32_bf16 v[218:221], v[8:11], v[250:253], 0\n"
    "v_mfma_f32_16x16x32_bf16 v[226:229], v[250:253], v[76:79], 0\n"
    "s_setprio 3\n"
    "ds_read_b128 v[250:253], v206 offset:17408\n"
    "s_setprio 1\n"
    "v_mfma_f32_16x16x32_bf16 v[210:213], v[12:15], v[234:237], v[210:213]\n"
    "v_mfma_f32_16x16x32_bf16 v[222:225], v[234:237], v[84:87], v[222:225]\n"
    "v_mfma_f32_16x16x32_bf16 v[214:217], v[16:19], v[234:237], v[214:217]\n"
    "s_waitcnt lgkmcnt(3)\n"
    "v_mfma_f32_16x16x32_bf16 v[230:233], v[238:241], v[104:107], v[230:233]\n"
    "v_mfma_f32_16x16x32_bf16 v[218:221], v[20:23], v[234:237], v[218:221]\n"
    "v_mfma_f32_16x16x32_bf16 v[226:229], v[234:237], v[88:91], v[226:229]\n"
    "v_mfma_f32_16x16x32_bf16 v[210:213], v[24:27], v[238:241], v[210:213]\n"
    "v_mfma_f32_16x16x32_bf16 v[222:225], v[238:241], v[96:99], v[222:225]\n"
    "v_mfma_f32_16x16x32_bf16 v[214:217], v[28:31], v[238:241], v[214:217]\n"
    "s_waitcnt lgkmcnt(2)\n"
    "v_mfma_f32_16x16x32_bf16 v[230:233], v[242:245], v[116:119], v[230:233]\n"
    "v_mfma_f32_16x16x32_bf16 v[218:221], v[32:35], v[238:241], v[218:221]\n"
    "v_mfma_f32_16x16x32_bf16 v[226:229], v[238:241], v[100:103], v[226:229]\n"
    "v_mfma_f32_16x16x32_bf16 v[210:213], v[36:39], v[242:245], v[210:213]\n"
    "v_mfma_f32_16x16x32_bf16 v[222:225], v[242:245], v[108:111], v[222:225]\n"
    "v_mfma_f32_16x16x32_bf16 v[214:217], v[40:43], v[242:245], v[214:217]\n"
    "s_waitcnt lgkmcnt(1)\n"
    "v_mfma_f32_16x16x32_bf16 v[230:233], v[246:249], v[128:131], v[230:233]\n"
    "v_mfma_f32_16x16x32_bf16 v[218:221], v[44:47], v[242:245], v[218:221]\n"
    "v_mfma_f32_16x16x32_bf16 v[226:229], v[242:245], v[112:115], v[226:229]\n"
    "v_mfma_f32_16x16x32_bf16 v[210:213], v[48:51], v[246:249], v[210:213]\n"
    "v_mfma_f32_16x16x32_bf16 v[222:225], v[246:249], v[120:123], v[222:225]\n"
    "v_mfma_f32_16x16x32_bf16 v[214:217], v[52:55], v[246:249], v[214:217]\n"
    "s_waitcnt lgkmcnt(0)\n"
    "v_mfma_f32_16x16x32_bf16 v[230:233], v[250:253], v[140:143], v[230:233]\n"
    "s_barrier\n"
    "s_setprio 2\n"
    "ds_read_b128 v[242:245], v207 offset:18432\n"
    "s_setprio 1\n"
    "v_mfma_f32_16x16x32_bf16 v[218:221], v[56:59], v[246:249], v[218:221]\n"
    "v_mfma_f32_16x16x32_bf16 v[226:229], v[246:249], v[124:127], v[226:229]\n"
    "s_setprio 2\n"
    "ds_read_b64 v[246:247], v208 offset:18432\n"
    "s_setprio 1\n"
    "v_mfma_f32_16x16x32_bf16 v[210:213], v[60:63], v[250:253], v[210:213]\n"
    "v_cvt_pk_bf16_f32 v188, v230, v231\n"
    "v_mfma_f32_16x16x32_bf16 v[222:225], v[250:253], v[132:135], v[222:225]\n"
    "v_cvt_pk_bf16_f32 v189, v232, v233\n"
    "v_mfma_f32_16x16x32_bf16 v[214:217], v[64:67], v[250:253], v[214:217]\n"
    "s_nop 3\n"
    "v_cvt_pk_bf16_f32 v156, v210, v211\n"
    "v_mfma_f32_16x16x32_bf16 v[218:221], v[68:71], v[250:253], v[218:221]\n"
    "v_cvt_pk_bf16_f32 v157, v212, v213\n"
    "v_mfma_f32_16x16x32_bf16 v[226:229], v[250:253], v[136:139], v[226:229]\n"
    "s_setprio 2\n"
    "ds_read_b128 v[248:251], v207 offset:24576\n"
    "ds_read_b64 v[252:253], v208 offset:24576\n"
    "s_setprio 1\n"
    "v_cvt_pk_bf16_f32 v172, v222, v223\n"
    "s_setprio 2\n"
    "s_waitcnt lgkmcnt(3)\n"
    "v_mfma_f32_16x16x32_bf16 v[210:213], v[144:147], v[242:245], 0\n"
    "s_setprio 1\n"
    "v_cvt_pk_bf16_f32 v173, v224, v225\n"
    "v_cvt_pk_bf16_f32 v158, v214, v215\n"
    "s_setprio 2\n"
    "v_mfma_f32_16x16x32_bf16 v[222:225], v[162:165], v[242:245], v[192:195]\n"
    "s_setprio 1\n"
    "v_cvt_pk_bf16_f32 v159, v216, v217\n"
    "v_cvt_pk_bf16_f32 v160, v218, v219\n"
    "s_setprio 2\n"
    "v_mfma_f32_16x16x32_bf16 v[214:217], v[150:153], v[242:245], 0\n"
    "s_setprio 1\n"
    "v_cvt_pk_bf16_f32 v161, v220, v221\n"
    "v_cvt_pk_bf16_f32 v180, v226, v227\n"
    "s_setprio 2\n"
    "v_mfma_f32_16x16x32_bf16 v[218:221], v[156:159], v[242:245], 0\n"
    "ds_read_b128 v[242:245], v207 offset:30720\n"
    "s_setprio 1\n"
    "v_cvt_pk_bf16_f32 v181, v228, v229\n"
    "s_setprio 2\n"
    "s_waitcnt lgkmcnt(2)\n"
    "v_mfma_f32_16x16x32_bf16 v[226:229], v[144:147], v[248:251], 0\n"
    "v_mfma_f32_16x16x32_bf16 v[238:241], v[162:165], v[248:251], v[192:195]\n"
    "v_mfma_f32_16x16x16_bf16 v[210:213], v[148:149], v[246:247], v[210:213]\n"
    "v_mfma_f32_16x16x16_bf16 v[222:225], v[166:167], v[246:247], v[222:225]\n"
    "v_mfma_f32_16x16x32_bf16 v[230:233], v[150:153], v[248:251], 0\n"
    "v_mfma_f32_16x16x16_bf16 v[214:217], v[154:155], v[246:247], v[214:217]\n"
    "v_mfma_f32_16x16x32_bf16 v[234:237], v[156:159], v[248:251], 0\n"
    "ds_read_b128 v[248:251], v207 offset:55296\n"
    "v_mfma_f32_16x16x16_bf16 v[218:221], v[160:161], v[246:247], v[218:221]\n"
    "ds_read_b64 v[246:247], v208 offset:30720\n"
    "s_waitcnt lgkmcnt(3)\n"
    "v_mfma_f32_16x16x16_bf16 v[226:229], v[148:149], v[252:253], v[226:229]\n"
    "v_max3_f32 v223, v222, v210, v211\n"
    "v_mfma_f32_16x16x16_bf16 v[238:241], v[166:167], v[252:253], v[238:241]\n"
    "v_max3_f32 v224, v212, v213, v214\n"
    "v_max3_f32 v223, v223, v215, v216\n"
    "v_mfma_f32_16x16x16_bf16 v[230:233], v[154:155], v[252:253], v[230:233]\n"
    "v_max3_f32 v224, v224, v217, v218\n"
    "v_max3_f32 v223, v223, v219, v220\n"
    "v_mfma_f32_16x16x16_bf16 v[234:237], v[160:161], v[252:253], v[234:237]\n"
    "ds_read_b64 v[252:253], v208 offset:55296\n"
    "s_setprio 0\n"
    "v_max3_f32 v223, v223, v224, v221\n"
    "v_mov_b32 v224, v223\n"
    "v_max3_f32 v239, v238, v226, v227\n"
    "v_max3_f32 v240, v228, v229, v230\n"
    "v_max3_f32 v239, v239, v231, v232\n"
    "v_permlane32_swap_b32 v223, v224\n"
    "v_max3_f32 v240, v240, v233, v234\n"
    "v_max3_f32 v239, v239, v235, v236\n"
    "v_max3_f32 v239, v239, v240, v237\n"
    "v_max_f32 v223, v223, v224\n"
    "v_mov_b32 v240, v239\n"
    "v_mov_b32 v224, v223\n"
    "s_nop 0\n"
    "v_permlane32_swap_b32 v239, v240\n"
    "v_permlane16_swap_b32 v223, v224\n"
    "v_max_f32 v239, v239, v240\n"
    "v_max_f32 v223, v223, v224\n"
    "v_mov_b32 v240, v239\n"
    "v_mul_f32 v223, 0xbe553b95, v223\n"
    "v_fmamk_f32 v210, v210, 0x3e553b95, v223\n"
    "v_fmamk_f32 v211, v211, 0x3e553b95, v223\n"
    "v_fmamk_f32 v212, v212, 0x3e553b95, v223\n"
    "v_fmamk_f32 v213, v213, 0x3e553b95, v223\n"
    "v_fmamk_f32 v214, v214, 0x3e553b95, v223\n"
    "v_fmamk_f32 v215, v215, 0x3e553b95, v223\n"
    "v_fmamk_f32 v216, v216, 0x3e553b95, v223\n"
    "v_fmamk_f32 v217, v217, 0x3e553b95, v223\n"
    "v_permlane16_swap_b32 v239, v240\n"
    "v_exp_f32 v210, v210\n"
    "v_exp_f32 v211, v211\n"
    "v_exp_f32 v212, v212\n"
    "v_exp_f32 v213, v213\n"
    "v_exp_f32 v214, v214\n"
    "v_exp_f32 v215, v215\n"
    "v_exp_f32 v216, v216\n"
    "v_exp_f32 v217, v217\n"
    "v_fmamk_f32 v218, v218, 0x3e553b95, v223\n"
    "v_fmamk_f32 v219, v219, 0x3e553b95, v223\n"
    "v_fmamk_f32 v220, v220, 0x3e553b95, v223\n"
    "v_fmamk_f32 v221, v221, 0x3e553b95, v223\n"
    "v_fmamk_f32 v222, v222, 0x3e553b95, v223\n"
    "v_exp_f32 v218, v218\n"
    "v_exp_f32 v219, v219\n"
    "v_exp_f32 v220, v220\n"
    "v_exp_f32 v221, v221\n"
    "v_exp_f32 v222, v222\n"
    "v_max_f32 v239, v239, v240\n"
    "v_cvt_pk_bf16_f32 v198, v210, v211\n"
    "v_cvt_pk_bf16_f32 v199, v212, v213\n"
    "v_cvt_pk_bf16_f32 v200, v214, v215\n"
    "v_cvt_pk_bf16_f32 v201, v216, v217\n"
    "v_mul_f32 v239, 0xbe553b95, v239\n"
    "v_fmamk_f32 v226, v226, 0x3e553b95, v239\n"
    "s_setprio 2\n"
    "v_mfma_f32_16x16x32_bf16 v[210:213], v[168:171], v[198:201], 0\n"
    "v_fmamk_f32 v227, v227, 0x3e553b95, v239\n"
    "v_fmamk_f32 v228, v228, 0x3e553b95, v239\n"
    "v_mfma_f32_16x16x32_bf16 v[214:217], v[176:179], v[198:201], 0\n"
    "s_setprio 0\n"
    "v_fmamk_f32 v229, v229, 0x3e553b95, v239\n"
    "v_fmamk_f32 v230, v230, 0x3e553b95, v239\n"
    "v_fmamk_f32 v231, v231, 0x3e553b95, v239\n"
    "v_fmamk_f32 v232, v232, 0x3e553b95, v239\n"
    "v_fmamk_f32 v233, v233, 0x3e553b95, v239\n"
    "v_cvt_pk_bf16_f32 v202, v218, v219\n"
    "v_cvt_pk_bf16_f32 v203, v220, v221\n"
    "s_setprio 2\n"
    "v_mfma_f32_16x16x32_bf16 v[218:221], v[184:187], v[198:201], 0\n"
    "v_cvt_pk_bf16_f32 v204, v222, 0\n"
    "v_exp_f32 v226, v226\n"
    "v_mfma_f32_16x16x32_bf16 v[222:225], v[194:197], v[198:201], 0\n"
    "v_exp_f32 v227, v227\n"
    "v_mfma_f32_16x16x32_bf16 v[210:213], v[172:175], v[202:205], v[210:213]\n"
    "v_exp_f32 v228, v228\n"
    "v_mfma_f32_16x16x32_bf16 v[214:217], v[180:183], v[202:205], v[214:217]\n"
    "v_exp_f32 v229, v229\n"
    "v_mfma_f32_16x16x32_bf16 v[218:221], v[188:191], v[202:205], v[218:221]\n"
    "v_exp_f32 v230, v230\n"
    "v_mfma_f32_16x16x32_bf16 v[222:225], v[194:197], v[202:205], v[222:225]\n"
    "s_setprio 0\n"
    "v_exp_f32 v231, v231\n"
    "v_exp_f32 v232, v232\n"
    "v_exp_f32 v233, v233\n"
    "v_fmamk_f32 v238, v238, 0x3e553b95, v239\n"
    "v_exp_f32 v238, v238\n"
    "v_fmamk_f32 v234, v234, 0x3e553b95, v239\n"
    "v_fmamk_f32 v235, v235, 0x3e553b95, v239\n"
    "v_fmamk_f32 v236, v236, 0x3e553b95, v239\n"
    "v_fmamk_f32 v237, v237, 0x3e553b95, v239\n"
    "v_cvt_pk_bf16_f32 v198, v226, v227\n"
    "v_cvt_pk_bf16_f32 v199, v228, v229\n"
    "v_cvt_pk_bf16_f32 v200, v230, v231\n"
    "v_cvt_pk_bf16_f32 v201, v232, v233\n"
    "v_exp_f32 v234, v234\n"
    "v_exp_f32 v235, v235\n"
    "s_setprio 2\n"
    "v_mfma_f32_16x16x32_bf16 v[226:229], v[168:171], v[198:201], 0\n"
    "v_exp_f32 v236, v236\n"
    "v_mfma_f32_16x16x32_bf16 v[230:233], v[176:179], v[198:201], 0\n"
    "v_exp_f32 v237, v237\n"
    "v_cvt_pk_bf16_f32 v204, v238, 0\n"
    "v_mfma_f32_16x16x32_bf16 v[238:241], v[194:197], v[198:201], 0\n"
    "v_cvt_pk_bf16_f32 v202, v234, v235\n"
    "v_cvt_pk_bf16_f32 v203, v236, v237\n"
    "v_mfma_f32_16x16x32_bf16 v[234:237], v[184:187], v[198:201], 0\n"
    "v_rcp_f32 v223, v222\n"
    "v_mfma_f32_16x16x32_bf16 v[238:241], v[194:197], v[202:205], v[238:241]\n"
    "v_mul_f32 v210, v210, v223\n"
    "v_mul_f32 v211, v211, v223\n"
    "v_mfma_f32_16x16x32_bf16 v[226:229], v[172:175], v[202:205], v[226:229]\n"
    "v_mul_f32 v212, v212, v223\n"
    "v_mul_f32 v213, v213, v223\n"
    "v_mfma_f32_16x16x32_bf16 v[230:233], v[180:183], v[202:205], v[230:233]\n"
    "v_mul_f32 v214, v214, v223\n"
    "v_mul_f32 v215, v215, v223\n"
    "v_mfma_f32_16x16x32_bf16 v[234:237], v[188:191], v[202:205], v[234:237]\n"
    "s_setprio 0\n"
    "v_mul_f32 v216, v216, v223\n"
    "v_mul_f32 v217, v217, v223\n"
    "v_cvt_pk_bf16_f32 v210, v210, v211\n"
    "v_cvt_pk_bf16_f32 v211, v212, v213\n"
    "v_cvt_pk_bf16_f32 v212, v214, v215\n"
    "v_cvt_pk_bf16_f32 v213, v216, v217\n"
    "v_rcp_f32 v239, v238\n"
    "v_mul_f32 v218, v218, v223\n"
    "v_mul_f32 v219, v219, v223\n"
    "v_mul_f32 v220, v220, v223\n"
    "v_mul_f32 v221, v221, v223\n"
    "s_setprio 2\n"
    "s_waitcnt lgkmcnt(3)\n"
    "v_mfma_f32_16x16x32_bf16 v[222:225], v[162:165], v[242:245], v[192:195]\n"
    "ds_write_b128 v207, v[210:213] offset:18432\n"
    "v_mfma_f32_16x16x32_bf16 v[210:213], v[144:147], v[242:245], 0\n"
    "v_cvt_pk_bf16_f32 v214, v218, v219\n"
    "v_cvt_pk_bf16_f32 v215, v220, v221\n"
    "v_mfma_f32_16x16x32_bf16 v[218:221], v[156:159], v[242:245], 0\n"
    "ds_write_b64 v208, v[214:215] offset:18432\n"
    "v_mfma_f32_16x16x32_bf16 v[214:217], v[150:153], v[242:245], 0\n"
    "v_mul_f32 v226, v226, v239\n"
    "v_mul_f32 v227, v227, v239\n"
    "s_waitcnt lgkmcnt(3)\n"
    "v_mfma_f32_16x16x16_bf16 v[210:213], v[148:149], v[246:247], v[210:213]\n"
    "v_mul_f32 v228, v228, v239\n"
    "v_mul_f32 v229, v229, v239\n"
    "v_mfma_f32_16x16x16_bf16 v[222:225], v[166:167], v[246:247], v[222:225]\n"
    "v_mul_f32 v230, v230, v239\n"
    "v_mul_f32 v231, v231, v239\n"
    "v_mfma_f32_16x16x16_bf16 v[214:217], v[154:155], v[246:247], v[214:217]\n"
    "v_mul_f32 v232, v232, v239\n"
    "v_mul_f32 v233, v233, v239\n"
    "v_mfma_f32_16x16x16_bf16 v[218:221], v[160:161], v[246:247], v[218:221]\n"
    "s_setprio 0\n"
    "v_cvt_pk_bf16_f32 v226, v226, v227\n"
    "v_cvt_pk_bf16_f32 v227, v228, v229\n"
    "v_cvt_pk_bf16_f32 v228, v230, v231\n"
    "v_cvt_pk_bf16_f32 v229, v232, v233\n"
    "v_mul_f32 v234, v234, v239\n"
    "v_mul_f32 v235, v235, v239\n"
    "v_mul_f32 v236, v236, v239\n"
    "v_mul_f32 v237, v237, v239\n"
    "s_setprio 2\n"
    "v_mfma_f32_16x16x32_bf16 v[238:241], v[162:165], v[248:251], v[192:195]\n"
    "ds_write_b128 v207, v[226:229] offset:24576\n"
    "v_mfma_f32_16x16x32_bf16 v[226:229], v[144:147], v[248:251], 0\n"
    "v_cvt_pk_bf16_f32 v230, v234, v235\n"
    "v_cvt_pk_bf16_f32 v231, v236, v237\n"
    "v_mfma_f32_16x16x32_bf16 v[234:237], v[156:159], v[248:251], 0\n"
    "ds_write_b64 v208, v[230:231] offset:24576\n"
    "v_mfma_f32_16x16x32_bf16 v[230:233], v[150:153], v[248:251], 0\n"
    "v_max3_f32 v223, v222, v210, v211\n"
    "v_max3_f32 v224, v212, v213, v214\n"
    "s_waitcnt lgkmcnt(4)\n"
    "v_mfma_f32_16x16x16_bf16 v[226:229], v[148:149], v[252:253], v[226:229]\n"
    "v_max3_f32 v223, v223, v215, v216\n"
    "v_max3_f32 v224, v224, v217, v218\n"
    "v_mfma_f32_16x16x16_bf16 v[238:241], v[166:167], v[252:253], v[238:241]\n"
    "v_max3_f32 v223, v223, v219, v220\n"
    "v_max3_f32 v223, v223, v224, v221\n"
    "v_mfma_f32_16x16x16_bf16 v[230:233], v[154:155], v[252:253], v[230:233]\n"
    "v_mov_b32 v224, v223\n"
    "v_cndmask_b32_e64 v192, v209, 0, s[60:61]\n"
    "v_mfma_f32_16x16x16_bf16 v[234:237], v[160:161], v[252:253], v[234:237]\n"
    "s_setprio 0\n"
    "v_permlane32_swap_b32 v223, v224\n"
    "v_max3_f32 v239, v238, v226, v227\n"
    "v_max_f32 v223, v223, v224\n"
    "v_mov_b32 v224, v223\n"
    "v_max3_f32 v240, v228, v229, v230\n"
    "v_max3_f32 v239, v239, v231, v232\n"
    "v_permlane16_swap_b32 v223, v224\n"
    "v_max3_f32 v240, v240, v233, v234\n"
    "v_max3_f32 v239, v239, v235, v236\n"
    "v_max3_f32 v239, v239, v240, v237\n"
    "v_mov_b32 v240, v239\n"
    "v_max_f32 v223, v223, v224\n"
    "v_mul_f32 v223, 0xbe553b95, v223\n"
    "s_barrier\n"
    "s_setprio 3\n"
    "ds_read_b128 v[242:245], v206 offset:2048\n"
    "ds_read_b128 v[246:249], v206 offset:3072\n"
    "ds_read_b128 v[250:253], v206 offset:4096\n"
    "s_setprio 0\n"
    "v_permlane32_swap_b32 v239, v240\n"
    "v_fmamk_f32 v210, v210, 0x3e553b95, v223\n"
    "v_max_f32 v239, v239, v240\n"
    "v_mov_b32 v240, v239\n"
    "v_fmamk_f32 v211, v211, 0x3e553b95, v223\n"
    "v_fmamk_f32 v212, v212, 0x3e553b95, v223\n"
    "v_fmamk_f32 v213, v213, 0x3e553b95, v223\n"
    "v_fmamk_f32 v214, v214, 0x3e553b95, v223\n"
    "v_fmamk_f32 v215, v215, 0x3e553b95, v223\n"
    "v_fmamk_f32 v216, v216, 0x3e553b95, v223\n"
    "v_fmamk_f32 v217, v217, 0x3e553b95, v223\n"
    "v_permlane16_swap_b32 v239, v240\n"
    "v_exp_f32 v210, v210\n"
    "v_exp_f32 v211, v211\n"
    "v_exp_f32 v212, v212\n"
    "v_exp_f32 v213, v213\n"
    "v_exp_f32 v214, v214\n"
    "v_exp_f32 v215, v215\n"
    "v_exp_f32 v216, v216\n"
    "v_exp_f32 v217, v217\n"
    "v_fmamk_f32 v218, v218, 0x3e553b95, v223\n"
    "v_fmamk_f32 v219, v219, 0x3e553b95, v223\n"
    "v_fmamk_f32 v220, v220, 0x3e553b95, v223\n"
    "v_fmamk_f32 v221, v221, 0x3e553b95, v223\n"
    "v_fmamk_f32 v222, v222, 0x3e553b95, v223\n"
    "v_exp_f32 v218, v218\n"
    "v_exp_f32 v219, v219\n"
    "v_exp_f32 v220, v220\n"
    "v_exp_f32 v221, v221\n"
    "v_exp_f32 v222, v222\n"
    "v_max_f32 v239, v239, v240\n"
    "v_cvt_pk_bf16_f32 v198, v210, v211\n"
    "v_cvt_pk_bf16_f32 v199, v212, v213\n"
    "v_cvt_pk_bf16_f32 v200, v214, v215\n"
    "v_cvt_pk_bf16_f32 v201, v216, v217\n"
    "v_mul_f32 v239, 0xbe553b95, v239\n"
    "v_fmamk_f32 v226, v226, 0x3e553b95, v239\n"
    "s_setprio 2\n"
    "v_mfma_f32_16x16x32_bf16 v[210:213], v[168:171], v[198:201], 0\n"
    "v_fmamk_f32 v227, v227, 0x3e553b95, v239\n"
    "v_fmamk_f32 v228, v228, 0x3e553b95, v239\n"
    "v_mfma_f32_16x16x32_bf16 v[214:217], v[176:179], v[198:201], 0\n"
    "s_setprio 0\n"
    "v_fmamk_f32 v229, v229, 0x3e553b95, v239\n"
    "v_fmamk_f32 v230, v230, 0x3e553b95, v239\n"
    "v_fmamk_f32 v231, v231, 0x3e553b95, v239\n"
    "v_fmamk_f32 v232, v232, 0x3e553b95, v239\n"
    "v_fmamk_f32 v233, v233, 0x3e553b95, v239\n"
    "v_cvt_pk_bf16_f32 v202, v218, v219\n"
    "v_cvt_pk_bf16_f32 v203, v220, v221\n"
    "s_setprio 2\n"
    "v_mfma_f32_16x16x32_bf16 v[218:221], v[184:187], v[198:201], 0\n"
    "v_cvt_pk_bf16_f32 v204, v222, 0\n"
    "v_exp_f32 v226, v226\n"
    "v_mfma_f32_16x16x32_bf16 v[222:225], v[194:197], v[198:201], 0\n"
    "v_exp_f32 v227, v227\n"
    "v_mfma_f32_16x16x32_bf16 v[210:213], v[172:175], v[202:205], v[210:213]\n"
    "v_exp_f32 v228, v228\n"
    "v_mfma_f32_16x16x32_bf16 v[214:217], v[180:183], v[202:205], v[214:217]\n"
    "v_exp_f32 v229, v229\n"
    "v_mfma_f32_16x16x32_bf16 v[218:221], v[188:191], v[202:205], v[218:221]\n"
    "v_exp_f32 v230, v230\n"
    "v_mfma_f32_16x16x32_bf16 v[222:225], v[194:197], v[202:205], v[222:225]\n"
    "s_setprio 0\n"
    "v_exp_f32 v231, v231\n"
    "v_exp_f32 v232, v232\n"
    "v_exp_f32 v233, v233\n"
    "v_fmamk_f32 v238, v238, 0x3e553b95, v239\n"
    "v_exp_f32 v238, v238\n"
    "v_fmamk_f32 v234, v234, 0x3e553b95, v239\n"
    "v_fmamk_f32 v235, v235, 0x3e553b95, v239\n"
    "v_fmamk_f32 v236, v236, 0x3e553b95, v239\n"
    "v_fmamk_f32 v237, v237, 0x3e553b95, v239\n"
    "v_cvt_pk_bf16_f32 v198, v226, v227\n"
    "v_cvt_pk_bf16_f32 v199, v228, v229\n"
    "v_cvt_pk_bf16_f32 v200, v230, v231\n"
    "v_cvt_pk_bf16_f32 v201, v232, v233\n"
    "v_exp_f32 v234, v234\n"
    "v_exp_f32 v235, v235\n"
    "s_setprio 2\n"
    "v_mfma_f32_16x16x32_bf16 v[226:229], v[168:171], v[198:201], 0\n"
    "v_exp_f32 v236, v236\n"
    "v_mfma_f32_16x16x32_bf16 v[230:233], v[176:179], v[198:201], 0\n"
    "v_exp_f32 v237, v237\n"
    "v_cvt_pk_bf16_f32 v204, v238, 0\n"
    "v_mfma_f32_16x16x32_bf16 v[238:241], v[194:197], v[198:201], 0\n"
    "v_cvt_pk_bf16_f32 v202, v234, v235\n"
    "v_cvt_pk_bf16_f32 v203, v236, v237\n"
    "v_mfma_f32_16x16x32_bf16 v[234:237], v[184:187], v[198:201], 0\n"
    "v_rcp_f32 v223, v222\n"
    "v_mfma_f32_16x16x32_bf16 v[238:241], v[194:197], v[202:205], v[238:241]\n"
    "v_mul_f32 v210, v210, v223\n"
    "v_mul_f32 v211, v211, v223\n"
    "v_mfma_f32_16x16x32_bf16 v[226:229], v[172:175], v[202:205], v[226:229]\n"
    "v_mul_f32 v212, v212, v223\n"
    "v_mul_f32 v213, v213, v223\n"
    "v_mfma_f32_16x16x32_bf16 v[230:233], v[180:183], v[202:205], v[230:233]\n"
    "v_mul_f32 v214, v214, v223\n"
    "v_mul_f32 v215, v215, v223\n"
    "v_mfma_f32_16x16x32_bf16 v[234:237], v[188:191], v[202:205], v[234:237]\n"
    "s_setprio 0\n"
    "v_rcp_f32 v239, v238\n"
    "v_mul_f32 v216, v216, v223\n"
    "v_mul_f32 v226, v226, v239\n"
    "v_mul_f32 v227, v227, v239\n"
    "v_mul_f32 v228, v228, v239\n"
    "v_mul_f32 v229, v229, v239\n"
    "v_mul_f32 v230, v230, v239\n"
    "v_mul_f32 v231, v231, v239\n"
    "v_cvt_pk_bf16_f32 v226, v226, v227\n"
    "v_mul_f32 v234, v234, v239\n"
    "v_mul_f32 v235, v235, v239\n"
    "v_mul_f32 v236, v236, v239\n"
    "v_mul_f32 v237, v237, v239\n"
    "v_cvt_pk_bf16_f32 v227, v228, v229\n"
    "v_cvt_pk_bf16_f32 v228, v230, v231\n"
    "v_mul_f32 v217, v217, v223\n"
    "v_cvt_pk_bf16_f32 v210, v210, v211\n"
    "v_cvt_pk_bf16_f32 v230, v234, v235\n"
    "v_cvt_pk_bf16_f32 v231, v236, v237\n"
    "s_setprio 3\n"
    "ds_read_b128 v[234:237], v206 offset:0\n"
    "s_setprio 0\n"
    "v_cvt_pk_bf16_f32 v211, v212, v213\n"
    "v_cvt_pk_bf16_f32 v212, v214, v215\n"
    "v_cvt_pk_bf16_f32 v213, v216, v217\n"
    "v_mul_f32 v218, v218, v223\n"
    "v_mul_f32 v219, v219, v223\n"
    "v_mul_f32 v220, v220, v223\n"
    "v_mul_f32 v221, v221, v223\n"
    "s_setprio 2\n"
    "ds_write_b128 v207, v[210:213] offset:30720\n"
    "s_setprio 1\n"
    "s_waitcnt lgkmcnt(1)\n"
    "v_mfma_f32_16x16x32_bf16 v[210:213], v[0:3], v[234:237], 0\n"
    "v_cvt_pk_bf16_f32 v214, v218, v219\n"
    "v_cvt_pk_bf16_f32 v215, v220, v221\n"
    "v_mfma_f32_16x16x32_bf16 v[222:225], v[234:237], v[72:75], 0\n"
    "s_setprio 2\n"
    "ds_write_b64 v208, v[214:215] offset:30720\n"
    "s_setprio 1\n"
    "v_mfma_f32_16x16x32_bf16 v[214:217], v[4:7], v[234:237], 0\n"
    "v_mul_f32 v232, v232, v239\n"
    "v_mul_f32 v233, v233, v239\n"
    "v_mfma_f32_16x16x32_bf16 v[218:221], v[8:11], v[234:237], 0\n"
    "s_setprio 3\n"
    "ds_read_b128 v[238:241], v206 offset:1024\n"
    "s_setprio 0\n"
    "v_cvt_pk_bf16_f32 v229, v232, v233\n"
    "s_setprio 2\n"
    "s_mov_b32 exec_lo, 0x1000100\n"
    "s_mov_b32 exec_hi, 0x1000100\n"
    "ds_write_b128 v207, v[226:229] offset:55296\n"
    "s_mov_b64 exec, -1\n"
    "s_setprio 1\n"
    "v_mfma_f32_16x16x32_bf16 v[226:229], v[234:237], v[76:79], 0\n"
    "s_setprio 2\n"
    "s_mov_b32 exec_lo, 0x1000100\n"
    "s_mov_b32 exec_hi, 0x1000100\n"
    "ds_write_b64 v208, v[230:231] offset:55296\n"
    "s_mov_b64 exec, -1\n"
    "s_setprio 1\n"
    "v_mfma_f32_16x16x32_bf16 v[230:233], v[234:237], v[80:83], 0\n"
    "s_setprio 3\n"
    "ds_read_b128 v[234:237], v206 offset:5120\n"
    "s_setprio 1\n"
    "s_waitcnt lgkmcnt(3)\n"
    "v_mfma_f32_16x16x32_bf16 v[230:233], v[238:241], v[92:95], v[230:233]\n"
    "v_mfma_f32_16x16x32_bf16 v[210:213], v[12:15], v[238:241], v[210:213]\n"
    "v_mfma_f32_16x16x32_bf16 v[222:225], v[238:241], v[84:87], v[222:225]\n"
    "v_mfma_f32_16x16x32_bf16 v[214:217], v[16:19], v[238:241], v[214:217]\n"
    "v_mfma_f32_16x16x32_bf16 v[226:229], v[238:241], v[88:91], v[226:229]\n"
    "v_mfma_f32_16x16x32_bf16 v[218:221], v[20:23], v[238:241], v[218:221]\n"
    "s_setprio 3\n"
    "ds_read_b128 v[238:241], v206 offset:6144\n"
    "s_setprio 1\n"
    "v_mfma_f32_16x16x32_bf16 v[230:233], v[242:245], v[104:107], v[230:233]\n"
    "v_mfma_f32_16x16x32_bf16 v[210:213], v[24:27], v[242:245], v[210:213]\n"
    "v_mfma_f32_16x16x32_bf16 v[222:225], v[242:245], v[96:99], v[222:225]\n"
    "v_mfma_f32_16x16x32_bf16 v[214:217], v[28:31], v[242:245], v[214:217]\n"
    "v_mfma_f32_16x16x32_bf16 v[226:229], v[242:245], v[100:103], v[226:229]\n"
    "v_mfma_f32_16x16x32_bf16 v[218:221], v[32:35], v[242:245], v[218:221]\n"
    "s_setprio 3\n"
    "ds_read_b128 v[242:245], v206 offset:7168\n"
    "s_setprio 1\n"
    "v_mfma_f32_16x16x32_bf16 v[230:233], v[246:249], v[116:119], v[230:233]\n"
    "v_mfma_f32_16x16x32_bf16 v[210:213], v[36:39], v[246:249], v[210:213]\n"
    "v_mfma_f32_16x16x32_bf16 v[222:225], v[246:249], v[108:111], v[222:225]\n"
    "v_mfma_f32_16x16x32_bf16 v[214:217], v[40:43], v[246:249], v[214:217]\n"
    "v_mfma_f32_16x16x32_bf16 v[226:229], v[246:249], v[112:115], v[226:229]\n"
    "v_mfma_f32_16x16x32_bf16 v[218:221], v[44:47], v[246:249], v[218:221]\n"
    "s_setprio 3\n"
    "ds_read_b128 v[246:249], v206 offset:8192\n"
    "s_setprio 1\n"
    "v_mfma_f32_16x16x32_bf16 v[230:233], v[250:253], v[128:131], v[230:233]\n"
    "v_mfma_f32_16x16x32_bf16 v[210:213], v[48:51], v[250:253], v[210:213]\n"
    "v_mfma_f32_16x16x32_bf16 v[222:225], v[250:253], v[120:123], v[222:225]\n"
    "v_mfma_f32_16x16x32_bf16 v[214:217], v[52:55], v[250:253], v[214:217]\n"
    "v_mfma_f32_16x16x32_bf16 v[226:229], v[250:253], v[124:127], v[226:229]\n"
    "v_mfma_f32_16x16x32_bf16 v[218:221], v[56:59], v[250:253], v[218:221]\n"
    "s_setprio 3\n"
    "ds_read_b128 v[250:253], v206 offset:9216\n"
    "s_setprio 1\n"
    "s_waitcnt lgkmcnt(4)\n"
    "v_mfma_f32_16x16x32_bf16 v[230:233], v[234:237], v[140:143], v[230:233]\n"
    "v_mfma_f32_16x16x32_bf16 v[210:213], v[60:63], v[234:237], v[210:213]\n"
    "v_mfma_f32_16x16x32_bf16 v[222:225], v[234:237], v[132:135], v[222:225]\n"
    "v_mfma_f32_16x16x32_bf16 v[214:217], v[64:67], v[234:237], v[214:217]\n"
    "v_mfma_f32_16x16x32_bf16 v[226:229], v[234:237], v[136:139], v[226:229]\n"
    "v_mfma_f32_16x16x32_bf16 v[218:221], v[68:71], v[234:237], v[218:221]\n"
    "s_setprio 3\n"
    "ds_read_b128 v[234:237], v206 offset:10240\n"
    "s_setprio 1\n"
    "v_cvt_pk_bf16_f32 v184, v230, v231\n"
    "v_cvt_pk_bf16_f32 v185, v232, v233\n"
    "s_waitcnt lgkmcnt(4)\n"
    "v_mfma_f32_16x16x32_bf16 v[230:233], v[238:241], v[80:83], 0\n"
    "v_cvt_pk_bf16_f32 v144, v210, v211\n"
    "v_cvt_pk_bf16_f32 v145, v212, v213\n"
    "v_mfma_f32_16x16x32_bf16 v[210:213], v[0:3], v[238:241], 0\n"
    "v_cvt_pk_bf16_f32 v168, v222, v223\n"
    "v_cvt_pk_bf16_f32 v169, v224, v225\n"
    "v_mfma_f32_16x16x32_bf16 v[222:225], v[238:241], v[72:75], 0\n"
    "v_cvt_pk_bf16_f32 v146, v214, v215\n"
    "v_cvt_pk_bf16_f32 v147, v216, v217\n"
    "v_mfma_f32_16x16x32_bf16 v[214:217], v[4:7], v[238:241], 0\n"
    "v_cvt_pk_bf16_f32 v176, v226, v227\n"
    "v_cvt_pk_bf16_f32 v177, v228, v229\n"
    "v_mfma_f32_16x16x32_bf16 v[226:229], v[238:241], v[76:79], 0\n"
    "v_cvt_pk_bf16_f32 v148, v218, v219\n"
    "v_cvt_pk_bf16_f32 v149, v220, v221\n"
    "v_mfma_f32_16x16x32_bf16 v[218:221], v[8:11], v[238:241], 0\n"
    "s_setprio 3\n"
    "ds_read_b128 v[238:241], v206 offset:11264\n"
    "s_setprio 1\n"
    "s_waitcnt lgkmcnt(4)\n"
    "v_mfma_f32_16x16x32_bf16 v[230:233], v[242:245], v[92:95], v[230:233]\n"
    "v_mfma_f32_16x16x32_bf16 v[210:213], v[12:15], v[242:245], v[210:213]\n"
    "v_mfma_f32_16x16x32_bf16 v[222:225], v[242:245], v[84:87], v[222:225]\n"
    "v_mfma_f32_16x16x32_bf16 v[214:217], v[16:19], v[242:245], v[214:217]\n"
    "v_mfma_f32_16x16x32_bf16 v[226:229], v[242:245], v[88:91], v[226:229]\n"
    "v_mfma_f32_16x16x32_bf16 v[218:221], v[20:23], v[242:245], v[218:221]\n"
    "s_setprio 3\n"
    "ds_read_b128 v[242:245], v206 offset:12288\n"
    "s_setprio 1\n"
    "s_waitcnt lgkmcnt(4)\n"
    "v_mfma_f32_16x16x32_bf16 v[230:233], v[246:249], v[104:107], v[230:233]\n"
    "v_mfma_f32_16x16x32_bf16 v[210:213], v[24:27], v[246:249], v[210:213]\n"
    "v_mfma_f32_16x16x32_bf16 v[222:225], v[246:249], v[96:99], v[222:225]\n"
    "v_mfma_f32_16x16x32_bf16 v[214:217], v[28:31], v[246:249], v[214:217]\n"
    "v_mfma_f32_16x16x32_bf16 v[226:229], v[246:249], v[100:103], v[226:229]\n"
    "v_mfma_f32_16x16x32_bf16 v[218:221], v[32:35], v[246:249], v[218:221]\n"
    "s_setprio 3\n"
    "ds_read_b128 v[246:249], v206 offset:13312\n"
    "s_setprio 1\n"
    "s_waitcnt lgkmcnt(4)\n"
    "v_mfma_f32_16x16x32_bf16 v[230:233], v[250:253], v[116:119], v[230:233]\n"
    "v_mfma_f32_16x16x32_bf16 v[210:213], v[36:39], v[250:253], v[210:213]\n"
    "v_mfma_f32_16x16x32_bf16 v[222:225], v[250:253], v[108:111], v[222:225]\n"
    "v_mfma_f32_16x16x32_bf16 v[214:217], v[40:43], v[250:253], v[214:217]\n"
    "v_mfma_f32_16x16x32_bf16 v[226:229], v[250:253], v[112:115], v[226:229]\n"
    "v_mfma_f32_16x16x32_bf16 v[218:221], v[44:47], v[250:253], v[218:221]\n"
    "s_setprio 3\n"
    "ds_read_b128 v[250:253], v206 offset:14336\n"
    "s_setprio 1\n"
    "s_waitcnt lgkmcnt(4)\n"
    "v_mfma_f32_16x16x32_bf16 v[230:233], v[234:237], v[128:131], v[230:233]\n"
    "v_mfma_f32_16x16x32_bf16 v[210:213], v[48:51], v[234:237], v[210:213]\n"
    "v_mfma_f32_16x16x32_bf16 v[222:225], v[234:237], v[120:123], v[222:225]\n"
    "v_mfma_f32_16x16x32_bf16 v[214:217], v[52:55], v[234:237], v[214:217]\n"
    "v_mfma_f32_16x16x32_bf16 v[226:229], v[234:237], v[124:127], v[226:229]\n"
    "v_mfma_f32_16x16x32_bf16 v[218:221], v[56:59], v[234:237], v[218:221]\n"
    "s_setprio 3\n"
    "ds_read_b128 v[234:237], v206 offset:15360\n"
    "s_setprio 1\n"
    "s_waitcnt lgkmcnt(4)\n"
    "v_mfma_f32_16x16x32_bf16 v[230:233], v[238:241], v[140:143], v[230:233]\n"
    "v_mfma_f32_16x16x32_bf16 v[210:213], v[60:63], v[238:241], v[210:213]\n"
    "v_mfma_f32_16x16x32_bf16 v[222:225], v[238:241], v[132:135], v[222:225]\n"
    "v_mfma_f32_16x16x32_bf16 v[214:217], v[64:67], v[238:241], v[214:217]\n"
    "v_mfma_f32_16x16x32_bf16 v[226:229], v[238:241], v[136:139], v[226:229]\n"
    "v_mfma_f32_16x16x32_bf16 v[218:221], v[68:71], v[238:241], v[218:221]\n"
    "s_setprio 3\n"
    "ds_read_b128 v[238:241], v206 offset:16384\n"
    "s_setprio 1\n"
    "v_cvt_pk_bf16_f32 v186, v230, v231\n"
    "v_cvt_pk_bf16_f32 v187, v232, v233\n"
    "s_waitcnt lgkmcnt(4)\n"
    "v_mfma_f32_16x16x32_bf16 v[230:233], v[242:245], v[80:83], 0\n"
    "v_cvt_pk_bf16_f32 v150, v210, v211\n"
    "v_cvt_pk_bf16_f32 v151, v212, v213\n"
    "v_mfma_f32_16x16x32_bf16 v[210:213], v[0:3], v[242:245], 0\n"
    "v_cvt_pk_bf16_f32 v170, v222, v223\n"
    "v_cvt_pk_bf16_f32 v171, v224, v225\n"
    "v_mfma_f32_16x16x32_bf16 v[222:225], v[242:245], v[72:75], 0\n"
    "v_cvt_pk_bf16_f32 v152, v214, v215\n"
    "v_cvt_pk_bf16_f32 v153, v216, v217\n"
    "v_mfma_f32_16x16x32_bf16 v[214:217], v[4:7], v[242:245], 0\n"
    "v_cvt_pk_bf16_f32 v178, v226, v227\n"
    "v_cvt_pk_bf16_f32 v179, v228, v229\n"
    "v_mfma_f32_16x16x32_bf16 v[226:229], v[242:245], v[76:79], 0\n"
    "v_cvt_pk_bf16_f32 v154, v218, v219\n"
    "v_cvt_pk_bf16_f32 v155, v220, v221\n"
    "v_mfma_f32_16x16x32_bf16 v[218:221], v[8:11], v[242:245], 0\n"
    "s_setprio 3\n"
    "ds_read_b128 v[242:245], v206 offset:17408\n"
    "s_setprio 1\n"
    "s_waitcnt lgkmcnt(4)\n"
    "v_mfma_f32_16x16x32_bf16 v[230:233], v[246:249], v[92:95], v[230:233]\n"
    "v_mfma_f32_16x16x32_bf16 v[210:213], v[12:15], v[246:249], v[210:213]\n"
    "v_mfma_f32_16x16x32_bf16 v[222:225], v[246:249], v[84:87], v[222:225]\n"
    "v_mfma_f32_16x16x32_bf16 v[214:217], v[16:19], v[246:249], v[214:217]\n"
    "v_mfma_f32_16x16x32_bf16 v[226:229], v[246:249], v[88:91], v[226:229]\n"
    "v_mfma_f32_16x16x32_bf16 v[218:221], v[20:23], v[246:249], v[218:221]\n"
    "s_waitcnt lgkmcnt(3)\n"
    "v_mfma_f32_16x16x32_bf16 v[230:233], v[250:253], v[104:107], v[230:233]\n"
    "v_mfma_f32_16x16x32_bf16 v[210:213], v[24:27], v[250:253], v[210:213]\n"
    "v_mfma_f32_16x16x32_bf16 v[222:225], v[250:253], v[96:99], v[222:225]\n"
    "v_mfma_f32_16x16x32_bf16 v[214:217], v[28:31], v[250:253], v[214:217]\n"
    "v_mfma_f32_16x16x32_bf16 v[226:229], v[250:253], v[100:103], v[226:229]\n"
    "v_mfma_f32_16x16x32_bf16 v[218:221], v[32:35], v[250:253], v[218:221]\n"
    "s_waitcnt lgkmcnt(2)\n"
    "v_mfma_f32_16x16x32_bf16 v[230:233], v[234:237], v[116:119], v[230:233]\n"
    "v_mfma_f32_16x16x32_bf16 v[210:213], v[36:39], v[234:237], v[210:213]\n"
    "v_mfma_f32_16x16x32_bf16 v[222:225], v[234:237], v[108:111], v[222:225]\n"
    "v_mfma_f32_16x16x32_bf16 v[214:217], v[40:43], v[234:237], v[214:217]\n"
    "v_mfma_f32_16x16x32_bf16 v[226:229], v[234:237], v[112:115], v[226:229]\n"
    "v_mfma_f32_16x16x32_bf16 v[218:221], v[44:47], v[234:237], v[218:221]\n"
    "s_waitcnt lgkmcnt(1)\n"
    "v_mfma_f32_16x16x32_bf16 v[230:233], v[238:241], v[128:131], v[230:233]\n"
    "v_mfma_f32_16x16x32_bf16 v[210:213], v[48:51], v[238:241], v[210:213]\n"
    "v_mfma_f32_16x16x32_bf16 v[222:225], v[238:241], v[120:123], v[222:225]\n"
    "v_mfma_f32_16x16x32_bf16 v[214:217], v[52:55], v[238:241], v[214:217]\n"
    "v_mfma_f32_16x16x32_bf16 v[226:229], v[238:241], v[124:127], v[226:229]\n"
    "v_mfma_f32_16x16x32_bf16 v[218:221], v[56:59], v[238:241], v[218:221]\n"
    "s_waitcnt lgkmcnt(0)\n"
    "v_mfma_f32_16x16x32_bf16 v[230:233], v[242:245], v[140:143], v[230:233]\n"
    "s_barrier\n"
    "s_setprio 2\n"
    "ds_read_b128 v[248:251], v207 offset:43008\n"
    "ds_read_b64 v[246:247], v208 offset:36864\n"
    "s_setprio 1\n"
    "v_mfma_f32_16x16x32_bf16 v[210:213], v[60:63], v[242:245], v[210:213]\n"
    "s_setprio 2\n"
    "ds_read_b64 v[252:253], v208 offset:43008\n"
    "s_setprio 1\n"
    "v_mfma_f32_16x16x32_bf16 v[222:225], v[242:245], v[132:135], v[222:225]\n"
    "v_cvt_pk_bf16_f32 v188, v230, v231\n"
    "v_mfma_f32_16x16x32_bf16 v[214:217], v[64:67], v[242:245], v[214:217]\n"
    "v_cvt_pk_bf16_f32 v189, v232, v233\n"
    "v_mfma_f32_16x16x32_bf16 v[226:229], v[242:245], v[136:139], v[226:229]\n"
    "v_cvt_pk_bf16_f32 v156, v210, v211\n"
    "v_cvt_pk_bf16_f32 v157, v212, v213\n"
    "v_mfma_f32_16x16x32_bf16 v[218:221], v[68:71], v[242:245], v[218:221]\n"
    "s_setprio 2\n"
    "ds_read_b128 v[242:245], v207 offset:36864\n"
    "s_setprio 1\n"
    "v_cvt_pk_bf16_f32 v172, v222, v223\n"
    "v_cvt_pk_bf16_f32 v173, v224, v225\n"
    "s_setprio 2\n"
    "s_waitcnt lgkmcnt(3)\n"
    "v_mfma_f32_16x16x32_bf16 v[238:241], v[162:165], v[248:251], v[192:195]\n"
    "s_setprio 1\n"
    "v_cvt_pk_bf16_f32 v158, v214, v215\n"
    "v_cvt_pk_bf16_f32 v159, v216, v217\n"
    "s_setprio 2\n"
    "v_mfma_f32_16x16x32_bf16 v[230:233], v[150:153], v[248:251], 0\n"
    "s_setprio 1\n"
    "v_cvt_pk_bf16_f32 v180, v226, v227\n"
    "v_cvt_pk_bf16_f32 v160, v218, v219\n"
    "s_setprio 2\n"
    "v_mfma_f32_16x16x32_bf16 v[234:237], v[156:159], v[248:251], 0\n"
    "s_setprio 1\n"
    "v_cvt_pk_bf16_f32 v161, v220, v221\n"
    "v_cvt_pk_bf16_f32 v181, v228, v229\n"
    "s_setprio 2\n"
    "s_waitcnt lgkmcnt(0)\n"
    "v_mfma_f32_16x16x32_bf16 v[210:213], v[144:147], v[242:245], 0\n"
    "v_mfma_f32_16x16x32_bf16 v[222:225], v[162:165], v[242:245], v[192:195]\n"
    "v_mfma_f32_16x16x32_bf16 v[214:217], v[150:153], v[242:245], 0\n"
    "v_mfma_f32_16x16x32_bf16 v[218:221], v[156:159], v[242:245], 0\n"
    "ds_read_b128 v[242:245], v207 offset:49152\n"
    "v_mfma_f32_16x16x32_bf16 v[226:229], v[144:147], v[248:251], 0\n"
    "ds_read_b128 v[248:251], v207 offset:55296\n"
    "v_mfma_f32_16x16x16_bf16 v[210:213], v[148:149], v[246:247], v[210:213]\n"
    "v_mfma_f32_16x16x16_bf16 v[222:225], v[166:167], v[246:247], v[222:225]\n"
    "v_mfma_f32_16x16x16_bf16 v[214:217], v[154:155], v[246:247], v[214:217]\n"
    "v_mfma_f32_16x16x16_bf16 v[218:221], v[160:161], v[246:247], v[218:221]\n"
    "ds_read_b64 v[246:247], v208 offset:49152\n"
    "v_mfma_f32_16x16x16_bf16 v[226:229], v[148:149], v[252:253], v[226:229]\n"
    "v_mfma_f32_16x16x16_bf16 v[238:241], v[166:167], v[252:253], v[238:241]\n"
    "v_mfma_f32_16x16x16_bf16 v[230:233], v[154:155], v[252:253], v[230:233]\n"
    "s_nop 1\n"
    "v_max3_f32 v223, v222, v210, v211\n"
    "v_mfma_f32_16x16x16_bf16 v[234:237], v[160:161], v[252:253], v[234:237]\n"
    "ds_read_b64 v[252:253], v208 offset:55296\n"
    "s_setprio 0\n"
    "v_max3_f32 v224, v212, v213, v214\n"
    "v_max3_f32 v223, v223, v215, v216\n"
    "v_max3_f32 v224, v224, v217, v218\n"
    "v_max3_f32 v223, v223, v219, v220\n"
    "v_max3_f32 v223, v223, v224, v221\n"
    "v_mov_b32 v224, v223\n"
    "v_max3_f32 v239, v238, v226, v227\n"
    "v_max3_f32 v240, v228, v229, v230\n"
    "v_max3_f32 v239, v239, v231, v232\n"
    "v_permlane32_swap_b32 v223, v224\n"
    "v_max3_f32 v240, v240, v233, v234\n"
    "v_max3_f32 v239, v239, v235, v236\n"
    "v_max3_f32 v239, v239, v240, v237\n"
    "v_max_f32 v223, v223, v224\n"
    "v_mov_b32 v240, v239\n"
    "v_mov_b32 v224, v223\n"
    "s_nop 0\n"
    "v_permlane32_swap_b32 v239, v240\n"
    "v_permlane16_swap_b32 v223, v224\n"
    "v_max_f32 v239, v239, v240\n"
    "v_max_f32 v223, v223, v224\n"
    "v_mov_b32 v240, v239\n"
    "v_mul_f32 v223, 0xbe553b95, v223\n"
    "v_fmamk_f32 v210, v210, 0x3e553b95, v223\n"
    "v_fmamk_f32 v211, v211, 0x3e553b95, v223\n"
    "v_fmamk_f32 v212, v212, 0x3e553b95, v223\n"
    "v_fmamk_f32 v213, v213, 0x3e553b95, v223\n"
    "v_fmamk_f32 v214, v214, 0x3e553b95, v223\n"
    "v_fmamk_f32 v215, v215, 0x3e553b95, v223\n"
    "v_fmamk_f32 v216, v216, 0x3e553b95, v223\n"
    "v_fmamk_f32 v217, v217, 0x3e553b95, v223\n"
    "v_permlane16_swap_b32 v239, v240\n"
    "v_exp_f32 v210, v210\n"
    "v_exp_f32 v211, v211\n"
    "v_exp_f32 v212, v212\n"
    "v_exp_f32 v213, v213\n"
    "v_exp_f32 v214, v214\n"
    "v_exp_f32 v215, v215\n"
    "v_exp_f32 v216, v216\n"
    "v_exp_f32 v217, v217\n"
    "v_fmamk_f32 v218, v218, 0x3e553b95, v223\n"
    "v_fmamk_f32 v219, v219, 0x3e553b95, v223\n"
    "v_fmamk_f32 v220, v220, 0x3e553b95, v223\n"
    "v_fmamk_f32 v221, v221, 0x3e553b95, v223\n"
    "v_fmamk_f32 v222, v222, 0x3e553b95, v223\n"
    "v_exp_f32 v218, v218\n"
    "v_exp_f32 v219, v219\n"
    "v_exp_f32 v220, v220\n"
    "v_exp_f32 v221, v221\n"
    "v_exp_f32 v222, v222\n"
    "v_max_f32 v239, v239, v240\n"
    "v_cvt_pk_bf16_f32 v198, v210, v211\n"
    "v_cvt_pk_bf16_f32 v199, v212, v213\n"
    "v_cvt_pk_bf16_f32 v200, v214, v215\n"
    "v_cvt_pk_bf16_f32 v201, v216, v217\n"
    "v_mul_f32 v239, 0xbe553b95, v239\n"
    "v_fmamk_f32 v226, v226, 0x3e553b95, v239\n"
    "s_setprio 2\n"
    "v_mfma_f32_16x16x32_bf16 v[210:213], v[168:171], v[198:201], 0\n"
    "v_fmamk_f32 v227, v227, 0x3e553b95, v239\n"
    "v_fmamk_f32 v228, v228, 0x3e553b95, v239\n"
    "v_mfma_f32_16x16x32_bf16 v[214:217], v[176:179], v[198:201], 0\n"
    "s_setprio 0\n"
    "v_fmamk_f32 v229, v229, 0x3e553b95, v239\n"
    "v_fmamk_f32 v230, v230, 0x3e553b95, v239\n"
    "v_fmamk_f32 v231, v231, 0x3e553b95, v239\n"
    "v_fmamk_f32 v232, v232, 0x3e553b95, v239\n"
    "v_fmamk_f32 v233, v233, 0x3e553b95, v239\n"
    "v_cvt_pk_bf16_f32 v202, v218, v219\n"
    "v_cvt_pk_bf16_f32 v203, v220, v221\n"
    "s_setprio 2\n"
    "v_mfma_f32_16x16x32_bf16 v[218:221], v[184:187], v[198:201], 0\n"
    "v_cvt_pk_bf16_f32 v204, v222, 0\n"
    "v_exp_f32 v226, v226\n"
    "v_mfma_f32_16x16x32_bf16 v[222:225], v[194:197], v[198:201], 0\n"
    "v_exp_f32 v227, v227\n"
    "v_mfma_f32_16x16x32_bf16 v[210:213], v[172:175], v[202:205], v[210:213]\n"
    "v_exp_f32 v228, v228\n"
    "v_mfma_f32_16x16x32_bf16 v[214:217], v[180:183], v[202:205], v[214:217]\n"
    "v_exp_f32 v229, v229\n"
    "v_mfma_f32_16x16x32_bf16 v[218:221], v[188:191], v[202:205], v[218:221]\n"
    "v_exp_f32 v230, v230\n"
    "v_mfma_f32_16x16x32_bf16 v[222:225], v[194:197], v[202:205], v[222:225]\n"
    "s_setprio 0\n"
    "v_exp_f32 v231, v231\n"
    "v_exp_f32 v232, v232\n"
    "v_exp_f32 v233, v233\n"
    "v_fmamk_f32 v238, v238, 0x3e553b95, v239\n"
    "v_exp_f32 v238, v238\n"
    "v_fmamk_f32 v234, v234, 0x3e553b95, v239\n"
    "v_fmamk_f32 v235, v235, 0x3e553b95, v239\n"
    "v_fmamk_f32 v236, v236, 0x3e553b95, v239\n"
    "v_fmamk_f32 v237, v237, 0x3e553b95, v239\n"
    "v_cvt_pk_bf16_f32 v198, v226, v227\n"
    "v_cvt_pk_bf16_f32 v199, v228, v229\n"
    "v_cvt_pk_bf16_f32 v200, v230, v231\n"
    "v_cvt_pk_bf16_f32 v201, v232, v233\n"
    "v_exp_f32 v234, v234\n"
    "v_exp_f32 v235, v235\n"
    "s_setprio 2\n"
    "v_mfma_f32_16x16x32_bf16 v[226:229], v[168:171], v[198:201], 0\n"
    "v_exp_f32 v236, v236\n"
    "v_mfma_f32_16x16x32_bf16 v[230:233], v[176:179], v[198:201], 0\n"
    "v_exp_f32 v237, v237\n"
    "v_cvt_pk_bf16_f32 v204, v238, 0\n"
    "v_mfma_f32_16x16x32_bf16 v[238:241], v[194:197], v[198:201], 0\n"
    "v_cvt_pk_bf16_f32 v202, v234, v235\n"
    "v_cvt_pk_bf16_f32 v203, v236, v237\n"
    "v_mfma_f32_16x16x32_bf16 v[234:237], v[184:187], v[198:201], 0\n"
    "v_rcp_f32 v223, v222\n"
    "v_mfma_f32_16x16x32_bf16 v[238:241], v[194:197], v[202:205], v[238:241]\n"
    "v_mul_f32 v210, v210, v223\n"
    "v_mul_f32 v211, v211, v223\n"
    "v_mfma_f32_16x16x32_bf16 v[226:229], v[172:175], v[202:205], v[226:229]\n"
    "v_mul_f32 v212, v212, v223\n"
    "v_mul_f32 v213, v213, v223\n"
    "v_mfma_f32_16x16x32_bf16 v[230:233], v[180:183], v[202:205], v[230:233]\n"
    "v_mul_f32 v214, v214, v223\n"
    "v_mul_f32 v215, v215, v223\n"
    "v_mfma_f32_16x16x32_bf16 v[234:237], v[188:191], v[202:205], v[234:237]\n"
    "s_setprio 0\n"
    "v_mul_f32 v216, v216, v223\n"
    "v_mul_f32 v217, v217, v223\n"
    "v_cvt_pk_bf16_f32 v210, v210, v211\n"
    "v_cvt_pk_bf16_f32 v211, v212, v213\n"
    "v_cvt_pk_bf16_f32 v212, v214, v215\n"
    "v_cvt_pk_bf16_f32 v213, v216, v217\n"
    "v_rcp_f32 v239, v238\n"
    "v_mul_f32 v218, v218, v223\n"
    "v_mul_f32 v219, v219, v223\n"
    "v_mul_f32 v220, v220, v223\n"
    "v_mul_f32 v221, v221, v223\n"
    "s_setprio 2\n"
    "s_waitcnt lgkmcnt(3)\n"
    "v_mfma_f32_16x16x32_bf16 v[222:225], v[162:165], v[242:245], v[192:195]\n"
    "ds_write_b128 v207, v[210:213] offset:36864\n"
    "v_mfma_f32_16x16x32_bf16 v[210:213], v[144:147], v[242:245], 0\n"
    "v_cvt_pk_bf16_f32 v214, v218, v219\n"
    "v_cvt_pk_bf16_f32 v215, v220, v221\n"
    "v_mfma_f32_16x16x32_bf16 v[218:221], v[156:159], v[242:245], 0\n"
    "ds_write_b64 v208, v[214:215] offset:36864\n"
    "v_mfma_f32_16x16x32_bf16 v[214:217], v[150:153], v[242:245], 0\n"
    "v_mul_f32 v226, v226, v239\n"
    "v_mul_f32 v227, v227, v239\n"
    "s_waitcnt lgkmcnt(3)\n"
    "v_mfma_f32_16x16x16_bf16 v[210:213], v[148:149], v[246:247], v[210:213]\n"
    "v_mul_f32 v228, v228, v239\n"
    "v_mul_f32 v229, v229, v239\n"
    "v_mfma_f32_16x16x16_bf16 v[222:225], v[166:167], v[246:247], v[222:225]\n"
    "v_mul_f32 v230, v230, v239\n"
    "v_mul_f32 v231, v231, v239\n"
    "v_mfma_f32_16x16x16_bf16 v[214:217], v[154:155], v[246:247], v[214:217]\n"
    "v_mul_f32 v232, v232, v239\n"
    "v_mul_f32 v233, v233, v239\n"
    "v_mfma_f32_16x16x16_bf16 v[218:221], v[160:161], v[246:247], v[218:221]\n"
    "s_setprio 0\n"
    "v_cvt_pk_bf16_f32 v226, v226, v227\n"
    "v_cvt_pk_bf16_f32 v227, v228, v229\n"
    "v_cvt_pk_bf16_f32 v228, v230, v231\n"
    "v_cvt_pk_bf16_f32 v229, v232, v233\n"
    "v_mul_f32 v234, v234, v239\n"
    "v_mul_f32 v235, v235, v239\n"
    "v_mul_f32 v236, v236, v239\n"
    "v_mul_f32 v237, v237, v239\n"
    "s_setprio 2\n"
    "v_mfma_f32_16x16x32_bf16 v[238:241], v[162:165], v[248:251], v[192:195]\n"
    "ds_write_b128 v207, v[226:229] offset:43008\n"
    "v_mfma_f32_16x16x32_bf16 v[226:229], v[144:147], v[248:251], 0\n"
    "v_cvt_pk_bf16_f32 v230, v234, v235\n"
    "v_cvt_pk_bf16_f32 v231, v236, v237\n"
    "v_mfma_f32_16x16x32_bf16 v[234:237], v[156:159], v[248:251], 0\n"
    "ds_write_b64 v208, v[230:231] offset:43008\n"
    "v_mfma_f32_16x16x32_bf16 v[230:233], v[150:153], v[248:251], 0\n"
    "v_max3_f32 v223, v222, v210, v211\n"
    "v_max3_f32 v224, v212, v213, v214\n"
    "s_waitcnt lgkmcnt(4)\n"
    "v_mfma_f32_16x16x16_bf16 v[226:229], v[148:149], v[252:253], v[226:229]\n"
    "v_max3_f32 v223, v223, v215, v216\n"
    "v_max3_f32 v224, v224, v217, v218\n"
    "v_mfma_f32_16x16x16_bf16 v[238:241], v[166:167], v[252:253], v[238:241]\n"
    "v_max3_f32 v223, v223, v219, v220\n"
    "v_max3_f32 v223, v223, v224, v221\n"
    "s_waitcnt lgkmcnt(0)\n"
    "s_barrier\n"
    "v_mfma_f32_16x16x16_bf16 v[230:233], v[154:155], v[252:253], v[230:233]\n"
    "v_mov_b32 v224, v223\n"
    "v_mfma_f32_16x16x16_bf16 v[234:237], v[160:161], v[252:253], v[234:237]\n"
    "s_setprio 0\n"
    "v_permlane32_swap_b32 v223, v224\n"
    "v_max3_f32 v239, v238, v226, v227\n"
    "v_max_f32 v223, v223, v224\n"
    "v_mov_b32 v224, v223\n"
    "s_nop 0\n"
    "v_max3_f32 v240, v228, v229, v230\n"
    "v_max3_f32 v239, v239, v231, v232\n"
    "v_max3_f32 v240, v240, v233, v234\n"
    "v_max3_f32 v239, v239, v235, v236\n"
    "v_max3_f32 v239, v239, v240, v237\n"
    "v_mov_b32 v240, v239\n"
    "v_permlane16_swap_b32 v223, v224\n"
    "s_nop 0\n"
    "v_permlane32_swap_b32 v239, v240\n"
    "v_max_f32 v223, v223, v224\n"
    "v_max_f32 v239, v239, v240\n"
    "v_mov_b32 v240, v239\n"
    "v_mul_f32 v223, 0xbe553b95, v223\n"
    "v_fmamk_f32 v210, v210, 0x3e553b95, v223\n"
    "v_fmamk_f32 v211, v211, 0x3e553b95, v223\n"
    "v_fmamk_f32 v212, v212, 0x3e553b95, v223\n"
    "v_fmamk_f32 v213, v213, 0x3e553b95, v223\n"
    "v_fmamk_f32 v214, v214, 0x3e553b95, v223\n"
    "v_fmamk_f32 v215, v215, 0x3e553b95, v223\n"
    "v_fmamk_f32 v216, v216, 0x3e553b95, v223\n"
    "v_fmamk_f32 v217, v217, 0x3e553b95, v223\n"
    "v_permlane16_swap_b32 v239, v240\n"
    "v_exp_f32 v210, v210\n"
    "v_exp_f32 v211, v211\n"
    "v_exp_f32 v212, v212\n"
    "v_exp_f32 v213, v213\n"
    "v_exp_f32 v214, v214\n"
    "v_exp_f32 v215, v215\n"
    "v_exp_f32 v216, v216\n"
    "v_exp_f32 v217, v217\n"
    "v_fmamk_f32 v218, v218, 0x3e553b95, v223\n"
    "v_fmamk_f32 v219, v219, 0x3e553b95, v223\n"
    "v_fmamk_f32 v220, v220, 0x3e553b95, v223\n"
    "v_fmamk_f32 v221, v221, 0x3e553b95, v223\n"
    "v_fmamk_f32 v222, v222, 0x3e553b95, v223\n"
    "v_exp_f32 v218, v218\n"
    "v_exp_f32 v219, v219\n"
    "v_exp_f32 v220, v220\n"
    "v_exp_f32 v221, v221\n"
    "v_exp_f32 v222, v222\n"
    "v_max_f32 v239, v239, v240\n"
    "v_cvt_pk_bf16_f32 v198, v210, v211\n"
    "v_cvt_pk_bf16_f32 v199, v212, v213\n"
    "v_cvt_pk_bf16_f32 v200, v214, v215\n"
    "v_cvt_pk_bf16_f32 v201, v216, v217\n"
    "v_mul_f32 v239, 0xbe553b95, v239\n"
    "v_fmamk_f32 v226, v226, 0x3e553b95, v239\n"
    "s_setprio 2\n"
    "v_mfma_f32_16x16x32_bf16 v[210:213], v[168:171], v[198:201], 0\n"
    "v_fmamk_f32 v227, v227, 0x3e553b95, v239\n"
    "v_fmamk_f32 v228, v228, 0x3e553b95, v239\n"
    "v_mfma_f32_16x16x32_bf16 v[214:217], v[176:179], v[198:201], 0\n"
    "s_setprio 0\n"
    "v_fmamk_f32 v229, v229, 0x3e553b95, v239\n"
    "v_fmamk_f32 v230, v230, 0x3e553b95, v239\n"
    "v_fmamk_f32 v231, v231, 0x3e553b95, v239\n"
    "v_fmamk_f32 v232, v232, 0x3e553b95, v239\n"
    "v_fmamk_f32 v233, v233, 0x3e553b95, v239\n"
    "v_cvt_pk_bf16_f32 v202, v218, v219\n"
    "v_cvt_pk_bf16_f32 v203, v220, v221\n"
    "s_setprio 2\n"
    "v_mfma_f32_16x16x32_bf16 v[218:221], v[184:187], v[198:201], 0\n"
    "v_cvt_pk_bf16_f32 v204, v222, 0\n"
    "v_exp_f32 v226, v226\n"
    "v_mfma_f32_16x16x32_bf16 v[222:225], v[194:197], v[198:201], 0\n"
    "v_exp_f32 v227, v227\n"
    "v_mfma_f32_16x16x32_bf16 v[210:213], v[172:175], v[202:205], v[210:213]\n"
    "v_exp_f32 v228, v228\n"
    "v_mfma_f32_16x16x32_bf16 v[214:217], v[180:183], v[202:205], v[214:217]\n"
    "v_exp_f32 v229, v229\n"
    "v_mfma_f32_16x16x32_bf16 v[218:221], v[188:191], v[202:205], v[218:221]\n"
    "v_exp_f32 v230, v230\n"
    "v_mfma_f32_16x16x32_bf16 v[222:225], v[194:197], v[202:205], v[222:225]\n"
    "s_setprio 0\n"
    "v_exp_f32 v231, v231\n"
    "v_exp_f32 v232, v232\n"
    "v_exp_f32 v233, v233\n"
    "v_fmamk_f32 v238, v238, 0x3e553b95, v239\n"
    "v_exp_f32 v238, v238\n"
    "v_fmamk_f32 v234, v234, 0x3e553b95, v239\n"
    "v_fmamk_f32 v235, v235, 0x3e553b95, v239\n"
    "v_fmamk_f32 v236, v236, 0x3e553b95, v239\n"
    "v_fmamk_f32 v237, v237, 0x3e553b95, v239\n"
    "v_cvt_pk_bf16_f32 v198, v226, v227\n"
    "v_cvt_pk_bf16_f32 v199, v228, v229\n"
    "v_cvt_pk_bf16_f32 v200, v230, v231\n"
    "v_cvt_pk_bf16_f32 v201, v232, v233\n"
    "v_exp_f32 v234, v234\n"
    "v_exp_f32 v235, v235\n"
    "s_setprio 2\n"
    "v_mfma_f32_16x16x32_bf16 v[226:229], v[168:171], v[198:201], 0\n"
    "v_exp_f32 v236, v236\n"
    "v_mfma_f32_16x16x32_bf16 v[230:233], v[176:179], v[198:201], 0\n"
    "v_exp_f32 v237, v237\n"
    "v_cvt_pk_bf16_f32 v204, v238, 0\n"
    "v_mfma_f32_16x16x32_bf16 v[238:241], v[194:197], v[198:201], 0\n"
    "v_cvt_pk_bf16_f32 v202, v234, v235\n"
    "v_cvt_pk_bf16_f32 v203, v236, v237\n"
    "v_mfma_f32_16x16x32_bf16 v[234:237], v[184:187], v[198:201], 0\n"
    "v_rcp_f32 v223, v222\n"
    "v_mfma_f32_16x16x32_bf16 v[238:241], v[194:197], v[202:205], v[238:241]\n"
    "v_mul_f32 v210, v210, v223\n"
    "v_mul_f32 v211, v211, v223\n"
    "v_mfma_f32_16x16x32_bf16 v[226:229], v[172:175], v[202:205], v[226:229]\n"
    "v_mul_f32 v212, v212, v223\n"
    "v_mul_f32 v213, v213, v223\n"
    "v_mfma_f32_16x16x32_bf16 v[230:233], v[180:183], v[202:205], v[230:233]\n"
    "v_mul_f32 v214, v214, v223\n"
    "v_mul_f32 v215, v215, v223\n"
    "v_mfma_f32_16x16x32_bf16 v[234:237], v[188:191], v[202:205], v[234:237]\n"
    "s_setprio 0\n"
    "v_rcp_f32 v239, v238\n"
    "v_mul_f32 v216, v216, v223\n"
    "v_mul_f32 v217, v217, v223\n"
    "v_cvt_pk_bf16_f32 v210, v210, v211\n"
    "v_cvt_pk_bf16_f32 v211, v212, v213\n"
    "v_cvt_pk_bf16_f32 v212, v214, v215\n"
    "v_cvt_pk_bf16_f32 v213, v216, v217\n"
    "v_mul_f32 v218, v218, v223\n"
    "v_mul_f32 v219, v219, v223\n"
    "v_mul_f32 v220, v220, v223\n"
    "v_mul_f32 v221, v221, v223\n"
    "s_setprio 2\n"
    "ds_write_b128 v207, v[210:213] offset:49152\n"
    "s_setprio 0\n"
    "v_mul_f32 v226, v226, v239\n"
    "v_mul_f32 v227, v227, v239\n"
    "v_cvt_pk_bf16_f32 v214, v218, v219\n"
    "v_cvt_pk_bf16_f32 v215, v220, v221\n"
    "v_mul_f32 v228, v228, v239\n"
    "v_mul_f32 v229, v229, v239\n"
    "v_mul_f32 v230, v230, v239\n"
    "v_mul_f32 v231, v231, v239\n"
    "v_mul_f32 v232, v232, v239\n"
    "v_mul_f32 v233, v233, v239\n"
    "v_cvt_pk_bf16_f32 v226, v226, v227\n"
    "s_setprio 2\n"
    "ds_write_b64 v208, v[214:215] offset:49152\n"
    "s_setprio 0\n"
    "v_cvt_pk_bf16_f32 v227, v228, v229\n"
    "v_cvt_pk_bf16_f32 v228, v230, v231\n"
    "v_cvt_pk_bf16_f32 v229, v232, v233\n"
    "v_mul_f32 v234, v234, v239\n"
    "v_mul_f32 v235, v235, v239\n"
    "v_mul_f32 v236, v236, v239\n"
    "v_mul_f32 v237, v237, v239\n"
    "s_setprio 2\n"
    "s_mov_b32 exec_lo, 0x10001000\n"
    "s_mov_b32 exec_hi, 0x10001000\n"
    "ds_write_b128 v207, v[226:229] offset:55296\n"
    "s_mov_b64 exec, -1\n"
    "s_setprio 0\n"
    "v_cvt_pk_bf16_f32 v230, v234, v235\n"
    "v_cvt_pk_bf16_f32 v231, v236, v237\n"
    "s_setprio 2\n"
    "s_mov_b32 exec_lo, 0x10001000\n"
    "s_mov_b32 exec_hi, 0x10001000\n"
    "ds_write_b64 v208, v[230:231] offset:55296\n"
    "s_mov_b64 exec, -1\n"
    "s_waitcnt lgkmcnt(0)\n"
    "s_barrier\n"
    "s_nop 0\n"
    "LEND%=:\n"
    :
    : "s"((unsigned)(uintptr_t)x), "s"((unsigned)((uintptr_t)x >> 32)),
      "s"((unsigned)(uintptr_t)dw), "s"((unsigned)((uintptr_t)dw >> 32)),
      "s"((unsigned)(uintptr_t)wqkv), "s"((unsigned)((uintptr_t)wqkv >> 32)),
      "s"((unsigned)(uintptr_t)wo), "s"((unsigned)((uintptr_t)wo >> 32)),
      "s"((unsigned)(uintptr_t)out), "s"((unsigned)((uintptr_t)out >> 32)),
      "s"(blockIdx.x), "s"(wave),

      "s"(ldsb)
    : "v0", "v1", "v2", "v3", "v4", "v5", "v6", "v7", "v8", "v9", "v10", "v11", "v12", "v13", "v14", "v15", "v16", "v17", "v18", "v19", "v20", "v21", "v22", "v23", "v24", "v25", "v26", "v27", "v28", "v29", "v30", "v31", "v32", "v33", "v34", "v35", "v36", "v37", "v38", "v39", "v40", "v41", "v42", "v43", "v44", "v45", "v46", "v47", "v48", "v49", "v50", "v51", "v52", "v53", "v54", "v55", "v56", "v57", "v58", "v59", "v60", "v61", "v62", "v63", "v64", "v65", "v66", "v67", "v68", "v69", "v70", "v71", "v72", "v73", "v74", "v75", "v76", "v77", "v78", "v79", "v80", "v81", "v82", "v83", "v84", "v85", "v86", "v87", "v88", "v89", "v90", "v91", "v92", "v93", "v94", "v95", "v96", "v97", "v98", "v99", "v100", "v101", "v102", "v103", "v104", "v105", "v106", "v107", "v108", "v109", "v110", "v111", "v112", "v113", "v114", "v115", "v116", "v117", "v118", "v119", "v120", "v121", "v122", "v123", "v124", "v125", "v126", "v127", "v128", "v129", "v130", "v131", "v132", "v133", "v134", "v135", "v136", "v137", "v138", "v139", "v140", "v141", "v142", "v143", "v144", "v145", "v146", "v147", "v148", "v149", "v150", "v151", "v152", "v153", "v154", "v155", "v156", "v157", "v158", "v159", "v160", "v161", "v162", "v163", "v164", "v165", "v166", "v167", "v168", "v169", "v170", "v171", "v172", "v173", "v174", "v175", "v176", "v177", "v178", "v179", "v180", "v181", "v182", "v183", "v184", "v185", "v186", "v187", "v188", "v189", "v190", "v191", "v192", "v193", "v194", "v195", "v196", "v197", "v198", "v199", "v200", "v201", "v202", "v203", "v204", "v205", "v206", "v207", "v208", "v209", "v210", "v211", "v212", "v213", "v214", "v215", "v216", "v217", "v218", "v219", "v220", "v221", "v222", "v223", "v224", "v225", "v226", "v227", "v228", "v229", "v230", "v231", "v232", "v233", "v234", "v235", "v236", "v237", "v238", "v239", "v240", "v241", "v242", "v243", "v244", "v245", "v246", "v247", "v248", "v249", "v250", "v251", "v252", "v253", "v254", "v255", "s20", "s21", "s22", "s23", "s24", "s25", "s26", "s27", "s28", "s29", "s30", "s31", "s36", "s37", "s38", "s39", "s40", "s41", "s42", "s43", "s44", "s45", "s46", "s47", "s48", "s49", "s50", "s51", "s52", "s53", "s54", "s55", "s56", "s57", "s58", "s59", "s60", "s61", "s62", "s63", "s64", "s65", "s66", "s67", "s68", "s69", "s70", "s71", "s72", "s73", "s74", "s75", "s76", "s77", "s78", "s79", "s80", "s81", "s82", "s83", "s84", "s85", "s86", "s87", "s88", "s89", "s90", "s91", "s92", "s93", "s94", "s95", "s96", "s97", "s98", "s99", "vcc", "scc", "memory");
}

extern "C" void kernel_launch(void* const* d_in, const int* in_sizes, int n_in,
                              void* d_out, int out_size, void* d_ws, size_t ws_size,
                              hipStream_t stream) {
  davit_window_attn_kernel<<<256, 512, 0, stream>>>(d_in[0], d_in[1], d_in[2], d_in[3], d_out);
}
```
